```python
import torch

def _softmax(x, axis=-1):
    return torch.softmax(x, dim=axis)
def _gelu(x):
    return torch.nn.functional.gelu(x, approximate="tanh")
def _silu(x):
    return torch.nn.functional.silu(x)
def _sigmoid(x):
    return torch.sigmoid(x)
def _layernorm(x, axis=-1, eps=1e-5):
    m = x.mean(dim=axis, keepdim=True)
    v = x.var(dim=axis, keepdim=True, unbiased=False)
    return (x - m) / torch.sqrt(v + eps)
def _scatter_add(tgt, idx, src, axis=0):
    return tgt.clone().index_add_(axis, idx, src)
def _segment_sum(src, seg, n):
    out = torch.zeros((n,) + src.shape[1:], dtype=src.dtype)
    return out.index_add_(0, seg, src)
def _pad(x, pw):
    flat = tuple(v for pair in reversed(pw) for v in pair)
    return torch.nn.functional.pad(x, flat)

kernel_name = 'interleave_layers__spectral_gfnet__mixer_token_mlp__prompt'

def setup_inputs():

    _w = (lambda a: (lambda t: t.to(torch.bfloat16) if t.is_floating_point() else t)(torch.as_tensor(a)))
    def _A_setup():
        B, S, D = 1, 2048, 448
        Df = D // 2 + 1
        return {
            "x": _w((lambda *s: torch.randn(*s, dtype=torch.bfloat16) * 0.05)(B, S, D)),
            "w_r": _w((lambda *s: torch.randn(*s, dtype=torch.bfloat16) * 1.6)(S, Df) * 0.02),
            "w_i": _w((lambda *s: torch.randn(*s, dtype=torch.bfloat16) * 1.6)(S, Df) * 0.02),
        }
    _A_in = _A_setup()

    def _B_setup():
        B, S, D, F = 1, 2048, 448, 1792
        return {
            "x": _w((lambda *s: torch.randn(*s, dtype=torch.bfloat16) * 0.05)(B, S, D)),
            "w1": _w((lambda *s: torch.randn(*s, dtype=torch.bfloat16) * 0.4)(S, F) * (S ** -0.5)),
            "b1": _w((lambda *s: torch.randn(*s, dtype=torch.bfloat16) * 3.2)(F) * 0.02),
            "w2": _w((lambda *s: torch.randn(*s, dtype=torch.bfloat16) * 0.8)(F, S) * (F ** -0.5)),
            "b2": _w((lambda *s: torch.randn(*s, dtype=torch.bfloat16) * 3.2)(S) * 0.02),
        }
    _B_in = _B_setup()

    return {
        "A_x": _A_in["x"],
        "A_w_r": _A_in["w_r"],
        "A_w_i": _A_in["w_i"],
        "B_x": _B_in["x"],
        "B_w1": _B_in["w1"],
        "B_b1": _B_in["b1"],
        "B_w2": _B_in["w2"],
        "B_b2": _B_in["b2"],
    }

def reference(A_x, A_w_r, A_w_i, B_x, B_w1, B_b1, B_w2, B_b2):
    def _A_ref(x, w_r, w_i):
        B, S, D = x.shape
        X = (lambda x, axes=(-2,-1): torch.fft.rfft2(x.float(), dim=axes))(x, axes=(-2, -1))
        W = w_r + 1j * w_i
        X = X * W
        y = (lambda x, s, axes=(-2,-1): torch.fft.irfft2(x, s=s, dim=axes).to(torch.bfloat16))(X, (S, D), axes=(-2, -1))
        return y

    def _B_ref(x, w1, b1, w2, b2):
        B, S, D = x.shape
        h = _layernorm(x, axis=-1)
        u = torch.einsum("bsd,sf->bdf", h, w1) + b1
        u = _gelu(u)
        y = torch.einsum("bdf,fs->bds", u, w2) + b2
        return (lambda x, *d: x.permute(*d))(y, 0, 2, 1)

    x = A_x
    x = x + _A_ref(_layernorm(x, axis=-1), A_w_r, A_w_i)
    x = x + _B_ref(_layernorm(x, axis=-1), B_w1, B_b1, B_w2, B_b2)
    x = x + _A_ref(_layernorm(x, axis=-1), A_w_r, A_w_i)
    x = x + _B_ref(_layernorm(x, axis=-1), B_w1, B_b1, B_w2, B_b2)
    return x
```

```cpp
#include <hip/hip_runtime.h>
#include <hip/hip_bf16.h>
#include <stdint.h>

#define DI __device__ __forceinline__
#define GAS __attribute__((address_space(1)))
#define LAS __attribute__((address_space(3)))
typedef unsigned v4u __attribute__((ext_vector_type(4)));
typedef unsigned v2u __attribute__((ext_vector_type(2)));
typedef float f4 __attribute__((ext_vector_type(4)));
typedef float f2 __attribute__((ext_vector_type(2)));
typedef _Float16 h8 __attribute__((ext_vector_type(8)));
typedef _Float16 h2 __attribute__((ext_vector_type(2)));
typedef __bf16 b8 __attribute__((ext_vector_type(8)));
typedef __bf16 b2 __attribute__((ext_vector_type(2)));
typedef short s4 __attribute__((ext_vector_type(4)));
typedef __amdgpu_buffer_rsrc_t rsrc_t;
typedef LAS unsigned char* lds_t;

constexpr int S = 2048, D = 448, F = 1792, NKF = 225;
constexpr float EPS = 1e-5f;
constexpr int XB = S * D * 2;
constexpr int GB = D * F * 2;
constexpr int CTRL_OFF = 0, CTRL_BYTES = 65536;
constexpr int ST_OFF = CTRL_BYTES;
constexpr int WT_OFF = ST_OFF + 4 * S * 4;
constexpr int XH0_OFF = WT_OFF + XB;
constexpr int XH1_OFF = XH0_OFF + 2 * XB;
constexpr int HF_OFF = XH1_OFF + 2 * XB;
constexpr int GF_OFF = HF_OFF + 2 * XB;
constexpr int X2_OFF = GF_OFF + 2 * GB;
constexpr int X1_OFF = X2_OFF + XB;
constexpr int WS_BYTES = X1_OFF + 256 * 16 * 256 * 4;
constexpr int L_A = 0, L_E = 57344, L_R = L_E + 512, L_W = 65536, LDS_BYTES = 131072;

struct Params {
  const unsigned short* x; const unsigned short* wr; const unsigned short* wi;
  const unsigned short* w1; const unsigned short* b1; const unsigned short* w2; const unsigned short* b2;
  unsigned short* out; unsigned char* ws;
};

DI rsrc_t mkrs(const void* p, int bytes) { return __builtin_amdgcn_make_buffer_rsrc((void*)p, 0, bytes, 0x00020000); }
DI f4 mfma_bf(v4u a, v4u b, f4 c) { return __builtin_amdgcn_mfma_f32_16x16x32_bf16(__builtin_bit_cast(b8, a), __builtin_bit_cast(b8, b), c, 0, 0, 0); }
DI f4 mfma_h(v4u a, v4u b, f4 c) { return __builtin_amdgcn_mfma_f32_16x16x32_f16(__builtin_bit_cast(h8, a), __builtin_bit_cast(h8, b), c, 0, 0, 0); }
DI unsigned pk_bf(float a, float b) { f2 v = {a, b}; return __builtin_bit_cast(unsigned, __builtin_convertvector(v, b2)); }
DI unsigned pk_h(float a, float b) { f2 v = {a, b}; return __builtin_bit_cast(unsigned, __builtin_convertvector(v, h2)); }
DI float h_lo(unsigned u) { return (float)__builtin_bit_cast(h2, u)[0]; }
DI float h_hi(unsigned u) { return (float)__builtin_bit_cast(h2, u)[1]; }
DI float bf_lo(unsigned u) { return __builtin_bit_cast(float, u << 16); }
DI float bf_hi(unsigned u) { return __builtin_bit_cast(float, u & 0xffff0000u); }
DI float vcos(float x) { return __builtin_amdgcn_cosf(x); }
DI float vsin(float x) { return __builtin_amdgcn_sinf(x); }
DI v2u trread(lds_t p) { return __builtin_bit_cast(v2u, __builtin_amdgcn_ds_read_tr16_b64_v4i16((LAS s4*)p)); }
DI v4u cat(v2u a, v2u b) { return v4u{a.x, a.y, b.x, b.y}; }
DI v4u ldsr128(lds_t p) { return *(LAS v4u*)p; }
DI void ldsw128(lds_t p, v4u v) { *(LAS v4u*)p = v; }
template <int AUX> DI void dma16(rsrc_t rs, lds_t dst, int voff, int soff) { __builtin_amdgcn_raw_ptr_buffer_load_lds(rs, (LAS void*)dst, 16, voff, soff, 0, AUX); }
template <int AUX> DI v4u ld128(rsrc_t rs, int voff, int soff = 0) { return __builtin_amdgcn_raw_buffer_load_b128(rs, voff, soff, AUX); }
template <int AUX> DI v2u ld64(rsrc_t rs, int voff, int soff = 0) { return __builtin_amdgcn_raw_buffer_load_b64(rs, voff, soff, AUX); }
template <int AUX> DI void st128(v4u v, rsrc_t rs, int voff, int soff = 0) { __builtin_amdgcn_raw_buffer_store_b128(v, rs, voff, soff, AUX); }
DI f4 asf4(v4u v) { return __builtin_bit_cast(f4, v); }
DI v4u asu4(f4 v) { return __builtin_bit_cast(v4u, v); }
#define VMWAIT(n) asm volatile("s_waitcnt vmcnt(" #n ")" ::: "memory")
#define LGWAIT0() asm volatile("s_waitcnt lgkmcnt(0)" ::: "memory")
DI void wgbar() { asm volatile("s_waitcnt lgkmcnt(0)" ::: "memory"); __builtin_amdgcn_s_barrier(); asm volatile("" ::: "memory"); }
DI float xor16(float v) { return __shfl_xor(v, 16); }
DI float xor32(float v) { return __shfl_xor(v, 32); }

struct Ids { int lane, w, l15, g; };
DI Ids get_ids() { Ids d; d.lane = threadIdx.x & 63; d.w = __builtin_amdgcn_readfirstlane(threadIdx.x >> 6); d.l15 = d.lane & 15; d.g = d.lane >> 4; return d; }

DI void gen_tf(const Ids& id, int j, v4u (&T)[14]) {
  const int c = 64 * j + 16 * id.w + id.l15;
  const bool dc = c == 447;
  const float kf = dc ? 0.f : (float)((c >> 1) + 1) * (1.0f / 448.0f);
  const float base = __builtin_fmaf((float)(8 * id.g), kf, (c & 1) && !dc ? 0.25f : 0.f);
#pragma unroll
  for (int ks = 0; ks < 14; ++ks) {
    unsigned u[4];
#pragma unroll
    for (int e = 0; e < 4; ++e)
      u[e] = pk_bf(vcos(__builtin_fmaf((float)(32 * ks + 2 * e), kf, base)), vcos(__builtin_fmaf((float)(32 * ks + 2 * e + 1), kf, base)));
    T[ks] = v4u{u[0], u[1], u[2], u[3]};
  }
}
template <int AUX> DI void f0_issue(const Ids& id, rsrc_t rx, int xbase, int i, lds_t L) {
  const int r7 = id.lane >> 3;
  const int v0 = r7 * 896, b = id.lane & 7;
#pragma unroll
  for (int n = 0; n < 14; ++n) {
    const int pc = 14 * id.w + n, a = pc >> 3, rb = pc & 7;
    const int xr = (rb & 3) * 2 + ((r7 >> 1) & 1);
    dma16<AUX>(rx, L + L_A + pc * 1024, v0 + ((b ^ xr) << 4), xbase + (64 * i + 8 * rb) * 896 + a * 128);
  }
}
template <int AUX> DI void f0_compute(const Ids& id, rsrc_t rw, int layer, int i, int j, lds_t L, const v4u (&T)[14]) {
  const int rho = id.l15, xr = (rho >> 1) & 7;
  const int rowb = (8 * (rho >> 2) + (rho & 3)) * 128;
  lds_t p0 = L + L_A + rowb + ((id.g ^ xr) << 4), p1 = L + L_A + rowb + (((id.g ^ xr) << 4) ^ 64);
  f4 acc[4];
#pragma unroll
  for (int t = 0; t < 4; ++t) acc[t] = f4{0.f, 0.f, 0.f, 0.f};
#pragma unroll
  for (int ks = 0; ks < 14; ++ks)
#pragma unroll
    for (int t = 0; t < 4; ++t) {
      const v4u a = ldsr128((ks & 1 ? p1 : p0) + (ks >> 1) * 8192 + (32 * (t >> 1) + 4 * (t & 1)) * 128);
      acc[t] = mfma_bf(a, T[ks], acc[t]);
    }
  const int c = 64 * j + 16 * id.w + id.l15;
  if (j == 5) {
    const int to = (32 * (id.w >> 1) + 4 * (id.w & 1)) * 128;
    float e0 = 0.f, e1 = 0.f;
#pragma unroll
    for (int ks = 0; ks < 14; ++ks) {
      const v4u a = ldsr128((ks & 1 ? p1 : p0) + (ks >> 1) * 8192 + to);
#pragma unroll
      for (int e = 0; e < 4; ++e) { const float lo = bf_lo(a[e]), hi = bf_hi(a[e]); e0 = __builtin_fmaf(lo, lo, e0); e1 = __builtin_fmaf(hi, hi, e1); }
    }
    float e = e0 + e1; e += xor16(e); e += xor32(e);
    const int sl = 32 * (id.w >> 1) + 4 * (id.w & 1) + 8 * (rho >> 2) + (rho & 3);
    if (id.g == 0) __builtin_amdgcn_raw_buffer_store_b32(__builtin_bit_cast(unsigned, e), rw, ST_OFF + (2 * layer + 1) * S * 4 + (64 * i + sl) * 4, 0, AUX);
  }
  if (c == 447) {
#pragma unroll
    for (int t = 0; t < 4; ++t) st128<AUX>(asu4(acc[t]), rw, ST_OFF + (2 * layer) * S * 4 + (64 * i + 32 * (t >> 1) + 8 * id.g + 4 * (t & 1)) * 4);
  }
  const float sc = c == 447 ? 0.f : (c == 446 ? 0.047245559126153f : 0.066815310478106f);
#pragma unroll
  for (int q = 0; q < 2; ++q) {
    const f4 a = acc[2 * q] * sc, b = acc[2 * q + 1] * sc;
    st128<AUX>(v4u{pk_h(a[0], a[1]), pk_h(a[2], a[3]), pk_h(b[0], b[1]), pk_h(b[2], b[3])}, rw, c * 128 + 64 * q + 16 * id.g, XH0_OFF + layer * XB + i * 57344);
  }
}
DI void wt_transpose(const Params& p, int b) {
  const int k1 = (b & 7) * 8 + ((b >> 3) & 7), g = b >> 6, t = threadIdx.x;
  if (t < 224) {
    unsigned u[8];
#pragma unroll
    for (int m = 0; m < 8; ++m) {
      const int o = (k1 + 512 * g + 64 * m) * NKF + t + 1;
      u[m] = (unsigned)p.wr[o] | ((unsigned)p.wi[o] << 16);
    }
    v4u* dst = (v4u*)(p.ws + WT_OFF + t * 8192 + (k1 >> 4) * 2048 + (g * 16 + (k1 & 15)) * 32);
    dst[0] = v4u{u[0], u[1], u[2], u[3]}; dst[1] = v4u{u[4], u[5], u[6], u[7]};
  }
}

struct PC { v4u fr64[4], fi64[4], fr32[2][2], fi32[2][2]; float twc[8], tws[8], tic[8], tis[8]; };
DI void cs_tuple(const float (&x)[4], v4u& re, v4u& im) {
  float c[4], s[4];
#pragma unroll
  for (int e = 0; e < 4; ++e) { c[e] = vcos(x[e]); s[e] = vsin(x[e]); }
  const unsigned c0 = pk_h(c[0], c[1]), c1 = pk_h(c[2], c[3]), s0 = pk_h(s[0], s[1]), s1 = pk_h(s[2], s[3]);
  re = v4u{c0, c1, s0, s1}; im = v4u{s0 ^ 0x80008000u, s1 ^ 0x80008000u, c0, c1};
}
DI void gen_pc(const Ids& id, PC& k) {
  const float a64 = (float)(16 * id.w + id.l15) * (1.0f / 64.0f), g4 = (float)(4 * id.g);
#pragma unroll
  for (int ks = 0; ks < 4; ++ks) {
    float x[4];
#pragma unroll
    for (int e = 0; e < 4; ++e) x[e] = (g4 + (float)(16 * ks + e)) * a64;
    cs_tuple(x, k.fr64[ks], k.fi64[ks]);
  }
  const float pl = (float)(8 * (id.l15 >> 2) + (id.l15 & 3)), g8 = (float)(8 * id.g);
#pragma unroll
  for (int tm = 0; tm < 2; ++tm)
#pragma unroll
    for (int tk = 0; tk < 2; ++tk) {
      float x[4];
#pragma unroll
      for (int e = 0; e < 4; ++e) x[e] = (g8 + (float)(4 * tk + e)) * ((pl + (float)(4 * tm)) * (1.0f / 32.0f));
      cs_tuple(x, k.fr32[tm][tk], k.fi32[tm][tk]);
    }
  const float k1a = (float)(16 * id.w + id.l15) * (1.0f / 2048.0f), k1b = (float)(16 * id.w + 4 * id.g);
#pragma unroll
  for (int m = 0; m < 8; ++m) {
    const float x = (g8 + (float)m) * k1a;
    k.twc[m] = vcos(x) * 0.125f; k.tws[m] = vsin(x) * 0.125f;
    const float y = (k1b + (float)(m & 3)) * ((pl + (float)(4 * (m >> 2))) * (1.0f / 2048.0f));
    k.tic[m] = vcos(y) * 0.17677669529663687f; k.tis[m] = vsin(y) * 0.17677669529663687f;
  }
}
template <int AUX> DI void p_phase(const Ids& id, rsrc_t rw, int layer, int jj, lds_t L, const PC& k, const v4u (&wt)[2]) {
  const int s0 = 512 * id.w + 32 * id.l15 + 8 * id.g;
  const int xo = (s0 >> 6) * 57344 + (s0 & 63) * 2 + jj * 256;
  const v4u xr = ld128<AUX>(rw, xo, XH0_OFF + layer * XB), xi = ld128<AUX>(rw, xo + 128, XH0_OFF + layer * XB);
  const f4 s1a = asf4(ld128<AUX>(rw, s0 * 4, ST_OFF + 2 * layer * S * 4)), s1b = asf4(ld128<AUX>(rw, s0 * 4 + 16, ST_OFF + 2 * layer * S * 4));
  const f4 s2a = asf4(ld128<AUX>(rw, s0 * 4, ST_OFF + (2 * layer + 1) * S * 4)), s2b = asf4(ld128<AUX>(rw, s0 * 4 + 16, ST_OFF + (2 * layer + 1) * S * 4));
  float r[8];
#pragma unroll
  for (int e = 0; e < 8; ++e) {
    const float m = (e < 4 ? s1a[e & 3] : s1b[e & 3]) * (1.0f / 448.0f), q = (e < 4 ? s2a[e & 3] : s2b[e & 3]) * (1.0f / 448.0f);
    r[e] = __builtin_amdgcn_rsqf(__builtin_fmaxf(__builtin_fmaf(-m, m, q), 0.f) + EPS);
  }
  v4u zr, zi;
#pragma unroll
  for (int e = 0; e < 4; ++e) {
    zr[e] = pk_h(h_lo(xr[e]) * r[2 * e], h_hi(xr[e]) * r[2 * e + 1]);
    zi[e] = pk_h(h_lo(xi[e]) * r[2 * e], h_hi(xi[e]) * r[2 * e + 1]);
  }
  lds_t zp = L + L_A + (16 * id.w + id.l15) * 64 + id.g * 16;
  ldsw128(zp, zr); ldsw128(zp + 4096, zi);
  wgbar();
  f4 ar[2], ai[2];
  lds_t tp = L + L_A + (4 * id.g + (id.l15 >> 2)) * 64 + (id.l15 & 3) * 16;
#pragma unroll
  for (int t = 0; t < 2; ++t) {
    ar[t] = f4{0.f, 0.f, 0.f, 0.f}; ai[t] = ar[t];
#pragma unroll
    for (int ks = 0; ks < 4; ++ks) {
      const v4u a = cat(trread(tp + ks * 1024 + t * 8), trread(tp + 4096 + ks * 1024 + t * 8));
      ar[t] = mfma_h(a, k.fr64[ks], ar[t]); ai[t] = mfma_h(a, k.fi64[ks], ai[t]);
    }
  }
  v4u bq[2];
#pragma unroll
  for (int t = 0; t < 2; ++t) {
    float u[4], v[4];
#pragma unroll
    for (int e = 0; e < 4; ++e) {
      const float c = k.twc[4 * t + e], s = k.tws[4 * t + e];
      u[e] = ar[t][e] * c + ai[t][e] * s; v[e] = ai[t][e] * c - ar[t][e] * s;
    }
    bq[t] = v4u{pk_h(u[0], u[1]), pk_h(u[2], u[3]), pk_h(v[0], v[1]), pk_h(v[2], v[3])};
  }
  v4u yq[2];
#pragma unroll
  for (int tm = 0; tm < 2; ++tm) {
    f4 yr = {0.f, 0.f, 0.f, 0.f}, yi = yr;
#pragma unroll
    for (int tk = 0; tk < 2; ++tk) { yr = mfma_h(k.fr32[tm][tk], bq[tk], yr); yi = mfma_h(k.fi32[tm][tk], bq[tk], yi); }
    float u[4], v[4];
#pragma unroll
    for (int e = 0; e < 4; ++e) {
      const float wr = bf_lo(wt[tm][e]) * 0.17677669529663687f, wi = bf_hi(wt[tm][e]) * 0.17677669529663687f;
      u[e] = yr[e] * wr - yi[e] * wi; v[e] = -(yr[e] * wi + yi[e] * wr);
    }
    yq[tm] = v4u{pk_h(u[0], u[1]), pk_h(u[2], u[3]), pk_h(v[0], v[1]), pk_h(v[2], v[3])};
  }
#pragma unroll
  for (int t = 0; t < 2; ++t) {
    f4 br = {0.f, 0.f, 0.f, 0.f}, bi = br;
#pragma unroll
    for (int tk = 0; tk < 2; ++tk) { br = mfma_h(yq[tk], k.fr32[t][tk], br); bi = mfma_h(yq[tk], k.fi32[t][tk], bi); }
    float u[4], v[4];
#pragma unroll
    for (int e = 0; e < 4; ++e) {
      const float c = k.tic[4 * t + e], s = k.tis[4 * t + e];
      u[e] = br[e] * c + bi[e] * s; v[e] = bi[e] * c - br[e] * s;
    }
    ldsw128(L + L_A + 8192 + (2 * id.w + t) * 1024 + id.lane * 16, v4u{pk_h(u[0], u[1]), pk_h(u[2], u[3]), pk_h(v[0], v[1]), pk_h(v[2], v[3])});
  }
  wgbar();
  f4 or_[2], oi[2];
#pragma unroll
  for (int t = 0; t < 2; ++t) {
    or_[t] = f4{0.f, 0.f, 0.f, 0.f}; oi[t] = or_[t];
#pragma unroll
    for (int ks = 0; ks < 4; ++ks) {
      const v4u a = ldsr128(L + L_A + 8192 + (2 * ks + t) * 1024 + id.lane * 16);
      or_[t] = mfma_h(a, k.fr64[ks], or_[t]); oi[t] = mfma_h(a, k.fi64[ks], oi[t]);
    }
  }
  const float si = jj == 223 ? 0.f : -0.125f;
  v4u o0, o1;
#pragma unroll
  for (int e = 0; e < 4; ++e) {
    const int t = e >> 1, a = 2 * (e & 1);
    o0[e] = pk_h(__builtin_fmaf(or_[t][a], 0.125f, h_lo(xr[e])), __builtin_fmaf(or_[t][a + 1], 0.125f, h_hi(xr[e])));
    o1[e] = pk_h(__builtin_fmaf(oi[t][a], si, h_lo(xi[e])), __builtin_fmaf(oi[t][a + 1], si, h_hi(xi[e])));
  }
  st128<AUX>(o0, rw, xo, XH1_OFF + layer * XB); st128<AUX>(o1, rw, xo + 128, XH1_OFF + layer * XB);
}

DI void gen_ti(const Ids& id, int j, v4u (&T)[14]) {
  const float df = (float)(64 * j + 16 * id.w + id.l15) * (1.0f / 448.0f);
  const float o = (float)(16 * (id.w & 1));
  const float be = ((float)(2 * id.g + 1) + o) * df, bo = ((float)(2 * id.g + 1) - o) * df;
#pragma unroll
  for (int m = 0; m < 14; ++m) {
    unsigned u[4];
#pragma unroll
    for (int e = 0; e < 4; ++e) {
      const float x = __builtin_fmaf((float)(16 * m + 8 * (e >> 1) + (e & 1)), df, m & 1 ? bo : be);
      float c = vcos(x);
      if (m >= 12 && e == 3) c *= ((m ^ (id.w & 1)) == 13 && id.g == 3) ? 0.70710678118654752f : 1.0f;
      u[e] = pk_h(c, -vsin(x));
    }
    T[m] = v4u{u[0], u[1], u[2], u[3]};
  }
}
template <int AUX> DI void s3_issue(const Ids& id, rsrc_t rw, int layer, int i, lds_t L) {
  const int r7 = id.lane >> 3;
  const int vo = r7 * 128 + (((id.lane & 7) ^ (2 * ((r7 >> 1) & 3))) << 4);
#pragma unroll
  for (int n = 0; n < 14; ++n) dma16<AUX>(rw, L + L_A + (14 * id.w + n) * 1024, vo, XH1_OFF + layer * XB + i * 57344 + (14 * id.w + n) * 1024);
}
template <int AUX> DI void s3_compute(const Ids& id, rsrc_t rw, int layer, int i, int j, lds_t L, const v4u (&T)[14], f4 (&x1)[4]) {
  f4 mu[4];
#pragma unroll
  for (int n = 0; n < 4; ++n) mu[n] = asf4(ld128<AUX>(rw, (64 * i + 16 * (n ^ (id.w & 2)) + 4 * id.g) * 4, ST_OFF + 2 * layer * S * 4));
  const int q = id.l15 >> 2, p = id.l15 & 3, row = 4 * id.g + q, fr = (row >> 1) & 3;
  lds_t tb[4];
#pragma unroll
  for (int n = 0; n < 4; ++n) tb[n] = L + L_A + row * 128 + 32 * ((n ^ (id.w & 2)) ^ fr) + 8 * p;
  const int ko = (id.w & 1) * 4096;
  f4 acc[4];
#pragma unroll
  for (int n = 0; n < 4; ++n) acc[n] = f4{0.f, 0.f, 0.f, 0.f};
  float e0 = 0.f, e1 = 0.f;
#pragma unroll
  for (int m = 0; m < 14; ++m)
#pragma unroll
    for (int n = 0; n < 4; ++n) {
      lds_t a0 = tb[n] + (m & 1 ? -ko : ko) + m * 4096;
      const v4u a = cat(trread(a0), trread(a0 + 2048));
      acc[n] = mfma_h(a, T[m], acc[n]);
      if (n < 2 && !(m & 1)) {
#pragma unroll
        for (int e = 0; e < 4; ++e) {
          const unsigned ae = a[e]; const h2 v = __builtin_bit_cast(h2, ae);
          if (n == 0) e0 = __builtin_amdgcn_fdot2(v, v, e0, false); else e1 = __builtin_amdgcn_fdot2(v, v, e1, false);
        }
      }
    }
  e0 += xor16(e0); e1 += xor16(e1); e0 += xor32(e0); e1 += xor32(e1);
  LAS float* eb = (LAS float*)(L + L_E);
  if (id.g < 2) eb[64 * (id.w & 1) + 16 * ((id.w & 2) + id.g) + id.l15] = id.g ? e1 : e0;
  wgbar();
  const float v = (eb[id.lane] + eb[64 + id.lane]) * (1.0f / 448.0f);
  const float r1 = __builtin_amdgcn_rsqf(v + EPS);
  const float r = r1 * __builtin_amdgcn_rsqf(v * r1 * r1 + EPS);
  LAS float* rb = (LAS float*)(L + L_R) + 64 * id.w;
  rb[id.lane] = r;
  LGWAIT0();
  f4 h[4];
#pragma unroll
  for (int n = 0; n < 4; ++n) {
    const f4 rr = *(LAS f4*)(rb + 16 * (n ^ (id.w & 2)) + 4 * id.g);
    const f4 xc = acc[n] * 0.066815310478106f;
    h[n] = xc * rr; x1[n] = xc + mu[n] * (1.0f / 448.0f);
  }
#pragma unroll
  for (int qq = 0; qq < 2; ++qq) {
    const f4 a = h[2 * qq], b = h[2 * qq + 1];
    st128<AUX>(v4u{pk_bf(a[0], a[1]), pk_bf(a[2], a[3]), pk_bf(b[0], b[1]), pk_bf(b[2], b[3])}, rw, id.lane * 16,
               HF_OFF + layer * XB + ((j * 64 + 2 * i + (qq ^ (id.w >> 1))) * 4 + id.w) * 1024);
  }
}

template <int K0, int K1> DI void w_issue(const Ids& id, rsrc_t rs, int pitch, int k0, int col0, lds_t L) {
  const int r7 = id.lane >> 3;
  const int vo = r7 * pitch + (((id.lane & 7) ^ (2 * ((r7 >> 1) & 3))) << 4);
#pragma unroll
  for (int ks = K0; ks < K1; ++ks)
#pragma unroll
    for (int n = 0; n < 4; ++n) dma16<0>(rs, L + L_W + id.w * 16384 + ((ks & 3) * 4 + n) * 1024, vo, (k0 + 32 * ks + 8 * n) * pitch + col0);
}
template <int K0, int K1, int NKS> DI void w_read(const Ids& id, lds_t L, v4u (&W)[NKS][4]) {
  const int q = id.l15 >> 2, p = id.l15 & 3, row = 4 * id.g + q, fr = (row >> 1) & 3;
#pragma unroll
  for (int n = 0; n < 4; ++n) {
    lds_t b = L + L_W + id.w * 16384 + row * 128 + 32 * ((n ^ (id.w & 2)) ^ fr) + 8 * p;
#pragma unroll
    for (int ks = K0; ks < K1; ++ks) W[ks][n] = cat(trread(b + (ks & 3) * 4096), trread(b + (ks & 3) * 4096 + 2048));
  }
}
template <int NKS> DI void w_load_all(const Ids& id, rsrc_t rs, int pitch, int k0, int col0, lds_t L, v4u (&W)[NKS][4]) {
  w_issue<0, 4>(id, rs, pitch, k0, col0, L);
  VMWAIT(8); w_read<0, 2>(id, L, W); LGWAIT0(); w_issue<4, 6>(id, rs, pitch, k0, col0, L);
  VMWAIT(8); w_read<2, 4>(id, L, W); LGWAIT0(); w_issue<6, 8>(id, rs, pitch, k0, col0, L);
  VMWAIT(8); w_read<4, 6>(id, L, W); LGWAIT0(); w_issue<8, 10>(id, rs, pitch, k0, col0, L);
  VMWAIT(8); w_read<6, 8>(id, L, W); LGWAIT0(); w_issue<10, 12>(id, rs, pitch, k0, col0, L);
  VMWAIT(8); w_read<8, 10>(id, L, W); LGWAIT0(); w_issue<12, 14>(id, rs, pitch, k0, col0, L);
  if constexpr (NKS == 16) {
    VMWAIT(8); w_read<10, 12>(id, L, W); LGWAIT0(); w_issue<14, 16>(id, rs, pitch, k0, col0, L);
    VMWAIT(8); w_read<12, 14>(id, L, W);
    VMWAIT(0); w_read<14, 16>(id, L, W);
  } else {
    VMWAIT(8); w_read<10, 12>(id, L, W);
    VMWAIT(0); w_read<12, 14>(id, L, W);
  }
  LGWAIT0();
}
template <int AUX, int NKS> DI void g_main(const Ids& id, rsrc_t rw, int abase, lds_t L, const v4u (&W)[NKS][4], f4 (&res)[4]) {
  constexpr int WIN = 6;
  v4u a[WIN][4];
  int vo[4];
#pragma unroll
  for (int n = 0; n < 4; ++n) vo[n] = (n ^ id.w) * 1024 + id.lane * 16;
  const int sb = abase + id.w * NKS * 4096;
#pragma unroll
  for (int ks = 0; ks < WIN; ++ks)
#pragma unroll
    for (int n = 0; n < 4; ++n) a[ks][n] = ld128<AUX>(rw, vo[n], sb + ks * 4096);
  __builtin_amdgcn_sched_barrier(0);
  f4 acc[4][4];
#pragma unroll
  for (int ks = 0; ks < NKS; ++ks) {
#pragma unroll
    for (int n = 0; n < 4; ++n) {
      const v4u cur = a[ks % WIN][n];
#pragma unroll
      for (int f = 0; f < 4; ++f) acc[f][n] = mfma_bf(W[ks][f], cur, ks == 0 ? f4{0.f, 0.f, 0.f, 0.f} : acc[f][n]);
      if (ks + WIN < NKS) a[ks % WIN][n] = ld128<AUX>(rw, vo[n], sb + (ks + WIN) * 4096);
      __builtin_amdgcn_sched_barrier(0);
    }
  }
#pragma unroll
  for (int n = 1; n < 4; ++n)
#pragma unroll
    for (int f = 0; f < 4; ++f) ldsw128(L + L_A + (((id.w ^ n) * 3 + n - 1) * 4 + (f ^ (id.w & 2))) * 1024 + id.lane * 16, asu4(acc[f][n]));
  wgbar();
#pragma unroll
  for (int f = 0; f < 4; ++f) {
    f4 s = acc[f][0];
#pragma unroll
    for (int n = 0; n < 3; ++n) s += asf4(ldsr128(L + L_A + ((id.w * 3 + n) * 4 + (f ^ (id.w & 2))) * 1024 + id.lane * 16));
    res[f] = s;
  }
}
DI void load_bias(const Ids& id, const unsigned short* b, int o, v2u (&bb)[4]) {
#pragma unroll
  for (int f = 0; f < 4; ++f) bb[f] = *(const GAS v2u*)(b + o + 16 * (f ^ (id.w & 2)) + 4 * id.g);
}
DI f4 bias4(v2u b) { return f4{bf_lo(b.x), bf_hi(b.x), bf_lo(b.y), bf_hi(b.y)}; }
DI float gelu(float u) {
  const float z = u * __builtin_fmaf(u * u, 0.044715f * -2.302208198144325f, -2.302208198144325f);
  return u * __builtin_amdgcn_rcpf(1.0f + __builtin_amdgcn_exp2f(z));
}
template <int AUX> DI void g1_epi(const Ids& id, rsrc_t rw, int layer, int j, int m, const f4 (&res)[4], const v2u (&bb)[4]) {
#pragma unroll
  for (int qq = 0; qq < 2; ++qq) {
    const f4 a = res[2 * qq] + bias4(bb[2 * qq]), b = res[2 * qq + 1] + bias4(bb[2 * qq + 1]);
    st128<AUX>(v4u{pk_bf(gelu(a[0]), gelu(a[1])), pk_bf(gelu(a[2]), gelu(a[3])), pk_bf(gelu(b[0]), gelu(b[1])), pk_bf(gelu(b[2]), gelu(b[3]))}, rw, id.lane * 16,
               GF_OFF + layer * GB + ((j * 56 + 2 * m + (qq ^ (id.w >> 1))) * 4 + id.w) * 1024);
  }
}
template <int AUX> DI void g2_epi(const Ids& id, rsrc_t ro, int obase, int i, int j, lds_t L, const f4 (&res)[4], const v2u (&bb)[4], const f4 (&x1)[4]) {
  lds_t T = L + L_A + 49152;
  {
    const int d = 16 * id.w + id.l15, hd = id.l15 ^ (id.w & 1) ^ ((id.w >> 1) << 2);
#pragma unroll
    for (int n = 0; n < 4; ++n) {
      const f4 v = res[n] + bias4(bb[n]) + x1[n];
      *(LAS v2u*)(T + d * 128 + (((4 * (n ^ (id.w & 2)) + id.g) ^ hd) << 3)) = v2u{pk_bf(v[0], v[1]), pk_bf(v[2], v[3])};
    }
  }
  wgbar();
  const int q = id.l15 >> 2, p = id.l15 & 3;
#pragma unroll
  for (int e = 0; e < 2; ++e) {
    const int sc = 4 * id.w + 2 * e + (id.g >> 1);
    v2u u[2];
#pragma unroll
    for (int hh = 0; hh < 2; ++hh) {
      const int d = 8 * (4 * (id.g & 1) + p) + 4 * hh + q;
      const int hd = (d & 15) ^ ((d >> 4) & 1) ^ (((d >> 5) & 1) << 2);
      u[hh] = trread(T + d * 128 + ((sc ^ hd) << 3));
    }
    const int row = 64 * i + 16 * id.w + 8 * e + 4 * (id.lane >> 5) + (id.lane & 3);
    st128<AUX>(cat(u[0], u[1]), ro, row * 896 + j * 128 + 16 * ((id.lane >> 2) & 7), obase);
  }
}

#define SMEM extern __shared__ __attribute__((aligned(16))) unsigned char smem[]; lds_t L = (lds_t)smem
__global__ void __launch_bounds__(256, 1) k_f0(Params p, int layer) {
  SMEM; const Ids id = get_ids(); const int b = blockIdx.x, q = b >> 3;
  const rsrc_t rw = mkrs(p.ws, WS_BYTES);
  if (q < 28) {
    const int i = (b & 7) + 8 * (q / 7), j = q % 7;
    if (layer == 0) f0_issue<0>(id, mkrs(p.x, XB), 0, i, L); else f0_issue<0>(id, rw, X2_OFF, i, L);
    if (layer == 0) wt_transpose(p, b);
    v4u T[14]; gen_tf(id, j, T);
    VMWAIT(0); wgbar();
    f0_compute<0>(id, rw, layer, i, j, L, T);
  } else if (layer == 0) wt_transpose(p, b);
}
__global__ void __launch_bounds__(256, 1) k_p(Params p, int layer) {
  SMEM; const Ids id = get_ids(); const int b = blockIdx.x, q = b >> 3;
  const rsrc_t rw = mkrs(p.ws, WS_BYTES);
  if (q >= 28) return;
  const int jj = (b & 7) * 28 + q;
  v4u wt[2];
  wt[0] = ld128<0>(rw, threadIdx.x * 32, WT_OFF + jj * 8192); wt[1] = ld128<0>(rw, threadIdx.x * 32 + 16, WT_OFF + jj * 8192);
  PC k; gen_pc(id, k);
  p_phase<0>(id, rw, layer, jj, L, k, wt);
}
__global__ void __launch_bounds__(256, 1) k_s3(Params p, int layer) {
  SMEM; const Ids id = get_ids(); const int b = blockIdx.x, q = b >> 3;
  const rsrc_t rw = mkrs(p.ws, WS_BYTES);
  if (q >= 28) return;
  const int i = (b & 7) + 8 * (q / 7), j = q % 7;
  s3_issue<0>(id, rw, layer, i, L);
  v4u T[14]; gen_ti(id, j, T);
  VMWAIT(0); wgbar();
  f4 x1[4];
  s3_compute<0>(id, rw, layer, i, j, L, T, x1);
#pragma unroll
  for (int n = 0; n < 4; ++n) st128<0>(asu4(x1[n]), rw, threadIdx.x * 16, X1_OFF + (b * 4 + n) * 4096);
}
__global__ void __launch_bounds__(256, 1) k_g1(Params p, int layer) {
  SMEM; const Ids id = get_ids(); const int b = blockIdx.x, q = b >> 3;
  const rsrc_t rw = mkrs(p.ws, WS_BYTES);
  const int m = (b & 7) + 8 * (q / 7), j = q % 7;
  if (q >= 28 || m >= 28) return;
  v2u bb[4]; load_bias(id, p.b1, 64 * m, bb);
  v4u W[16][4];
  w_load_all<16>(id, mkrs(p.w1, S * F * 2), F * 2, 512 * id.w, m * 128, L, W);
  f4 res[4];
  g_main<0, 16>(id, rw, HF_OFF + layer * XB + j * 64 * 4096, L, W, res);
  g1_epi<0>(id, rw, layer, j, m, res, bb);
}
__global__ void __launch_bounds__(256, 1) k_g2(Params p, int layer) {
  SMEM; const Ids id = get_ids(); const int b = blockIdx.x, q = b >> 3;
  const rsrc_t rw = mkrs(p.ws, WS_BYTES);
  if (q >= 28) return;
  const int i = (b & 7) + 8 * (q / 7), j = q % 7;
  v2u bb[4]; load_bias(id, p.b2, 64 * i, bb);
  f4 x1[4];
#pragma unroll
  for (int n = 0; n < 4; ++n) x1[n] = asf4(ld128<0>(rw, threadIdx.x * 16, X1_OFF + (b * 4 + n) * 4096));
  v4u W[14][4];
  w_load_all<14>(id, mkrs(p.w2, S * F * 2), S * 2, 448 * id.w, i * 128, L, W);
  f4 res[4];
  g_main<0, 14>(id, rw, GF_OFF + layer * GB + j * 56 * 4096, L, W, res);
  if (layer == 0) g2_epi<0>(id, rw, X2_OFF, i, j, L, res, bb, x1); else g2_epi<0>(id, mkrs(p.out, XB), 0, i, j, L, res, bb, x1);
}

extern "C" void kernel_launch(void* const* d_in, const int* in_sizes, int n_in, void* d_out, int out_size, void* d_ws, size_t ws_size, hipStream_t stream) {
  Params p;
  p.x = (const unsigned short*)d_in[0]; p.wr = (const unsigned short*)d_in[1]; p.wi = (const unsigned short*)d_in[2];
  p.w1 = (const unsigned short*)d_in[4]; p.b1 = (const unsigned short*)d_in[5]; p.w2 = (const unsigned short*)d_in[6]; p.b2 = (const unsigned short*)d_in[7];
  p.out = (unsigned short*)d_out; p.ws = (unsigned char*)d_ws;
  (void)hipFuncSetAttribute((const void*)k_f0, hipFuncAttributeMaxDynamicSharedMemorySize, LDS_BYTES);
  (void)hipFuncSetAttribute((const void*)k_p, hipFuncAttributeMaxDynamicSharedMemorySize, LDS_BYTES);
  (void)hipFuncSetAttribute((const void*)k_s3, hipFuncAttributeMaxDynamicSharedMemorySize, LDS_BYTES);
  (void)hipFuncSetAttribute((const void*)k_g1, hipFuncAttributeMaxDynamicSharedMemorySize, LDS_BYTES);
  (void)hipFuncSetAttribute((const void*)k_g2, hipFuncAttributeMaxDynamicSharedMemorySize, LDS_BYTES);
  for (int layer = 0; layer < 2; ++layer) {
    k_f0<<<256, 256, LDS_BYTES, stream>>>(p, layer);
    k_p<<<256, 256, LDS_BYTES, stream>>>(p, layer);
    k_s3<<<256, 256, LDS_BYTES, stream>>>(p, layer);
    k_g1<<<256, 256, LDS_BYTES, stream>>>(p, layer);
    k_g2<<<256, 256, LDS_BYTES, stream>>>(p, layer);
  }
}
```

```cpp
#include <hip/hip_runtime.h>
#include <hip/hip_bf16.h>
#include <stdint.h>

#define DI __device__ __forceinline__
#define GAS __attribute__((address_space(1)))
#define LAS __attribute__((address_space(3)))
typedef unsigned v4u __attribute__((ext_vector_type(4)));
typedef unsigned v2u __attribute__((ext_vector_type(2)));
typedef float f4 __attribute__((ext_vector_type(4)));
typedef float f2 __attribute__((ext_vector_type(2)));
typedef _Float16 h8 __attribute__((ext_vector_type(8)));
typedef _Float16 h2 __attribute__((ext_vector_type(2)));
typedef __bf16 b8 __attribute__((ext_vector_type(8)));
typedef __bf16 b2 __attribute__((ext_vector_type(2)));
typedef short s4 __attribute__((ext_vector_type(4)));
typedef __amdgpu_buffer_rsrc_t rsrc_t;
typedef LAS unsigned char* lds_t;

constexpr int S = 2048, D = 448, F = 1792, NKF = 225;
constexpr float EPS = 1e-5f;
constexpr int XB = S * D * 2;
constexpr int GB = D * F * 2;
constexpr int CTRL_OFF = 0, CTRL_BYTES = 262144;
constexpr int ST_OFF = CTRL_BYTES;
constexpr int WT_OFF = ST_OFF + 4 * S * 4;
constexpr int XH0_OFF = WT_OFF + XB;
constexpr int XH1_OFF = XH0_OFF + 2 * XB;
constexpr int HF_OFF = XH1_OFF + 2 * XB;
constexpr int GF_OFF = HF_OFF + 2 * XB;
constexpr int X2_OFF = GF_OFF + 2 * GB;
constexpr int WS_BYTES = X2_OFF + XB;
#define STAMP(n)
constexpr int L_A = 0, L_E = 57344, L_R = L_E + 512, L_W = 59392, WSLOT = 6, LDS_BYTES = L_W + 4 * WSLOT * 4096;

struct Params {
  const unsigned short* x; const unsigned short* wr; const unsigned short* wi;
  const unsigned short* w1; const unsigned short* b1; const unsigned short* w2; const unsigned short* b2;
  unsigned short* out; unsigned char* ws;
};

DI rsrc_t mkrs(const void* p, int bytes) { return __builtin_amdgcn_make_buffer_rsrc((void*)p, 0, bytes, 0x00020000); }
DI f4 mfma_bf(v4u a, v4u b, f4 c) { return __builtin_amdgcn_mfma_f32_16x16x32_bf16(__builtin_bit_cast(b8, a), __builtin_bit_cast(b8, b), c, 0, 0, 0); }
DI f4 mfma_h(v4u a, v4u b, f4 c) { return __builtin_amdgcn_mfma_f32_16x16x32_f16(__builtin_bit_cast(h8, a), __builtin_bit_cast(h8, b), c, 0, 0, 0); }
DI unsigned pk_bf(float a, float b) { f2 v = {a, b}; return __builtin_bit_cast(unsigned, __builtin_convertvector(v, b2)); }
DI unsigned pk_h(float a, float b) { f2 v = {a, b}; return __builtin_bit_cast(unsigned, __builtin_convertvector(v, h2)); }
DI float h_lo(unsigned u) { return (float)__builtin_bit_cast(h2, u)[0]; }
DI float h_hi(unsigned u) { return (float)__builtin_bit_cast(h2, u)[1]; }
DI float bf_lo(unsigned u) { return __builtin_bit_cast(float, u << 16); }
DI float bf_hi(unsigned u) { return __builtin_bit_cast(float, u & 0xffff0000u); }
DI float vcos(float x) { return __builtin_amdgcn_cosf(x); }
DI float vsin(float x) { return __builtin_amdgcn_sinf(x); }
DI v2u trread(lds_t p) { return __builtin_bit_cast(v2u, __builtin_amdgcn_ds_read_tr16_b64_v4i16((LAS s4*)p)); }
DI v4u cat(v2u a, v2u b) { return v4u{a.x, a.y, b.x, b.y}; }
DI v4u ldsr128(lds_t p) { return *(LAS v4u*)p; }
DI void ldsw128(lds_t p, v4u v) { *(LAS v4u*)p = v; }
template <int AUX> DI void dma16(rsrc_t rs, lds_t dst, int voff, int soff) {
  const unsigned la = (unsigned)(size_t)dst;
  if constexpr (AUX == 16) asm volatile("s_mov_b32 m0, %0\n\ts_nop 0\n\tbuffer_load_dwordx4 %1, %2, %3 offen sc1 lds" ::"s"(la), "v"(voff), "s"(rs), "s"(soff) : "memory");
  else asm volatile("s_mov_b32 m0, %0\n\ts_nop 0\n\tbuffer_load_dwordx4 %1, %2, %3 offen lds" ::"s"(la), "v"(voff), "s"(rs), "s"(soff) : "memory");
}
template <int N> DI void pin(v4u (&T)[N]) {
#pragma unroll
  for (int n = 0; n < N; ++n) asm volatile("" : "+v"(T[n]));
}
template <int N> DI void pinf(float (&T)[N]) {
#pragma unroll
  for (int n = 0; n < N; ++n) asm volatile("" : "+v"(T[n]));
}
template <int AUX> DI v4u ld128(rsrc_t rs, int voff, int soff = 0) { return __builtin_amdgcn_raw_buffer_load_b128(rs, voff, soff, AUX); }
template <int AUX> DI v2u ld64(rsrc_t rs, int voff, int soff = 0) { return __builtin_amdgcn_raw_buffer_load_b64(rs, voff, soff, AUX); }
template <int AUX> DI void st128(v4u v, rsrc_t rs, int voff, int soff = 0) { __builtin_amdgcn_raw_buffer_store_b128(v, rs, voff, soff, AUX); }
DI f4 asf4(v4u v) { return __builtin_bit_cast(f4, v); }
DI v4u asu4(f4 v) { return __builtin_bit_cast(v4u, v); }
#define VMWAIT(n) asm volatile("s_waitcnt vmcnt(" #n ")" ::: "memory")
#define LGWAIT0() asm volatile("s_waitcnt lgkmcnt(0)" ::: "memory")
DI void wgbar() { asm volatile("s_waitcnt lgkmcnt(0)" ::: "memory"); __builtin_amdgcn_s_barrier(); asm volatile("" ::: "memory"); }
DI float xor16(float v) { return __shfl_xor(v, 16); }
DI float xor32(float v) { return __shfl_xor(v, 32); }

struct Ids { int lane, w, l15, g; };
DI Ids get_ids() { Ids d; d.lane = threadIdx.x & 63; d.w = __builtin_amdgcn_readfirstlane(threadIdx.x >> 6); d.l15 = d.lane & 15; d.g = d.lane >> 4; return d; }

DI Ids fresh(const Ids& a) { Ids d; d.lane = a.lane; d.w = a.w; asm volatile("" : "+v"(d.lane), "+s"(d.w)); d.l15 = d.lane & 15; d.g = d.lane >> 4; return d; }
DI int fresh_s(int v) { asm volatile("" : "+s"(v)); return v; }

DI void gen_tf(const Ids& id, int j, v4u (&T)[14]) {
  const int c = 64 * j + 16 * id.w + id.l15;
  const bool dc = c == 447;
  const float kf = dc ? 0.f : (float)((c >> 1) + 1) * (1.0f / 448.0f);
  const float base = __builtin_fmaf((float)(8 * id.g), kf, (c & 1) && !dc ? 0.25f : 0.f);
#pragma unroll
  for (int ks = 0; ks < 14; ++ks) {
    unsigned u[4];
#pragma unroll
    for (int e = 0; e < 4; ++e)
      u[e] = pk_bf(vcos(__builtin_fmaf((float)(32 * ks + 2 * e), kf, base)), vcos(__builtin_fmaf((float)(32 * ks + 2 * e + 1), kf, base)));
    T[ks] = v4u{u[0], u[1], u[2], u[3]};
  }
}
template <int AUX> DI void f0_issue(const Ids& id, rsrc_t rx, int xbase, int i, lds_t L) {
  const int r7 = id.lane >> 3;
  const int v0 = r7 * 896, b = id.lane & 7;
#pragma unroll
  for (int n = 0; n < 14; ++n) {
    const int pc = 14 * id.w + n, a = pc >> 3, rb = pc & 7;
    const int xr = (rb & 3) * 2 + ((r7 >> 1) & 1);
    dma16<AUX>(rx, L + L_A + pc * 1024, v0 + ((b ^ xr) << 4), xbase + (64 * i + 8 * rb) * 896 + a * 128);
  }
}
template <int AUX> DI void f0_compute(const Ids& id, rsrc_t rw, int layer, int i, int j, lds_t L, const v4u (&T)[14]) {
  const int rho = id.l15, xr = (rho >> 1) & 7;
  const int rowb = (8 * (rho >> 2) + (rho & 3)) * 128;
  lds_t p0 = L + L_A + rowb + ((id.g ^ xr) << 4), p1 = L + L_A + rowb + (((id.g ^ xr) << 4) ^ 64);
  f4 acc[4];
#pragma unroll
  for (int t = 0; t < 4; ++t) acc[t] = f4{0.f, 0.f, 0.f, 0.f};
#pragma unroll
  for (int ks = 0; ks < 14; ++ks)
#pragma unroll
    for (int t = 0; t < 4; ++t) {
      const v4u a = ldsr128((ks & 1 ? p1 : p0) + (ks >> 1) * 8192 + (32 * (t >> 1) + 4 * (t & 1)) * 128);
      acc[t] = mfma_bf(a, T[ks], acc[t]);
    }
  const int c = 64 * j + 16 * id.w + id.l15;
  if (j == 5) {
    const int to = (32 * (id.w >> 1) + 4 * (id.w & 1)) * 128;
    float e0 = 0.f, e1 = 0.f;
#pragma unroll
    for (int ks = 0; ks < 14; ++ks) {
      const v4u a = ldsr128((ks & 1 ? p1 : p0) + (ks >> 1) * 8192 + to);
#pragma unroll
      for (int e = 0; e < 4; ++e) { const float lo = bf_lo(a[e]), hi = bf_hi(a[e]); e0 = __builtin_fmaf(lo, lo, e0); e1 = __builtin_fmaf(hi, hi, e1); }
    }
    float e = e0 + e1; e += xor16(e); e += xor32(e);
    const int sl = 32 * (id.w >> 1) + 4 * (id.w & 1) + 8 * (rho >> 2) + (rho & 3);
    if (id.g == 0) __builtin_amdgcn_raw_buffer_store_b32(__builtin_bit_cast(unsigned, e), rw, ST_OFF + (2 * layer + 1) * S * 4 + (64 * i + sl) * 4, 0, AUX);
  }
  if (c == 447) {
#pragma unroll
    for (int t = 0; t < 4; ++t) st128<AUX>(asu4(acc[t]), rw, ST_OFF + (2 * layer) * S * 4 + (64 * i + 32 * (t >> 1) + 8 * id.g + 4 * (t & 1)) * 4);
  }
  const float sc = c == 447 ? 0.f : (c == 446 ? 0.047245559126153f : 0.066815310478106f);
#pragma unroll
  for (int q = 0; q < 2; ++q) {
    const f4 a = acc[2 * q] * sc, b = acc[2 * q + 1] * sc;
    st128<AUX>(v4u{pk_h(a[0], a[1]), pk_h(a[2], a[3]), pk_h(b[0], b[1]), pk_h(b[2], b[3])}, rw, c * 128 + 64 * q + 16 * id.g, XH0_OFF + layer * XB + i * 57344);
  }
}
DI void wt_transpose(const Params& p, int b) {
  const int k1 = (b & 7) * 8 + ((b >> 3) & 7), g = b >> 6, t = threadIdx.x;
  if (t < 224) {
    unsigned u[8];
#pragma unroll
    for (int m = 0; m < 8; ++m) {
      const int o = (k1 + 512 * g + 64 * m) * NKF + t + 1;
      u[m] = (unsigned)p.wr[o] | ((unsigned)p.wi[o] << 16);
    }
    v4u* dst = (v4u*)(p.ws + WT_OFF + t * 8192 + (k1 >> 4) * 2048 + (g * 16 + (k1 & 15)) * 32);
    dst[0] = v4u{u[0], u[1], u[2], u[3]}; dst[1] = v4u{u[4], u[5], u[6], u[7]};
  }
}

struct PC { v4u fr64[4], fi64[4], fr32[2][2], fi32[2][2]; float twc[8], tws[8], tic[8], tis[8]; };
DI void cs_tuple(const float (&x)[4], v4u& re, v4u& im) {
  float c[4], s[4];
#pragma unroll
  for (int e = 0; e < 4; ++e) { c[e] = vcos(x[e]); s[e] = vsin(x[e]); }
  const unsigned c0 = pk_h(c[0], c[1]), c1 = pk_h(c[2], c[3]), s0 = pk_h(s[0], s[1]), s1 = pk_h(s[2], s[3]);
  re = v4u{c0, c1, s0, s1}; im = v4u{s0 ^ 0x80008000u, s1 ^ 0x80008000u, c0, c1};
}
DI void gen_pc(const Ids& id, PC& k) {
  const float a64 = (float)(16 * id.w + id.l15) * (1.0f / 64.0f), g4 = (float)(4 * id.g);
#pragma unroll
  for (int ks = 0; ks < 4; ++ks) {
    float x[4];
#pragma unroll
    for (int e = 0; e < 4; ++e) x[e] = (g4 + (float)(16 * ks + e)) * a64;
    cs_tuple(x, k.fr64[ks], k.fi64[ks]);
  }
  const float pl = (float)(8 * (id.l15 >> 2) + (id.l15 & 3)), g8 = (float)(8 * id.g);
#pragma unroll
  for (int tm = 0; tm < 2; ++tm)
#pragma unroll
    for (int tk = 0; tk < 2; ++tk) {
      float x[4];
#pragma unroll
      for (int e = 0; e < 4; ++e) x[e] = (g8 + (float)(4 * tk + e)) * ((pl + (float)(4 * tm)) * (1.0f / 32.0f));
      cs_tuple(x, k.fr32[tm][tk], k.fi32[tm][tk]);
    }
  const float k1a = (float)(16 * id.w + id.l15) * (1.0f / 2048.0f), k1b = (float)(16 * id.w + 4 * id.g);
#pragma unroll
  for (int m = 0; m < 8; ++m) {
    const float x = (g8 + (float)m) * k1a;
    k.twc[m] = vcos(x) * 0.125f; k.tws[m] = vsin(x) * 0.125f;
    const float y = (k1b + (float)(m & 3)) * ((pl + (float)(4 * (m >> 2))) * (1.0f / 2048.0f));
    k.tic[m] = vcos(y) * 0.17677669529663687f; k.tis[m] = vsin(y) * 0.17677669529663687f;
  }
}
DI void pin_pc(PC& k) { pin(k.fr64); pin(k.fi64); pin(k.fr32[0]); pin(k.fr32[1]); pin(k.fi32[0]); pin(k.fi32[1]); pinf(k.twc); pinf(k.tws); pinf(k.tic); pinf(k.tis); }
template <int AUX> DI void p_load(const Ids& id, rsrc_t rw, int layer, int jj, v4u (&in)[6]) {
  const int s0 = 512 * id.w + 32 * id.l15 + 8 * id.g;
  const int xo = (s0 >> 6) * 57344 + (s0 & 63) * 2 + jj * 256;
  in[2] = ld128<AUX>(rw, s0 * 4, ST_OFF + 2 * layer * S * 4); in[3] = ld128<AUX>(rw, s0 * 4 + 16, ST_OFF + 2 * layer * S * 4);
  in[4] = ld128<AUX>(rw, s0 * 4, ST_OFF + (2 * layer + 1) * S * 4); in[5] = ld128<AUX>(rw, s0 * 4 + 16, ST_OFF + (2 * layer + 1) * S * 4);
  in[0] = ld128<AUX>(rw, xo, XH0_OFF + layer * XB); in[1] = ld128<AUX>(rw, xo + 128, XH0_OFF + layer * XB);
  __builtin_amdgcn_sched_barrier(0);
}
template <int AUX> DI void p_phase(const Ids& id, rsrc_t rw, int layer, int jj, lds_t L, const PC& k, const v4u (&wt)[2], const v4u (&in)[6]) {
  const int s0 = 512 * id.w + 32 * id.l15 + 8 * id.g;
  const int xo = (s0 >> 6) * 57344 + (s0 & 63) * 2 + jj * 256;
  const v4u xr = in[0], xi = in[1];
  const f4 s1a = asf4(in[2]), s1b = asf4(in[3]), s2a = asf4(in[4]), s2b = asf4(in[5]);
  float r[8];
#pragma unroll
  for (int e = 0; e < 8; ++e) {
    const float m = (e < 4 ? s1a[e & 3] : s1b[e & 3]) * (1.0f / 448.0f), q = (e < 4 ? s2a[e & 3] : s2b[e & 3]) * (1.0f / 448.0f);
    r[e] = __builtin_amdgcn_rsqf(__builtin_fmaxf(__builtin_fmaf(-m, m, q), 0.f) + EPS);
  }
  v4u zr, zi;
#pragma unroll
  for (int e = 0; e < 4; ++e) {
    zr[e] = pk_h(h_lo(xr[e]) * r[2 * e], h_hi(xr[e]) * r[2 * e + 1]);
    zi[e] = pk_h(h_lo(xi[e]) * r[2 * e], h_hi(xi[e]) * r[2 * e + 1]);
  }
  lds_t zp = L + L_A + (16 * id.w + id.l15) * 64 + id.g * 16;
  ldsw128(zp, zr); ldsw128(zp + 4096, zi);
  wgbar();
  f4 ar[2], ai[2];
  lds_t tp = L + L_A + (4 * id.g + (id.l15 >> 2)) * 64 + (id.l15 & 3) * 16;
#pragma unroll
  for (int t = 0; t < 2; ++t) {
    ar[t] = f4{0.f, 0.f, 0.f, 0.f}; ai[t] = ar[t];
#pragma unroll
    for (int ks = 0; ks < 4; ++ks) {
      const v4u a = cat(trread(tp + ks * 1024 + t * 8), trread(tp + 4096 + ks * 1024 + t * 8));
      ar[t] = mfma_h(a, k.fr64[ks], ar[t]); ai[t] = mfma_h(a, k.fi64[ks], ai[t]);
    }
  }
  v4u bq[2];
#pragma unroll
  for (int t = 0; t < 2; ++t) {
    float u[4], v[4];
#pragma unroll
    for (int e = 0; e < 4; ++e) {
      const float c = k.twc[4 * t + e], s = k.tws[4 * t + e];
      u[e] = ar[t][e] * c + ai[t][e] * s; v[e] = ai[t][e] * c - ar[t][e] * s;
    }
    bq[t] = v4u{pk_h(u[0], u[1]), pk_h(u[2], u[3]), pk_h(v[0], v[1]), pk_h(v[2], v[3])};
  }
  v4u yq[2];
#pragma unroll
  for (int tm = 0; tm < 2; ++tm) {
    f4 yr = {0.f, 0.f, 0.f, 0.f}, yi = yr;
#pragma unroll
    for (int tk = 0; tk < 2; ++tk) { yr = mfma_h(k.fr32[tm][tk], bq[tk], yr); yi = mfma_h(k.fi32[tm][tk], bq[tk], yi); }
    float u[4], v[4];
#pragma unroll
    for (int e = 0; e < 4; ++e) {
      const float wr = bf_lo(wt[tm][e]) * 0.17677669529663687f, wi = bf_hi(wt[tm][e]) * 0.17677669529663687f;
      u[e] = yr[e] * wr - yi[e] * wi; v[e] = -(yr[e] * wi + yi[e] * wr);
    }
    yq[tm] = v4u{pk_h(u[0], u[1]), pk_h(u[2], u[3]), pk_h(v[0], v[1]), pk_h(v[2], v[3])};
  }
#pragma unroll
  for (int t = 0; t < 2; ++t) {
    f4 br = {0.f, 0.f, 0.f, 0.f}, bi = br;
#pragma unroll
    for (int tk = 0; tk < 2; ++tk) { br = mfma_h(yq[tk], k.fr32[t][tk], br); bi = mfma_h(yq[tk], k.fi32[t][tk], bi); }
    float u[4], v[4];
#pragma unroll
    for (int e = 0; e < 4; ++e) {
      const float c = k.tic[4 * t + e], s = k.tis[4 * t + e];
      u[e] = br[e] * c + bi[e] * s; v[e] = bi[e] * c - br[e] * s;
    }
    ldsw128(L + L_A + 8192 + (2 * id.w + t) * 1024 + id.lane * 16, v4u{pk_h(u[0], u[1]), pk_h(u[2], u[3]), pk_h(v[0], v[1]), pk_h(v[2], v[3])});
  }
  wgbar();
  f4 or_[2], oi[2];
#pragma unroll
  for (int t = 0; t < 2; ++t) {
    or_[t] = f4{0.f, 0.f, 0.f, 0.f}; oi[t] = or_[t];
#pragma unroll
    for (int ks = 0; ks < 4; ++ks) {
      const v4u a = ldsr128(L + L_A + 8192 + (2 * ks + t) * 1024 + id.lane * 16);
      or_[t] = mfma_h(a, k.fr64[ks], or_[t]); oi[t] = mfma_h(a, k.fi64[ks], oi[t]);
    }
  }
  const float si = jj == 223 ? 0.f : -0.125f;
  v4u o0, o1;
#pragma unroll
  for (int e = 0; e < 4; ++e) {
    const int t = e >> 1, a = 2 * (e & 1);
    o0[e] = pk_h(__builtin_fmaf(or_[t][a], 0.125f, h_lo(xr[e])), __builtin_fmaf(or_[t][a + 1], 0.125f, h_hi(xr[e])));
    o1[e] = pk_h(__builtin_fmaf(oi[t][a], si, h_lo(xi[e])), __builtin_fmaf(oi[t][a + 1], si, h_hi(xi[e])));
  }
  st128<AUX>(o0, rw, xo, XH1_OFF + layer * XB); st128<AUX>(o1, rw, xo + 128, XH1_OFF + layer * XB);
}

DI void gen_ti(const Ids& id, int j, v4u (&T)[14]) {
  const float df = (float)(64 * j + 16 * id.w + id.l15) * (1.0f / 448.0f);
  const float o = (float)(16 * (id.w & 1));
  const float be = ((float)(2 * id.g + 1) + o) * df, bo = ((float)(2 * id.g + 1) - o) * df;
#pragma unroll
  for (int m = 0; m < 14; ++m) {
    unsigned u[4];
#pragma unroll
    for (int e = 0; e < 4; ++e) {
      const float x = __builtin_fmaf((float)(16 * m + 8 * (e >> 1) + (e & 1)), df, m & 1 ? bo : be);
      float c = vcos(x);
      if (m >= 12 && e == 3) c *= ((m ^ (id.w & 1)) == 13 && id.g == 3) ? 0.70710678118654752f : 1.0f;
      u[e] = pk_h(c, -vsin(x));
    }
    T[m] = v4u{u[0], u[1], u[2], u[3]};
  }
}
template <int AUX> DI void s3_issue(const Ids& id, rsrc_t rw, int layer, int i, lds_t L) {
  const int r7 = id.lane >> 3;
  const int vo = r7 * 128 + (((id.lane & 7) ^ (2 * ((r7 >> 1) & 3))) << 4);
#pragma unroll
  for (int n = 0; n < 14; ++n) dma16<AUX>(rw, L + L_A + (14 * id.w + n) * 1024, vo, XH1_OFF + layer * XB + i * 57344 + (14 * id.w + n) * 1024);
}
template <int AUX> DI void s3_compute(const Ids& id, rsrc_t rw, int layer, int i, int j, lds_t L, const v4u (&T)[14], f4 (&x1)[4]) {
  f4 mu[4];
#pragma unroll
  for (int n = 0; n < 4; ++n) mu[n] = asf4(ld128<AUX>(rw, (64 * i + 16 * (n ^ (id.w & 2)) + 4 * id.g) * 4, ST_OFF + 2 * layer * S * 4));
  const int q = id.l15 >> 2, p = id.l15 & 3, row = 4 * id.g + q, fr = (row >> 1) & 3;
  lds_t tb[4];
#pragma unroll
  for (int n = 0; n < 4; ++n) tb[n] = L + L_A + row * 128 + 32 * ((n ^ (id.w & 2)) ^ fr) + 8 * p;
  const int ko = (id.w & 1) * 4096;
  f4 acc[4];
#pragma unroll
  for (int n = 0; n < 4; ++n) acc[n] = f4{0.f, 0.f, 0.f, 0.f};
  float e0 = 0.f, e1 = 0.f;
#pragma unroll
  for (int m = 0; m < 14; ++m)
#pragma unroll
    for (int n = 0; n < 4; ++n) {
      lds_t a0 = tb[n] + (m & 1 ? -ko : ko) + m * 4096;
      const v4u a = cat(trread(a0), trread(a0 + 2048));
      acc[n] = mfma_h(a, T[m], acc[n]);
      if (n < 2 && !(m & 1)) {
#pragma unroll
        for (int e = 0; e < 4; ++e) {
          const unsigned ae = a[e]; const h2 v = __builtin_bit_cast(h2, ae);
          if (n == 0) e0 = __builtin_amdgcn_fdot2(v, v, e0, false); else e1 = __builtin_amdgcn_fdot2(v, v, e1, false);
        }
      }
    }
  e0 += xor16(e0); e1 += xor16(e1); e0 += xor32(e0); e1 += xor32(e1);
  LAS float* eb = (LAS float*)(L + L_E);
  if (id.g < 2) eb[64 * (id.w & 1) + 16 * ((id.w & 2) + id.g) + id.l15] = id.g ? e1 : e0;
  wgbar();
  const float v = (eb[id.lane] + eb[64 + id.lane]) * (1.0f / 448.0f);
  const float r1 = __builtin_amdgcn_rsqf(v + EPS);
  const float r = r1 * __builtin_amdgcn_rsqf(v * r1 * r1 + EPS);
  LAS float* rb = (LAS float*)(L + L_R) + 64 * id.w;
  rb[id.lane] = r;
  LGWAIT0();
  f4 h[4];
#pragma unroll
  for (int n = 0; n < 4; ++n) {
    const f4 rr = *(LAS f4*)(rb + 16 * (n ^ (id.w & 2)) + 4 * id.g);
    const f4 xc = acc[n] * 0.066815310478106f;
    h[n] = xc * rr; x1[n] = xc + mu[n] * (1.0f / 448.0f);
  }
#pragma unroll
  for (int qq = 0; qq < 2; ++qq) {
    const f4 a = h[2 * qq], b = h[2 * qq + 1];
    st128<AUX>(v4u{pk_bf(a[0], a[1]), pk_bf(a[2], a[3]), pk_bf(b[0], b[1]), pk_bf(b[2], b[3])}, rw, id.lane * 16,
               HF_OFF + layer * XB + ((j * 64 + 2 * i + (qq ^ (id.w >> 1))) * 4 + id.w) * 1024);
  }
}

struct WSrc { rsrc_t rs; int pitch, base; };
template <int K0, int K1> DI void w_issue(const Ids& id, const WSrc& ws, lds_t stage) {
  const int r7 = id.lane >> 3;
  const int vo = r7 * ws.pitch + (((id.lane & 7) ^ (2 * ((r7 >> 1) & 3))) << 4);
#pragma unroll
  for (int ks = K0; ks < K1; ++ks)
#pragma unroll
    for (int n = 0; n < 4; ++n) dma16<0>(ws.rs, stage + ((ks - K0) * 4 + n) * 1024, vo, ws.base + (32 * ks + 8 * n) * ws.pitch);
}
template <int K0, int K1> DI void w_read(const Ids& id, lds_t stage, v4u (&W)[16][4]) {
  const int q = id.l15 >> 2, p = id.l15 & 3, row = 4 * id.g + q, fr = (row >> 1) & 3;
#pragma unroll
  for (int n = 0; n < 4; ++n) {
    lds_t b = stage + row * 128 + 32 * ((n ^ (id.w & 2)) ^ fr) + 8 * p;
#pragma unroll
    for (int ks = K0; ks < K1; ++ks) W[ks][n] = cat(trread(b + (ks - K0) * 4096), trread(b + (ks - K0) * 4096 + 2048));
  }
}
template <int AUX, int NKS> DI void g_main(const Ids& id, rsrc_t rw, int abase, lds_t L, const v4u (&W)[16][4], f4 (&res)[4]) {
  constexpr int WIN = 24, NU = 4 * NKS;
  v4u a[WIN];
  const int vo = id.lane * 16;
  int sb[4];
#pragma unroll
  for (int n = 0; n < 4; ++n) sb[n] = abase + id.w * NKS * 4096 + ((((n + 1) & 3) ^ id.w) << 10);
#pragma unroll
  for (int u = 0; u < WIN; ++u) a[u] = ld128<AUX>(rw, vo, sb[u / NKS] + (u % NKS) * 4096);
  __builtin_amdgcn_sched_barrier(0);
  f4 part[4];
#pragma unroll
  for (int qn = 0; qn < 4; ++qn) {
    f4 acc[4];
#pragma unroll
    for (int ks = 0; ks < NKS; ++ks) {
      const int u = qn * NKS + ks;
      const v4u cur = a[u % WIN];
#pragma unroll
      for (int f = 0; f < 4; ++f) acc[f] = mfma_bf(W[ks][f], cur, ks == 0 ? f4{0.f, 0.f, 0.f, 0.f} : acc[f]);
      if (u + WIN < NU) a[u % WIN] = ld128<AUX>(rw, vo, sb[(u + WIN) / NKS] + ((u + WIN) % NKS) * 4096);
      if (qn == 3 && ks == 4) {
        wgbar();
#pragma unroll
        for (int f = 0; f < 4; ++f) {
          lds_t rp = L + L_A + (id.w * 12 + (f ^ (id.w & 2))) * 1024 + id.lane * 16;
          part[f] = (asf4(ldsr128(rp)) + asf4(ldsr128(rp + 4096))) + asf4(ldsr128(rp + 8192));
        }
      }
      __builtin_amdgcn_sched_barrier(0);
    }
    if (qn < 3) {
#pragma unroll
      for (int f = 0; f < 4; ++f) ldsw128(L + L_A + (((id.w ^ (qn + 1)) * 3 + qn) * 4 + (f ^ (id.w & 2))) * 1024 + id.lane * 16, asu4(acc[f]));
    } else {
#pragma unroll
      for (int f = 0; f < 4; ++f) res[f] = acc[f] + part[f];
    }
  }
}
DI void load_bias(const Ids& id, const unsigned short* b, int o, v2u (&bb)[4]) {
#pragma unroll
  for (int f = 0; f < 4; ++f) bb[f] = *(const GAS v2u*)(b + o + 16 * (f ^ (id.w & 2)) + 4 * id.g);
}
DI f4 bias4(v2u b) { return f4{bf_lo(b.x), bf_hi(b.x), bf_lo(b.y), bf_hi(b.y)}; }
DI float gelu(float u) {
  const float z = u * __builtin_fmaf(u * u, 0.044715f * -2.302208198144325f, -2.302208198144325f);
  return u * __builtin_amdgcn_rcpf(1.0f + __builtin_amdgcn_exp2f(z));
}
template <int AUX> DI void g1_epi(const Ids& id, rsrc_t rw, int layer, int j, int m, const f4 (&res)[4], const v2u (&bb)[4]) {
#pragma unroll
  for (int qq = 0; qq < 2; ++qq) {
    const f4 a = res[2 * qq] + bias4(bb[2 * qq]), b = res[2 * qq + 1] + bias4(bb[2 * qq + 1]);
    st128<AUX>(v4u{pk_bf(gelu(a[0]), gelu(a[1])), pk_bf(gelu(a[2]), gelu(a[3])), pk_bf(gelu(b[0]), gelu(b[1])), pk_bf(gelu(b[2]), gelu(b[3]))}, rw, id.lane * 16,
               GF_OFF + layer * GB + ((j * 56 + 2 * m + (qq ^ (id.w >> 1))) * 4 + id.w) * 1024);
  }
}
template <int AUX> DI void g2_epi(const Ids& id, rsrc_t ro, int obase, int i, int j, lds_t L, const f4 (&res)[4], const v2u (&bb)[4], const f4 (&x1)[4]) {
  lds_t T = L + L_A + 49152;
  {
    const int d = 16 * id.w + id.l15, hd = id.l15 ^ (id.w & 1) ^ ((id.w >> 1) << 2);
#pragma unroll
    for (int n = 0; n < 4; ++n) {
      const f4 v = res[n] + bias4(bb[n]) + x1[n];
      *(LAS v2u*)(T + d * 128 + (((4 * (n ^ (id.w & 2)) + id.g) ^ hd) << 3)) = v2u{pk_bf(v[0], v[1]), pk_bf(v[2], v[3])};
    }
  }
  wgbar();
  const int q = id.l15 >> 2, p = id.l15 & 3;
#pragma unroll
  for (int e = 0; e < 2; ++e) {
    const int sc = 4 * id.w + 2 * e + (id.g >> 1);
    v2u u[2];
#pragma unroll
    for (int hh = 0; hh < 2; ++hh) {
      const int d = 8 * (4 * (id.g & 1) + p) + 4 * hh + q;
      const int hd = (d & 15) ^ ((d >> 4) & 1) ^ (((d >> 5) & 1) << 2);
      u[hh] = trread(T + d * 128 + ((sc ^ hd) << 3));
    }
    const int row = 64 * i + 16 * id.w + 8 * e + 4 * (id.lane >> 5) + (id.lane & 3);
    st128<AUX>(cat(u[0], u[1]), ro, row * 896 + j * 128 + 16 * ((id.lane >> 2) & 7), obase);
  }
}

typedef GAS unsigned gu32;
constexpr int C_F0 = 0, C_P = 128, C_S3 = C_P + 2 * 128, C_G1 = C_S3 + 2 * 7 * 32, C_G2 = C_G1 + 2 * 7 * 32, C_LINES = C_G2 + 32 * 8;
static_assert(C_LINES * 128 <= CTRL_BYTES, "control block");
template <int NS, int NR> DI void seam_arrive(const Ids& id, unsigned char* ws, int line0, int shard) {
  VMWAIT(0);
  wgbar();
  if (id.w == 0 && id.lane < NR) __hip_atomic_fetch_add((gu32*)ws + 32 * (line0 + id.lane * NS + shard), 1u, __ATOMIC_RELAXED, __HIP_MEMORY_SCOPE_AGENT);
}
template <int NS> DI void seam_wait(const Ids& id, unsigned char* ws, int line0, int rep, unsigned want) {
  if (id.w == 0) {
    gu32* w = (gu32*)ws + 32 * (line0 + rep * NS + (id.lane & (NS - 1)));
    for (unsigned sp = 0; sp < (1u << 20); ++sp) {
      const unsigned v = __hip_atomic_load(w, __ATOMIC_RELAXED, __HIP_MEMORY_SCOPE_AGENT);
      if (__all(v >= want)) break;
    }
    __builtin_amdgcn_fence(__ATOMIC_ACQUIRE, "wavefront");
  }
  wgbar();
}

#define SMEM extern __shared__ __attribute__((aligned(16))) unsigned char smem[]; lds_t L = (lds_t)smem
__global__ void __launch_bounds__(256, 1) k_pre(Params p) {
  SMEM; const Ids id = get_ids(); const int b = blockIdx.x, q = b >> 3;
  const rsrc_t rw = mkrs(p.ws, WS_BYTES);
  STAMP(0);
  if (q < 28) {
    const int i = (b & 7) + 8 * (q / 7), j = q % 7;
    f0_issue<0>(id, mkrs(p.x, XB), 0, i, L);
    wt_transpose(p, b);
    v4u T[14]; gen_tf(id, j, T); pin(T);
    STAMP(1);
    VMWAIT(0); wgbar();
    STAMP(2);
    f0_compute<0>(id, rw, 0, i, j, L, T);
    STAMP(3);
  } else {
    wt_transpose(p, b);
    for (int n = (b - 224) * 256 + threadIdx.x; n < C_LINES; n += 32 * 256) ((unsigned*)p.ws)[32 * n] = 0u;
  }
}

__global__ void __launch_bounds__(256, 1) k_mega(Params p) {
  SMEM; const Ids id0 = get_ids(); const int b = blockIdx.x, q = b >> 3, x8 = b & 7;
  const rsrc_t rw = mkrs(p.ws, WS_BYTES);
  const int i = x8 + 8 * (q / 7), j = q % 7, jj = x8 * 28 + q;
  const bool g1 = i < 28;
#define id fresh(id0)
#define w1s (WSrc{mkrs(p.w1, S * F * 2), F * 2, fresh_s(512 * id0.w * F * 2 + i * 128)})
#define w2s (WSrc{mkrs(p.w2, S * F * 2), S * 2, fresh_s(448 * id0.w * S * 2 + i * 128)})
#define stage (L + L_W + fresh_s(id0.w) * (WSLOT * 4096))
  STAMP(4);
#pragma unroll 1
  for (int layer = 0; layer < 2; ++layer) {
    const int sb = layer * 30; (void)sb;
    v4u in[6], wt[2];
    if (layer == 1) {
      v4u T[14]; gen_tf(id, j, T); pin(T);
      STAMP(sb + 0);
      seam_wait<1>(id, p.ws, C_G2 + i * 8, j, 7u);
      STAMP(sb + 1);
      f0_issue<16>(id, rw, X2_OFF, i, L);
      VMWAIT(0); wgbar();
      STAMP(sb + 2);
      f0_compute<16>(id, rw, 1, i, j, L, T);
      STAMP(sb + 3);
      seam_arrive<16, 8>(id, p.ws, C_F0, b & 15);
      STAMP(sb + 4);
    } else p_load<16>(id, rw, 0, jj, in);
    wt[0] = ld128<0>(rw, threadIdx.x * 32, WT_OFF + jj * 8192); wt[1] = ld128<0>(rw, threadIdx.x * 32 + 16, WT_OFF + jj * 8192);
    {
      PC k; gen_pc(id, k); pin_pc(k);
      STAMP(sb + 5);
      if (layer == 1) { seam_wait<16>(id, p.ws, C_F0, x8, 14u); p_load<16>(id, rw, 1, jj, in); }
      STAMP(sb + 6);
      p_phase<16>(id, rw, layer, jj, L, k, wt, in);
    }
    STAMP(sb + 7);
    seam_arrive<16, 8>(id, p.ws, C_P + layer * 128, b & 15);
    STAMP(sb + 8);
    v4u W[16][4];
    f4 x1[4];
    {
      if (g1) w_issue<0, 6>(id, w1s, stage);
      v4u T[14]; gen_ti(id, j, T); pin(T);
      VMWAIT(0);
      w_read<0, 6>(id, stage, W);
      STAMP(sb + 9);
      seam_wait<16>(id, p.ws, C_P + layer * 128, x8, 14u);
      STAMP(sb + 10);
      s3_issue<16>(id, rw, layer, i, L);
      VMWAIT(0); wgbar();
      STAMP(sb + 11);
      if (g1) w_issue<6, 12>(id, w1s, stage);
      s3_compute<16>(id, rw, layer, i, j, L, T, x1);
      STAMP(sb + 12);
    }
    seam_arrive<4, 8>(id, p.ws, C_S3 + (layer * 7 + j) * 32, i & 3);
    STAMP(sb + 13);
    v2u bb[4];
    f4 res[4];
    if (g1) {
      w_read<6, 12>(id, stage, W); LGWAIT0();
      w_issue<12, 16>(id, w1s, stage);
      load_bias(id, p.b1, 64 * i, bb);
      VMWAIT(0);
      w_read<12, 16>(id, stage, W);
      STAMP(sb + 14);
      seam_wait<4>(id, p.ws, C_S3 + (layer * 7 + j) * 32, x8, 8u);
      STAMP(sb + 15);
      g_main<16, 16>(id, rw, HF_OFF + layer * XB + j * 64 * 4096, L, W, res);
      STAMP(sb + 16);
      w_issue<0, 6>(id, w2s, stage);
      g1_epi<16>(id, rw, layer, j, i, res, bb);
      STAMP(sb + 17);
      seam_arrive<4, 8>(id, p.ws, C_G1 + (layer * 7 + j) * 32, i & 3);
      STAMP(sb + 18);
    } else { w_issue<0, 6>(id, w2s, stage); VMWAIT(0); }
    w_read<0, 6>(id, stage, W); LGWAIT0();
    w_issue<6, 12>(id, w2s, stage);
    w_issue<12, 14>(id, w2s, L + L_A + fresh_s(id0.w) * 8192);
    load_bias(id, p.b2, 64 * i, bb);
    VMWAIT(0);
    w_read<6, 12>(id, stage, W); w_read<12, 14>(id, L + L_A + fresh_s(id0.w) * 8192, W);
    STAMP(sb + 19);
    seam_wait<4>(id, p.ws, C_G1 + (layer * 7 + j) * 32, x8, 7u);
    STAMP(sb + 20);
    g_main<16, 14>(id, rw, GF_OFF + layer * GB + j * 56 * 4096, L, W, res);
    STAMP(sb + 21);
    if (layer == 0) {
      g2_epi<16>(id, rw, X2_OFF, i, j, L, res, bb, x1);
      STAMP(sb + 22);
      seam_arrive<1, 7>(id, p.ws, C_G2 + i * 8, 0);
      STAMP(sb + 23);
    } else { g2_epi<0>(id, mkrs(p.out, XB), 0, i, j, L, res, bb, x1); STAMP(sb + 22); VMWAIT(0); STAMP(sb + 23); }
  }
}

#undef id
#undef stage
extern "C" void kernel_launch(void* const* d_in, const int* in_sizes, int n_in, void* d_out, int out_size, void* d_ws, size_t ws_size, hipStream_t stream) {
  Params p;
  p.x = (const unsigned short*)d_in[0]; p.wr = (const unsigned short*)d_in[1]; p.wi = (const unsigned short*)d_in[2];
  p.w1 = (const unsigned short*)d_in[4]; p.b1 = (const unsigned short*)d_in[5]; p.w2 = (const unsigned short*)d_in[6]; p.b2 = (const unsigned short*)d_in[7];
  p.out = (unsigned short*)d_out; p.ws = (unsigned char*)d_ws + ((size_t)230  __H0__ );
  (void)hipFuncSetAttribute((const void*)k_pre, hipFuncAttributeMaxDynamicSharedMemorySize, LDS_BYTES);
  (void)hipFuncSetAttribute((const void*)k_mega, hipFuncAttributeMaxDynamicSharedMemorySize, LDS_BYTES);
  k_pre<<<256, 256, LDS_BYTES, stream>>>(p);
  k_mega<<<224, 256, LDS_BYTES, stream>>>(p);
}
```

```cpp
#define DIAG 53
#define DIAG0 0
#define DIAGDIV 50
#define DIAGCLK 0
#include <hip/hip_runtime.h>
#include <hip/hip_bf16.h>
#include <stdint.h>

#define DI __device__ __forceinline__
#define GAS __attribute__((address_space(1)))
#define LAS __attribute__((address_space(3)))
typedef unsigned v4u __attribute__((ext_vector_type(4)));
typedef unsigned v2u __attribute__((ext_vector_type(2)));
typedef float f4 __attribute__((ext_vector_type(4)));
typedef float f2 __attribute__((ext_vector_type(2)));
typedef _Float16 h8 __attribute__((ext_vector_type(8)));
typedef _Float16 h2 __attribute__((ext_vector_type(2)));
typedef __bf16 b8 __attribute__((ext_vector_type(8)));
typedef __bf16 b2 __attribute__((ext_vector_type(2)));
typedef short s4 __attribute__((ext_vector_type(4)));
typedef __amdgpu_buffer_rsrc_t rsrc_t;
typedef LAS unsigned char* lds_t;

constexpr int S = 2048, D = 448, F = 1792, NKF = 225;
constexpr float EPS = 1e-5f;
constexpr int XB = S * D * 2;
constexpr int GB = D * F * 2;
constexpr int CTRL_OFF = 0, CTRL_BYTES = 262144;
constexpr int ST_OFF = CTRL_BYTES;
constexpr int WT_OFF = ST_OFF + 4 * S * 4;
constexpr int XH0_OFF = WT_OFF + XB;
constexpr int XH1_OFF = XH0_OFF + 2 * XB;
constexpr int HF_OFF = XH1_OFF + 2 * XB;
constexpr int GF_OFF = HF_OFF + 2 * XB;
constexpr int X2_OFF = GF_OFF + 2 * GB;
constexpr int WS_BYTES = X2_OFF + XB;
constexpr int STAMP_OFF = 64 << 20;
#define STAMP(n) do { if (threadIdx.x == 0 && blockIdx.x == 0 && ((n) == 0 || (n) == DIAG || (n) == DIAG0)) { ((unsigned long long*)(p.ws + STAMP_OFF))[(n) == 0 ? 0 : ((n) == DIAG ? 1 : 2)] = DIAGCLK ? __builtin_amdgcn_s_memtime() : __builtin_amdgcn_s_memrealtime(); } } while (0)
constexpr int L_A = 0, L_E = 57344, L_R = L_E + 512, L_W = 59392, WSLOT = 6, LDS_BYTES = L_W + 4 * WSLOT * 4096;

struct Params {
  const unsigned short* x; const unsigned short* wr; const unsigned short* wi;
  const unsigned short* w1; const unsigned short* b1; const unsigned short* w2; const unsigned short* b2;
  unsigned short* out; unsigned char* ws;
};

DI rsrc_t mkrs(const void* p, int bytes) { return __builtin_amdgcn_make_buffer_rsrc((void*)p, 0, bytes, 0x00020000); }
DI f4 mfma_bf(v4u a, v4u b, f4 c) { return __builtin_amdgcn_mfma_f32_16x16x32_bf16(__builtin_bit_cast(b8, a), __builtin_bit_cast(b8, b), c, 0, 0, 0); }
DI f4 mfma_h(v4u a, v4u b, f4 c) { return __builtin_amdgcn_mfma_f32_16x16x32_f16(__builtin_bit_cast(h8, a), __builtin_bit_cast(h8, b), c, 0, 0, 0); }
DI unsigned pk_bf(float a, float b) { f2 v = {a, b}; return __builtin_bit_cast(unsigned, __builtin_convertvector(v, b2)); }
DI unsigned pk_h(float a, float b) { f2 v = {a, b}; return __builtin_bit_cast(unsigned, __builtin_convertvector(v, h2)); }
DI float h_lo(unsigned u) { return (float)__builtin_bit_cast(h2, u)[0]; }
DI float h_hi(unsigned u) { return (float)__builtin_bit_cast(h2, u)[1]; }
DI float bf_lo(unsigned u) { return __builtin_bit_cast(float, u << 16); }
DI float bf_hi(unsigned u) { return __builtin_bit_cast(float, u & 0xffff0000u); }
DI float vcos(float x) { return __builtin_amdgcn_cosf(x); }
DI float vsin(float x) { return __builtin_amdgcn_sinf(x); }
DI v2u trread(lds_t p) { return __builtin_bit_cast(v2u, __builtin_amdgcn_ds_read_tr16_b64_v4i16((LAS s4*)p)); }
DI v4u cat(v2u a, v2u b) { return v4u{a.x, a.y, b.x, b.y}; }
DI v4u ldsr128(lds_t p) { return *(LAS v4u*)p; }
DI void ldsw128(lds_t p, v4u v) { *(LAS v4u*)p = v; }
template <int AUX> DI void dma16(rsrc_t rs, lds_t dst, int voff, int soff) {
  const unsigned la = (unsigned)(size_t)dst;
  if constexpr (AUX == 16) asm volatile("s_mov_b32 m0, %0\n\ts_nop 0\n\tbuffer_load_dwordx4 %1, %2, %3 offen sc1 lds" ::"s"(la), "v"(voff), "s"(rs), "s"(soff) : "memory");
  else asm volatile("s_mov_b32 m0, %0\n\ts_nop 0\n\tbuffer_load_dwordx4 %1, %2, %3 offen lds" ::"s"(la), "v"(voff), "s"(rs), "s"(soff) : "memory");
}
template <int N> DI void pin(v4u (&T)[N]) {
#pragma unroll
  for (int n = 0; n < N; ++n) asm volatile("" : "+v"(T[n]));
}
template <int N> DI void pinf(float (&T)[N]) {
#pragma unroll
  for (int n = 0; n < N; ++n) asm volatile("" : "+v"(T[n]));
}
template <int AUX> DI v4u ld128(rsrc_t rs, int voff, int soff = 0) { return __builtin_amdgcn_raw_buffer_load_b128(rs, voff, soff, AUX); }
template <int AUX> DI v2u ld64(rsrc_t rs, int voff, int soff = 0) { return __builtin_amdgcn_raw_buffer_load_b64(rs, voff, soff, AUX); }
template <int AUX> DI void st128(v4u v, rsrc_t rs, int voff, int soff = 0) { __builtin_amdgcn_raw_buffer_store_b128(v, rs, voff, soff, AUX); }
DI f4 asf4(v4u v) { return __builtin_bit_cast(f4, v); }
DI v4u asu4(f4 v) { return __builtin_bit_cast(v4u, v); }
#define VMWAIT(n) asm volatile("s_waitcnt vmcnt(" #n ")" ::: "memory")
#define LGWAIT0() asm volatile("s_waitcnt lgkmcnt(0)" ::: "memory")
DI void wgbar() { asm volatile("s_waitcnt lgkmcnt(0)" ::: "memory"); __builtin_amdgcn_s_barrier(); asm volatile("" ::: "memory"); }
DI float xor16(float v) { return __shfl_xor(v, 16); }
DI float xor32(float v) { return __shfl_xor(v, 32); }

struct Ids { int lane, w, l15, g; };
DI Ids get_ids() { Ids d; d.lane = threadIdx.x & 63; d.w = __builtin_amdgcn_readfirstlane(threadIdx.x >> 6); d.l15 = d.lane & 15; d.g = d.lane >> 4; return d; }

DI Ids fresh(const Ids& a) { Ids d; d.lane = a.lane; d.w = a.w; asm volatile("" : "+v"(d.lane), "+s"(d.w)); d.l15 = d.lane & 15; d.g = d.lane >> 4; return d; }
DI int fresh_s(int v) { asm volatile("" : "+s"(v)); return v; }

DI void gen_tf(const Ids& id, int j, v4u (&T)[14]) {
  const int c = 64 * j + 16 * id.w + id.l15;
  const bool dc = c == 447;
  const float kf = dc ? 0.f : (float)((c >> 1) + 1) * (1.0f / 448.0f);
  const float base = __builtin_fmaf((float)(8 * id.g), kf, (c & 1) && !dc ? 0.25f : 0.f);
#pragma unroll
  for (int ks = 0; ks < 14; ++ks) {
    unsigned u[4];
#pragma unroll
    for (int e = 0; e < 4; ++e)
      u[e] = pk_bf(vcos(__builtin_fmaf((float)(32 * ks + 2 * e), kf, base)), vcos(__builtin_fmaf((float)(32 * ks + 2 * e + 1), kf, base)));
    T[ks] = v4u{u[0], u[1], u[2], u[3]};
  }
}
template <int AUX> DI void f0_issue(const Ids& id, rsrc_t rx, int xbase, int i, lds_t L) {
  const int r7 = id.lane >> 3;
  const int v0 = r7 * 896, b = id.lane & 7;
#pragma unroll
  for (int n = 0; n < 14; ++n) {
    const int pc = 14 * id.w + n, a = pc >> 3, rb = pc & 7;
    const int xr = (rb & 3) * 2 + ((r7 >> 1) & 1);
    dma16<AUX>(rx, L + L_A + pc * 1024, v0 + ((b ^ xr) << 4), xbase + (64 * i + 8 * rb) * 896 + a * 128);
  }
}
template <int AUX> DI void f0_compute(const Ids& id, rsrc_t rw, int layer, int i, int j, lds_t L, const v4u (&T)[14]) {
  const int rho = id.l15, xr = (rho >> 1) & 7;
  const int rowb = (8 * (rho >> 2) + (rho & 3)) * 128;
  lds_t p0 = L + L_A + rowb + ((id.g ^ xr) << 4), p1 = L + L_A + rowb + (((id.g ^ xr) << 4) ^ 64);
  f4 acc[4];
#pragma unroll
  for (int t = 0; t < 4; ++t) acc[t] = f4{0.f, 0.f, 0.f, 0.f};
#pragma unroll
  for (int ks = 0; ks < 14; ++ks)
#pragma unroll
    for (int t = 0; t < 4; ++t) {
      const v4u a = ldsr128((ks & 1 ? p1 : p0) + (ks >> 1) * 8192 + (32 * (t >> 1) + 4 * (t & 1)) * 128);
      acc[t] = mfma_bf(a, T[ks], acc[t]);
    }
  const int c = 64 * j + 16 * id.w + id.l15;
  if (j == 5) {
    const int to = (32 * (id.w >> 1) + 4 * (id.w & 1)) * 128;
    float e0 = 0.f, e1 = 0.f;
#pragma unroll
    for (int ks = 0; ks < 14; ++ks) {
      const v4u a = ldsr128((ks & 1 ? p1 : p0) + (ks >> 1) * 8192 + to);
#pragma unroll
      for (int e = 0; e < 4; ++e) { const float lo = bf_lo(a[e]), hi = bf_hi(a[e]); e0 = __builtin_fmaf(lo, lo, e0); e1 = __builtin_fmaf(hi, hi, e1); }
    }
    float e = e0 + e1; e += xor16(e); e += xor32(e);
    const int sl = 32 * (id.w >> 1) + 4 * (id.w & 1) + 8 * (rho >> 2) + (rho & 3);
    if (id.g == 0) __builtin_amdgcn_raw_buffer_store_b32(__builtin_bit_cast(unsigned, e), rw, ST_OFF + (2 * layer + 1) * S * 4 + (64 * i + sl) * 4, 0, AUX);
  }
  if (c == 447) {
#pragma unroll
    for (int t = 0; t < 4; ++t) st128<AUX>(asu4(acc[t]), rw, ST_OFF + (2 * layer) * S * 4 + (64 * i + 32 * (t >> 1) + 8 * id.g + 4 * (t & 1)) * 4);
  }
  const float sc = c == 447 ? 0.f : (c == 446 ? 0.047245559126153f : 0.066815310478106f);
#pragma unroll
  for (int q = 0; q < 2; ++q) {
    const f4 a = acc[2 * q] * sc, b = acc[2 * q + 1] * sc;
    st128<AUX>(v4u{pk_h(a[0], a[1]), pk_h(a[2], a[3]), pk_h(b[0], b[1]), pk_h(b[2], b[3])}, rw, c * 128 + 64 * q + 16 * id.g, XH0_OFF + layer * XB + i * 57344);
  }
}
DI void wt_transpose(const Params& p, int b) {
  const int k1 = (b & 7) * 8 + ((b >> 3) & 7), g = b >> 6, t = threadIdx.x;
  if (t < 224) {
    unsigned u[8];
#pragma unroll
    for (int m = 0; m < 8; ++m) {
      const int o = (k1 + 512 * g + 64 * m) * NKF + t + 1;
      u[m] = (unsigned)p.wr[o] | ((unsigned)p.wi[o] << 16);
    }
    v4u* dst = (v4u*)(p.ws + WT_OFF + t * 8192 + (k1 >> 4) * 2048 + (g * 16 + (k1 & 15)) * 32);
    dst[0] = v4u{u[0], u[1], u[2], u[3]}; dst[1] = v4u{u[4], u[5], u[6], u[7]};
  }
}

struct PC { v4u fr64[4], fi64[4], fr32[2][2], fi32[2][2]; float twc[8], tws[8], tic[8], tis[8]; };
DI void cs_tuple(const float (&x)[4], v4u& re, v4u& im) {
  float c[4], s[4];
#pragma unroll
  for (int e = 0; e < 4; ++e) { c[e] = vcos(x[e]); s[e] = vsin(x[e]); }
  const unsigned c0 = pk_h(c[0], c[1]), c1 = pk_h(c[2], c[3]), s0 = pk_h(s[0], s[1]), s1 = pk_h(s[2], s[3]);
  re = v4u{c0, c1, s0, s1}; im = v4u{s0 ^ 0x80008000u, s1 ^ 0x80008000u, c0, c1};
}
DI void gen_pc(const Ids& id, PC& k) {
  const float a64 = (float)(16 * id.w + id.l15) * (1.0f / 64.0f), g4 = (float)(4 * id.g);
#pragma unroll
  for (int ks = 0; ks < 4; ++ks) {
    float x[4];
#pragma unroll
    for (int e = 0; e < 4; ++e) x[e] = (g4 + (float)(16 * ks + e)) * a64;
    cs_tuple(x, k.fr64[ks], k.fi64[ks]);
  }
  const float pl = (float)(8 * (id.l15 >> 2) + (id.l15 & 3)), g8 = (float)(8 * id.g);
#pragma unroll
  for (int tm = 0; tm < 2; ++tm)
#pragma unroll
    for (int tk = 0; tk < 2; ++tk) {
      float x[4];
#pragma unroll
      for (int e = 0; e < 4; ++e) x[e] = (g8 + (float)(4 * tk + e)) * ((pl + (float)(4 * tm)) * (1.0f / 32.0f));
      cs_tuple(x, k.fr32[tm][tk], k.fi32[tm][tk]);
    }
  const float k1a = (float)(16 * id.w + id.l15) * (1.0f / 2048.0f), k1b = (float)(16 * id.w + 4 * id.g);
#pragma unroll
  for (int m = 0; m < 8; ++m) {
    const float x = (g8 + (float)m) * k1a;
    k.twc[m] = vcos(x) * 0.125f; k.tws[m] = vsin(x) * 0.125f;
    const float y = (k1b + (float)(m & 3)) * ((pl + (float)(4 * (m >> 2))) * (1.0f / 2048.0f));
    k.tic[m] = vcos(y) * 0.17677669529663687f; k.tis[m] = vsin(y) * 0.17677669529663687f;
  }
}
DI void pin_pc(PC& k) { pin(k.fr64); pin(k.fi64); pin(k.fr32[0]); pin(k.fr32[1]); pin(k.fi32[0]); pin(k.fi32[1]); pinf(k.twc); pinf(k.tws); pinf(k.tic); pinf(k.tis); }
template <int AUX> DI void p_load(const Ids& id, rsrc_t rw, int layer, int jj, v4u (&in)[6]) {
  const int s0 = 512 * id.w + 32 * id.l15 + 8 * id.g;
  const int xo = (s0 >> 6) * 57344 + (s0 & 63) * 2 + jj * 256;
  in[2] = ld128<AUX>(rw, s0 * 4, ST_OFF + 2 * layer * S * 4); in[3] = ld128<AUX>(rw, s0 * 4 + 16, ST_OFF + 2 * layer * S * 4);
  in[4] = ld128<AUX>(rw, s0 * 4, ST_OFF + (2 * layer + 1) * S * 4); in[5] = ld128<AUX>(rw, s0 * 4 + 16, ST_OFF + (2 * layer + 1) * S * 4);
  in[0] = ld128<AUX>(rw, xo, XH0_OFF + layer * XB); in[1] = ld128<AUX>(rw, xo + 128, XH0_OFF + layer * XB);
  __builtin_amdgcn_sched_barrier(0);
}
template <int AUX> DI void p_phase(const Ids& id, rsrc_t rw, int layer, int jj, lds_t L, const PC& k, const v4u (&wt)[2], const v4u (&in)[6]) {
  const int s0 = 512 * id.w + 32 * id.l15 + 8 * id.g;
  const int xo = (s0 >> 6) * 57344 + (s0 & 63) * 2 + jj * 256;
  const v4u xr = in[0], xi = in[1];
  const f4 s1a = asf4(in[2]), s1b = asf4(in[3]), s2a = asf4(in[4]), s2b = asf4(in[5]);
  float r[8];
#pragma unroll
  for (int e = 0; e < 8; ++e) {
    const float m = (e < 4 ? s1a[e & 3] : s1b[e & 3]) * (1.0f / 448.0f), q = (e < 4 ? s2a[e & 3] : s2b[e & 3]) * (1.0f / 448.0f);
    r[e] = __builtin_amdgcn_rsqf(__builtin_fmaxf(__builtin_fmaf(-m, m, q), 0.f) + EPS);
  }
  v4u zr, zi;
#pragma unroll
  for (int e = 0; e < 4; ++e) {
    zr[e] = pk_h(h_lo(xr[e]) * r[2 * e], h_hi(xr[e]) * r[2 * e + 1]);
    zi[e] = pk_h(h_lo(xi[e]) * r[2 * e], h_hi(xi[e]) * r[2 * e + 1]);
  }
  lds_t zp = L + L_A + (16 * id.w + id.l15) * 64 + id.g * 16;
  ldsw128(zp, zr); ldsw128(zp + 4096, zi);
  wgbar();
  f4 ar[2], ai[2];
  lds_t tp = L + L_A + (4 * id.g + (id.l15 >> 2)) * 64 + (id.l15 & 3) * 16;
#pragma unroll
  for (int t = 0; t < 2; ++t) {
    ar[t] = f4{0.f, 0.f, 0.f, 0.f}; ai[t] = ar[t];
#pragma unroll
    for (int ks = 0; ks < 4; ++ks) {
      const v4u a = cat(trread(tp + ks * 1024 + t * 8), trread(tp + 4096 + ks * 1024 + t * 8));
      ar[t] = mfma_h(a, k.fr64[ks], ar[t]); ai[t] = mfma_h(a, k.fi64[ks], ai[t]);
    }
  }
  v4u bq[2];
#pragma unroll
  for (int t = 0; t < 2; ++t) {
    float u[4], v[4];
#pragma unroll
    for (int e = 0; e < 4; ++e) {
      const float c = k.twc[4 * t + e], s = k.tws[4 * t + e];
      u[e] = ar[t][e] * c + ai[t][e] * s; v[e] = ai[t][e] * c - ar[t][e] * s;
    }
    bq[t] = v4u{pk_h(u[0], u[1]), pk_h(u[2], u[3]), pk_h(v[0], v[1]), pk_h(v[2], v[3])};
  }
  v4u yq[2];
#pragma unroll
  for (int tm = 0; tm < 2; ++tm) {
    f4 yr = {0.f, 0.f, 0.f, 0.f}, yi = yr;
#pragma unroll
    for (int tk = 0; tk < 2; ++tk) { yr = mfma_h(k.fr32[tm][tk], bq[tk], yr); yi = mfma_h(k.fi32[tm][tk], bq[tk], yi); }
    float u[4], v[4];
#pragma unroll
    for (int e = 0; e < 4; ++e) {
      const float wr = bf_lo(wt[tm][e]) * 0.17677669529663687f, wi = bf_hi(wt[tm][e]) * 0.17677669529663687f;
      u[e] = yr[e] * wr - yi[e] * wi; v[e] = -(yr[e] * wi + yi[e] * wr);
    }
    yq[tm] = v4u{pk_h(u[0], u[1]), pk_h(u[2], u[3]), pk_h(v[0], v[1]), pk_h(v[2], v[3])};
  }
#pragma unroll
  for (int t = 0; t < 2; ++t) {
    f4 br = {0.f, 0.f, 0.f, 0.f}, bi = br;
#pragma unroll
    for (int tk = 0; tk < 2; ++tk) { br = mfma_h(yq[tk], k.fr32[t][tk], br); bi = mfma_h(yq[tk], k.fi32[t][tk], bi); }
    float u[4], v[4];
#pragma unroll
    for (int e = 0; e < 4; ++e) {
      const float c = k.tic[4 * t + e], s = k.tis[4 * t + e];
      u[e] = br[e] * c + bi[e] * s; v[e] = bi[e] * c - br[e] * s;
    }
    ldsw128(L + L_A + 8192 + (2 * id.w + t) * 1024 + id.lane * 16, v4u{pk_h(u[0], u[1]), pk_h(u[2], u[3]), pk_h(v[0], v[1]), pk_h(v[2], v[3])});
  }
  wgbar();
  f4 or_[2], oi[2];
#pragma unroll
  for (int t = 0; t < 2; ++t) {
    or_[t] = f4{0.f, 0.f, 0.f, 0.f}; oi[t] = or_[t];
#pragma unroll
    for (int ks = 0; ks < 4; ++ks) {
      const v4u a = ldsr128(L + L_A + 8192 + (2 * ks + t) * 1024 + id.lane * 16);
      or_[t] = mfma_h(a, k.fr64[ks], or_[t]); oi[t] = mfma_h(a, k.fi64[ks], oi[t]);
    }
  }
  const float si = jj == 223 ? 0.f : -0.125f;
  v4u o0, o1;
#pragma unroll
  for (int e = 0; e < 4; ++e) {
    const int t = e >> 1, a = 2 * (e & 1);
    o0[e] = pk_h(__builtin_fmaf(or_[t][a], 0.125f, h_lo(xr[e])), __builtin_fmaf(or_[t][a + 1], 0.125f, h_hi(xr[e])));
    o1[e] = pk_h(__builtin_fmaf(oi[t][a], si, h_lo(xi[e])), __builtin_fmaf(oi[t][a + 1], si, h_hi(xi[e])));
  }
  st128<AUX>(o0, rw, xo, XH1_OFF + layer * XB); st128<AUX>(o1, rw, xo + 128, XH1_OFF + layer * XB);
}

DI void gen_ti(const Ids& id, int j, v4u (&T)[14]) {
  const float df = (float)(64 * j + 16 * id.w + id.l15) * (1.0f / 448.0f);
  const float o = (float)(16 * (id.w & 1));
  const float be = ((float)(2 * id.g + 1) + o) * df, bo = ((float)(2 * id.g + 1) - o) * df;
#pragma unroll
  for (int m = 0; m < 14; ++m) {
    unsigned u[4];
#pragma unroll
    for (int e = 0; e < 4; ++e) {
      const float x = __builtin_fmaf((float)(16 * m + 8 * (e >> 1) + (e & 1)), df, m & 1 ? bo : be);
      float c = vcos(x);
      if (m >= 12 && e == 3) c *= ((m ^ (id.w & 1)) == 13 && id.g == 3) ? 0.70710678118654752f : 1.0f;
      u[e] = pk_h(c, -vsin(x));
    }
    T[m] = v4u{u[0], u[1], u[2], u[3]};
  }
}
template <int AUX> DI void s3_issue(const Ids& id, rsrc_t rw, int layer, int i, lds_t L) {
  const int r7 = id.lane >> 3;
  const int vo = r7 * 128 + (((id.lane & 7) ^ (2 * ((r7 >> 1) & 3))) << 4);
#pragma unroll
  for (int n = 0; n < 14; ++n) dma16<AUX>(rw, L + L_A + (14 * id.w + n) * 1024, vo, XH1_OFF + layer * XB + i * 57344 + (14 * id.w + n) * 1024);
}
template <int AUX> DI void s3_compute(const Ids& id, rsrc_t rw, int layer, int i, int j, lds_t L, const v4u (&T)[14], f4 (&x1)[4]) {
  f4 mu[4];
#pragma unroll
  for (int n = 0; n < 4; ++n) mu[n] = asf4(ld128<AUX>(rw, (64 * i + 16 * (n ^ (id.w & 2)) + 4 * id.g) * 4, ST_OFF + 2 * layer * S * 4));
  const int q = id.l15 >> 2, p = id.l15 & 3, row = 4 * id.g + q, fr = (row >> 1) & 3;
  lds_t tb[4];
#pragma unroll
  for (int n = 0; n < 4; ++n) tb[n] = L + L_A + row * 128 + 32 * ((n ^ (id.w & 2)) ^ fr) + 8 * p;
  const int ko = (id.w & 1) * 4096;
  f4 acc[4];
#pragma unroll
  for (int n = 0; n < 4; ++n) acc[n] = f4{0.f, 0.f, 0.f, 0.f};
  float e0 = 0.f, e1 = 0.f;
#pragma unroll
  for (int m = 0; m < 14; ++m)
#pragma unroll
    for (int n = 0; n < 4; ++n) {
      lds_t a0 = tb[n] + (m & 1 ? -ko : ko) + m * 4096;
      const v4u a = cat(trread(a0), trread(a0 + 2048));
      acc[n] = mfma_h(a, T[m], acc[n]);
      if (n < 2 && !(m & 1)) {
#pragma unroll
        for (int e = 0; e < 4; ++e) {
          const unsigned ae = a[e]; const h2 v = __builtin_bit_cast(h2, ae);
          if (n == 0) e0 = __builtin_amdgcn_fdot2(v, v, e0, false); else e1 = __builtin_amdgcn_fdot2(v, v, e1, false);
        }
      }
    }
  e0 += xor16(e0); e1 += xor16(e1); e0 += xor32(e0); e1 += xor32(e1);
  LAS float* eb = (LAS float*)(L + L_E);
  if (id.g < 2) eb[64 * (id.w & 1) + 16 * ((id.w & 2) + id.g) + id.l15] = id.g ? e1 : e0;
  wgbar();
  const float v = (eb[id.lane] + eb[64 + id.lane]) * (1.0f / 448.0f);
  const float r1 = __builtin_amdgcn_rsqf(v + EPS);
  const float r = r1 * __builtin_amdgcn_rsqf(v * r1 * r1 + EPS);
  LAS float* rb = (LAS float*)(L + L_R) + 64 * id.w;
  rb[id.lane] = r;
  LGWAIT0();
  f4 h[4];
#pragma unroll
  for (int n = 0; n < 4; ++n) {
    const f4 rr = *(LAS f4*)(rb + 16 * (n ^ (id.w & 2)) + 4 * id.g);
    const f4 xc = acc[n] * 0.066815310478106f;
    h[n] = xc * rr; x1[n] = xc + mu[n] * (1.0f / 448.0f);
  }
#pragma unroll
  for (int qq = 0; qq < 2; ++qq) {
    const f4 a = h[2 * qq], b = h[2 * qq + 1];
    st128<AUX>(v4u{pk_bf(a[0], a[1]), pk_bf(a[2], a[3]), pk_bf(b[0], b[1]), pk_bf(b[2], b[3])}, rw, id.lane * 16,
               HF_OFF + layer * XB + ((j * 64 + 2 * i + (qq ^ (id.w >> 1))) * 4 + id.w) * 1024);
  }
}

struct WSrc { rsrc_t rs; int pitch, base; };
template <int K0, int K1> DI void w_issue(const Ids& id, const WSrc& ws, lds_t stage) {
  const int r7 = id.lane >> 3;
  const int vo = r7 * ws.pitch + (((id.lane & 7) ^ (2 * ((r7 >> 1) & 3))) << 4);
#pragma unroll
  for (int ks = K0; ks < K1; ++ks)
#pragma unroll
    for (int n = 0; n < 4; ++n) dma16<0>(ws.rs, stage + ((ks - K0) * 4 + n) * 1024, vo, ws.base + (32 * ks + 8 * n) * ws.pitch);
}
template <int K0, int K1> DI void w_read(const Ids& id, lds_t stage, v4u (&W)[16][4]) {
  const int q = id.l15 >> 2, p = id.l15 & 3, row = 4 * id.g + q, fr = (row >> 1) & 3;
#pragma unroll
  for (int n = 0; n < 4; ++n) {
    lds_t b = stage + row * 128 + 32 * ((n ^ (id.w & 2)) ^ fr) + 8 * p;
#pragma unroll
    for (int ks = K0; ks < K1; ++ks) W[ks][n] = cat(trread(b + (ks - K0) * 4096), trread(b + (ks - K0) * 4096 + 2048));
  }
}
template <int AUX, int NKS> DI void g_main(const Ids& id, rsrc_t rw, int abase, lds_t L, const v4u (&W)[16][4], f4 (&res)[4]) {
  constexpr int WIN = 24, NU = 4 * NKS;
  v4u a[WIN];
  const int vo = id.lane * 16;
  int sb[4];
#pragma unroll
  for (int n = 0; n < 4; ++n) sb[n] = abase + id.w * NKS * 4096 + ((((n + 1) & 3) ^ id.w) << 10);
#pragma unroll
  for (int u = 0; u < WIN; ++u) a[u] = ld128<AUX>(rw, vo, sb[u / NKS] + (u % NKS) * 4096);
  __builtin_amdgcn_sched_barrier(0);
  f4 part[4];
#pragma unroll
  for (int qn = 0; qn < 4; ++qn) {
    f4 acc[4];
#pragma unroll
    for (int ks = 0; ks < NKS; ++ks) {
      const int u = qn * NKS + ks;
      const v4u cur = a[u % WIN];
#pragma unroll
      for (int f = 0; f < 4; ++f) acc[f] = mfma_bf(W[ks][f], cur, ks == 0 ? f4{0.f, 0.f, 0.f, 0.f} : acc[f]);
      if (u + WIN < NU) a[u % WIN] = ld128<AUX>(rw, vo, sb[(u + WIN) / NKS] + ((u + WIN) % NKS) * 4096);
      if (qn == 3 && ks == 4) {
        wgbar();
#pragma unroll
        for (int f = 0; f < 4; ++f) {
          lds_t rp = L + L_A + (id.w * 12 + (f ^ (id.w & 2))) * 1024 + id.lane * 16;
          part[f] = (asf4(ldsr128(rp)) + asf4(ldsr128(rp + 4096))) + asf4(ldsr128(rp + 8192));
        }
      }
      __builtin_amdgcn_sched_barrier(0);
    }
    if (qn < 3) {
#pragma unroll
      for (int f = 0; f < 4; ++f) ldsw128(L + L_A + (((id.w ^ (qn + 1)) * 3 + qn) * 4 + (f ^ (id.w & 2))) * 1024 + id.lane * 16, asu4(acc[f]));
    } else {
#pragma unroll
      for (int f = 0; f < 4; ++f) res[f] = acc[f] + part[f];
    }
  }
}
DI void load_bias(const Ids& id, const unsigned short* b, int o, v2u (&bb)[4]) {
#pragma unroll
  for (int f = 0; f < 4; ++f) bb[f] = *(const GAS v2u*)(b + o + 16 * (f ^ (id.w & 2)) + 4 * id.g);
}
DI f4 bias4(v2u b) { return f4{bf_lo(b.x), bf_hi(b.x), bf_lo(b.y), bf_hi(b.y)}; }
DI float gelu(float u) {
  const float z = u * __builtin_fmaf(u * u, 0.044715f * -2.302208198144325f, -2.302208198144325f);
  return u * __builtin_amdgcn_rcpf(1.0f + __builtin_amdgcn_exp2f(z));
}
template <int AUX> DI void g1_epi(const Ids& id, rsrc_t rw, int layer, int j, int m, const f4 (&res)[4], const v2u (&bb)[4]) {
#pragma unroll
  for (int qq = 0; qq < 2; ++qq) {
    const f4 a = res[2 * qq] + bias4(bb[2 * qq]), b = res[2 * qq + 1] + bias4(bb[2 * qq + 1]);
    st128<AUX>(v4u{pk_bf(gelu(a[0]), gelu(a[1])), pk_bf(gelu(a[2]), gelu(a[3])), pk_bf(gelu(b[0]), gelu(b[1])), pk_bf(gelu(b[2]), gelu(b[3]))}, rw, id.lane * 16,
               GF_OFF + layer * GB + ((j * 56 + 2 * m + (qq ^ (id.w >> 1))) * 4 + id.w) * 1024);
  }
}
template <int AUX> DI void g2_epi(const Ids& id, rsrc_t ro, int obase, int i, int j, lds_t L, const f4 (&res)[4], const v2u (&bb)[4], const f4 (&x1)[4]) {
  lds_t T = L + L_A + 49152;
  {
    const int d = 16 * id.w + id.l15, hd = id.l15 ^ (id.w & 1) ^ ((id.w >> 1) << 2);
#pragma unroll
    for (int n = 0; n < 4; ++n) {
      const f4 v = res[n] + bias4(bb[n]) + x1[n];
      *(LAS v2u*)(T + d * 128 + (((4 * (n ^ (id.w & 2)) + id.g) ^ hd) << 3)) = v2u{pk_bf(v[0], v[1]), pk_bf(v[2], v[3])};
    }
  }
  wgbar();
  const int q = id.l15 >> 2, p = id.l15 & 3;
#pragma unroll
  for (int e = 0; e < 2; ++e) {
    const int sc = 4 * id.w + 2 * e + (id.g >> 1);
    v2u u[2];
#pragma unroll
    for (int hh = 0; hh < 2; ++hh) {
      const int d = 8 * (4 * (id.g & 1) + p) + 4 * hh + q;
      const int hd = (d & 15) ^ ((d >> 4) & 1) ^ (((d >> 5) & 1) << 2);
      u[hh] = trread(T + d * 128 + ((sc ^ hd) << 3));
    }
    const int row = 64 * i + 16 * id.w + 8 * e + 4 * (id.lane >> 5) + (id.lane & 3);
    st128<AUX>(cat(u[0], u[1]), ro, row * 896 + j * 128 + 16 * ((id.lane >> 2) & 7), obase);
  }
}

typedef GAS unsigned gu32;
constexpr int C_F0 = 0, C_P = 128, C_S3 = C_P + 2 * 128, C_G1 = C_S3 + 2 * 7 * 32, C_G2 = C_G1 + 2 * 7 * 32, C_LINES = C_G2 + 32 * 8;
static_assert(C_LINES * 128 <= CTRL_BYTES, "control block");
template <int NS, int NR> DI void seam_arrive(const Ids& id, unsigned char* ws, int line0, int shard) {
  VMWAIT(0);
  wgbar();
  if (id.w == 0 && id.lane < NR) __hip_atomic_fetch_add((gu32*)ws + 32 * (line0 + id.lane * NS + shard), 1u, __ATOMIC_RELAXED, __HIP_MEMORY_SCOPE_AGENT);
}
template <int NS> DI void seam_wait(const Ids& id, unsigned char* ws, int line0, int rep, unsigned want) {
  if (id.w == 0) {
    gu32* w = (gu32*)ws + 32 * (line0 + rep * NS + (id.lane & (NS - 1)));
    for (unsigned sp = 0; sp < (1u << 20); ++sp) {
      const unsigned v = __hip_atomic_load(w, __ATOMIC_RELAXED, __HIP_MEMORY_SCOPE_AGENT);
      if (__all(v >= want)) break;
    }
    __builtin_amdgcn_fence(__ATOMIC_ACQUIRE, "wavefront");
  }
  wgbar();
}

#define SMEM extern __shared__ __attribute__((aligned(16))) unsigned char smem[]; lds_t L = (lds_t)smem
__global__ void __launch_bounds__(256, 1) k_pre(Params p) {
  SMEM; const Ids id = get_ids(); const int b = blockIdx.x, q = b >> 3;
  const rsrc_t rw = mkrs(p.ws, WS_BYTES);
  STAMP(0);
  if (q < 28) {
    const int i = (b & 7) + 8 * (q / 7), j = q % 7;
    f0_issue<0>(id, mkrs(p.x, XB), 0, i, L);
    wt_transpose(p, b);
    v4u T[14]; gen_tf(id, j, T); pin(T);
    STAMP(1);
    VMWAIT(0); wgbar();
    STAMP(2);
    f0_compute<0>(id, rw, 0, i, j, L, T);
    STAMP(3);
  } else {
    wt_transpose(p, b);
    for (int n = (b - 224) * 256 + threadIdx.x; n < C_LINES; n += 32 * 256) ((unsigned*)p.ws)[32 * n] = 0u;
  }
}

__global__ void __launch_bounds__(256, 1) k_mega(Params p) {
  SMEM; const Ids id0 = get_ids(); const int b = blockIdx.x, q = b >> 3, x8 = b & 7;
  const rsrc_t rw = mkrs(p.ws, WS_BYTES);
  const int i = x8 + 8 * (q / 7), j = q % 7, jj = x8 * 28 + q;
  const bool g1 = i < 28;
#define id fresh(id0)
#define w1s (WSrc{mkrs(p.w1, S * F * 2), F * 2, fresh_s(512 * id0.w * F * 2 + i * 128)})
#define w2s (WSrc{mkrs(p.w2, S * F * 2), S * 2, fresh_s(448 * id0.w * S * 2 + i * 128)})
#define stage (L + L_W + fresh_s(id0.w) * (WSLOT * 4096))
  STAMP(4);
#pragma unroll 1
  for (int layer = 0; layer < 2; ++layer) {
    const int sb = layer * 30; (void)sb;
    v4u in[6], wt[2];
    if (layer == 1) {
      v4u T[14]; gen_tf(id, j, T); pin(T);
      STAMP(sb + 0);
      seam_wait<1>(id, p.ws, C_G2 + i * 8, j, 7u);
      STAMP(sb + 1);
      f0_issue<16>(id, rw, X2_OFF, i, L);
      VMWAIT(0); wgbar();
      STAMP(sb + 2);
      f0_compute<16>(id, rw, 1, i, j, L, T);
      STAMP(sb + 3);
      seam_arrive<16, 8>(id, p.ws, C_F0, b & 15);
      STAMP(sb + 4);
    } else p_load<16>(id, rw, 0, jj, in);
    wt[0] = ld128<0>(rw, threadIdx.x * 32, WT_OFF + jj * 8192); wt[1] = ld128<0>(rw, threadIdx.x * 32 + 16, WT_OFF + jj * 8192);
    {
      PC k; gen_pc(id, k); pin_pc(k);
      STAMP(sb + 5);
      if (layer == 1) { seam_wait<16>(id, p.ws, C_F0, x8, 14u); p_load<16>(id, rw, 1, jj, in); }
      STAMP(sb + 6);
      p_phase<16>(id, rw, layer, jj, L, k, wt, in);
    }
    STAMP(sb + 7);
    seam_arrive<16, 8>(id, p.ws, C_P + layer * 128, b & 15);
    STAMP(sb + 8);
    v4u W[16][4];
    f4 x1[4];
    {
      if (g1) w_issue<0, 6>(id, w1s, stage);
      v4u T[14]; gen_ti(id, j, T); pin(T);
      VMWAIT(0);
      w_read<0, 6>(id, stage, W);
      STAMP(sb + 9);
      seam_wait<16>(id, p.ws, C_P + layer * 128, x8, 14u);
      STAMP(sb + 10);
      s3_issue<16>(id, rw, layer, i, L);
      VMWAIT(0); wgbar();
      STAMP(sb + 11);
      if (g1) w_issue<6, 12>(id, w1s, stage);
      s3_compute<16>(id, rw, layer, i, j, L, T, x1);
      STAMP(sb + 12);
    }
    seam_arrive<4, 8>(id, p.ws, C_S3 + (layer * 7 + j) * 32, i & 3);
    STAMP(sb + 13);
    v2u bb[4];
    f4 res[4];
    if (g1) {
      w_read<6, 12>(id, stage, W); LGWAIT0();
      w_issue<12, 16>(id, w1s, stage);
      load_bias(id, p.b1, 64 * i, bb);
      VMWAIT(0);
      w_read<12, 16>(id, stage, W);
      STAMP(sb + 14);
      seam_wait<4>(id, p.ws, C_S3 + (layer * 7 + j) * 32, x8, 8u);
      STAMP(sb + 15);
      g_main<16, 16>(id, rw, HF_OFF + layer * XB + j * 64 * 4096, L, W, res);
      STAMP(sb + 16);
      w_issue<0, 6>(id, w2s, stage);
      g1_epi<16>(id, rw, layer, j, i, res, bb);
      STAMP(sb + 17);
      seam_arrive<4, 8>(id, p.ws, C_G1 + (layer * 7 + j) * 32, i & 3);
      STAMP(sb + 18);
    } else { w_issue<0, 6>(id, w2s, stage); VMWAIT(0); }
    w_read<0, 6>(id, stage, W); LGWAIT0();
    w_issue<6, 12>(id, w2s, stage);
    w_issue<12, 14>(id, w2s, L + L_A + fresh_s(id0.w) * 8192);
    load_bias(id, p.b2, 64 * i, bb);
    VMWAIT(0);
    w_read<6, 12>(id, stage, W); w_read<12, 14>(id, L + L_A + fresh_s(id0.w) * 8192, W);
    STAMP(sb + 19);
    seam_wait<4>(id, p.ws, C_G1 + (layer * 7 + j) * 32, x8, 7u);
    STAMP(sb + 20);
    g_main<16, 14>(id, rw, GF_OFF + layer * GB + j * 56 * 4096, L, W, res);
    STAMP(sb + 21);
    if (layer == 0) {
      g2_epi<16>(id, rw, X2_OFF, i, j, L, res, bb, x1);
      STAMP(sb + 22);
      seam_arrive<1, 7>(id, p.ws, C_G2 + i * 8, 0);
      STAMP(sb + 23);
    } else { g2_epi<0>(id, mkrs(p.out, XB), 0, i, j, L, res, bb, x1); STAMP(sb + 22); VMWAIT(0); STAMP(sb + 23); }
  }
  VMWAIT(0); wgbar();
  if (threadIdx.x == 0 && blockIdx.x == 0) {
    volatile GAS unsigned long long* q = (volatile GAS unsigned long long*)(p.ws + STAMP_OFF);
    const unsigned long long dt = q[1] - q[DIAG0 == 0 ? 0 : 2];
    const float v = 16.0f * (float)(unsigned)((dt + DIAGDIV / 2) / DIAGDIV);
    if (dt >= 7000ull) p.out[0] = (unsigned short)(__builtin_bit_cast(unsigned, v) >> 16);
  }
}

#undef id
#undef stage
extern "C" void kernel_launch(void* const* d_in, const int* in_sizes, int n_in, void* d_out, int out_size, void* d_ws, size_t ws_size, hipStream_t stream) {
  Params p;
  p.x = (const unsigned short*)d_in[0]; p.wr = (const unsigned short*)d_in[1]; p.wi = (const unsigned short*)d_in[2];
  p.w1 = (const unsigned short*)d_in[4]; p.b1 = (const unsigned short*)d_in[5]; p.w2 = (const unsigned short*)d_in[6]; p.b2 = (const unsigned short*)d_in[7];
  p.out = (unsigned short*)d_out; p.ws = (unsigned char*)d_ws;
  (void)hipFuncSetAttribute((const void*)k_pre, hipFuncAttributeMaxDynamicSharedMemorySize, LDS_BYTES);
  (void)hipFuncSetAttribute((const void*)k_mega, hipFuncAttributeMaxDynamicSharedMemorySize, LDS_BYTES);
  k_pre<<<256, 256, LDS_BYTES, stream>>>(p);
  k_mega<<<224, 256, LDS_BYTES, stream>>>(p);
}
```

```cpp
#include <hip/hip_runtime.h>
#include <hip/hip_bf16.h>
#include <stdint.h>

#define DI __device__ __forceinline__
#define GAS __attribute__((address_space(1)))
#define LAS __attribute__((address_space(3)))
typedef unsigned v4u __attribute__((ext_vector_type(4)));
typedef unsigned v2u __attribute__((ext_vector_type(2)));
typedef float f4 __attribute__((ext_vector_type(4)));
typedef float f2 __attribute__((ext_vector_type(2)));
typedef _Float16 h8 __attribute__((ext_vector_type(8)));
typedef _Float16 h2 __attribute__((ext_vector_type(2)));
typedef __bf16 b8 __attribute__((ext_vector_type(8)));
typedef __bf16 b2 __attribute__((ext_vector_type(2)));
typedef short s4 __attribute__((ext_vector_type(4)));
typedef __amdgpu_buffer_rsrc_t rsrc_t;
typedef LAS unsigned char* lds_t;
typedef GAS unsigned gu32;

#define MAPB 1
#define GWIN 11
#define TLBWARM 1

constexpr int S = 2048, D = 448, F = 1792, NKF = 225;
constexpr float EPS = 1e-5f;
constexpr int XB = S * D * 2;
constexpr int GB = D * F * 2;
constexpr int SC1 = 16;
constexpr int CTRL_BYTES = 262144;
constexpr int MU_OFF = CTRL_BYTES;
constexpr int RS_OFF = MU_OFF + 2 * S * 4;
constexpr int WT_OFF = RS_OFF + 2 * S * 2;
constexpr int XH0_OFF = WT_OFF + XB;
constexpr int XH1_OFF = XH0_OFF + 2 * XB;
constexpr int HF_OFF = XH1_OFF + 2 * XB;
constexpr int GF_OFF = HF_OFF + 2 * XB;
constexpr int X2_OFF = GF_OFF + 2 * GB;
constexpr int WS_BYTES = X2_OFF + XB;
#define STAMP(n)
constexpr int L_A = 0, L_E = 57344, L_R = L_E + 512, L_W = 59392, WSLOT = 6, LDS_BYTES = L_W + 4 * WSLOT * 4096;
constexpr int NR1 = 4, NR2 = 6;

struct Params {
  const unsigned short* x; const unsigned short* wr; const unsigned short* wi;
  const unsigned short* w1; const unsigned short* b1; const unsigned short* w2; const unsigned short* b2;
  unsigned short* out; unsigned char* ws;
};

DI rsrc_t mkrs(const void* p, int bytes) { return __builtin_amdgcn_make_buffer_rsrc((void*)p, 0, bytes, 0x00020000); }
DI f4 mfma_bf(v4u a, v4u b, f4 c) { return __builtin_amdgcn_mfma_f32_16x16x32_bf16(__builtin_bit_cast(b8, a), __builtin_bit_cast(b8, b), c, 0, 0, 0); }
DI f4 mfma_h(v4u a, v4u b, f4 c) { return __builtin_amdgcn_mfma_f32_16x16x32_f16(__builtin_bit_cast(h8, a), __builtin_bit_cast(h8, b), c, 0, 0, 0); }
DI unsigned pk_bf(float a, float b) { f2 v = {a, b}; return __builtin_bit_cast(unsigned, __builtin_convertvector(v, b2)); }
DI unsigned pk_h(float a, float b) { f2 v = {a, b}; return __builtin_bit_cast(unsigned, __builtin_convertvector(v, h2)); }
DI float h_lo(unsigned u) { return (float)__builtin_bit_cast(h2, u)[0]; }
DI float h_hi(unsigned u) { return (float)__builtin_bit_cast(h2, u)[1]; }
DI float bf_lo(unsigned u) { return __builtin_bit_cast(float, u << 16); }
DI float bf_hi(unsigned u) { return __builtin_bit_cast(float, u & 0xffff0000u); }
DI unsigned mul_h2(unsigned a, unsigned b) { return __builtin_bit_cast(unsigned, __builtin_bit_cast(h2, a) * __builtin_bit_cast(h2, b)); }
DI float sq_h2(unsigned a, float c) { return __builtin_amdgcn_fdot2(__builtin_bit_cast(h2, a), __builtin_bit_cast(h2, a), c, false); }
DI float sq_b2(unsigned a, float c) { return __builtin_amdgcn_fdot2_f32_bf16(__builtin_bit_cast(b2, a), __builtin_bit_cast(b2, a), c, false); }
DI float vcos(float x) { return __builtin_amdgcn_cosf(x); }
DI float vsin(float x) { return __builtin_amdgcn_sinf(x); }
DI v2u trread(lds_t p) { return __builtin_bit_cast(v2u, __builtin_amdgcn_ds_read_tr16_b64_v4i16((LAS s4*)p)); }
DI v4u cat(v2u a, v2u b) { return v4u{a.x, a.y, b.x, b.y}; }
DI v4u ldsr128(lds_t p) { return *(LAS v4u*)p; }
DI void ldsw128(lds_t p, v4u v) { *(LAS v4u*)p = v; }
template <int AUX> DI void dma16(rsrc_t rs, lds_t dst, int voff, int soff) {
  const unsigned la = (unsigned)(size_t)dst;
  if constexpr (AUX == SC1) asm volatile("s_mov_b32 m0, %0\n\ts_nop 0\n\tbuffer_load_dwordx4 %1, %2, %3 offen sc1 lds" ::"s"(la), "v"(voff), "s"(rs), "s"(soff) : "memory");
  else asm volatile("s_mov_b32 m0, %0\n\ts_nop 0\n\tbuffer_load_dwordx4 %1, %2, %3 offen lds" ::"s"(la), "v"(voff), "s"(rs), "s"(soff) : "memory");
}
template <int AUX> DI v4u ld128(rsrc_t rs, int voff, int soff = 0) { return __builtin_amdgcn_raw_buffer_load_b128(rs, voff, soff, AUX); }
template <int AUX> DI void st128(v4u v, rsrc_t rs, int voff, int soff = 0) { __builtin_amdgcn_raw_buffer_store_b128(v, rs, voff + soff, 0, AUX); }
DI f4 asf4(v4u v) { return __builtin_bit_cast(f4, v); }
DI v4u asu4(f4 v) { return __builtin_bit_cast(v4u, v); }
template <int N> DI void vmwait() { asm volatile("s_waitcnt vmcnt(%0)" ::"n"(N) : "memory"); }
template <int N> DI void lgwait() { asm volatile("s_waitcnt lgkmcnt(%0)" ::"n"(N) : "memory"); }
DI void rawbar() { asm volatile("" ::: "memory"); __builtin_amdgcn_s_barrier(); asm volatile("" ::: "memory"); }
DI void wgbar() { lgwait<0>(); rawbar(); }
#define SB() __builtin_amdgcn_sched_barrier(0)
DI float sum16(float v) { const unsigned u = __builtin_bit_cast(unsigned, v); const auto r = __builtin_amdgcn_permlane16_swap(u, u, false, false);
  return __builtin_bit_cast(float, (unsigned)r[0]) + __builtin_bit_cast(float, (unsigned)r[1]); }
DI float sum32(float v) { const unsigned u = __builtin_bit_cast(unsigned, v); const auto r = __builtin_amdgcn_permlane32_swap(u, u, false, false);
  return __builtin_bit_cast(float, (unsigned)r[0]) + __builtin_bit_cast(float, (unsigned)r[1]); }
template <class T> DI void pin1(T& v) { asm volatile("" : "+v"(v)); }
template <class T, int N> DI void pin(T (&a)[N]) {
#pragma unroll
  for (int n = 0; n < N; ++n) pin1(a[n]);
}
constexpr f4 Z4 = {0.f, 0.f, 0.f, 0.f};
template <int V> struct IC { static constexpr int v = V; };
template <int A, int B, class Fn> DI void static_for(Fn&& fn) { if constexpr (A < B) { fn(IC<A>{}); static_for<A + 1, B>(fn); } }

struct Ids { int lane, w, l15, g; };
DI Ids get_ids() { Ids d; d.lane = threadIdx.x & 63; d.w = __builtin_amdgcn_readfirstlane(threadIdx.x >> 6); d.l15 = d.lane & 15; d.g = d.lane >> 4; return d; }
DI Ids fresh(const Ids& a) { Ids d; d.lane = a.lane; d.w = a.w; asm volatile("" : "+v"(d.lane), "+s"(d.w)); d.l15 = d.lane & 15; d.g = d.lane >> 4; return d; }
DI int fresh_s(int v) { asm volatile("" : "+s"(v)); return v; }

struct WSrc { rsrc_t rs; int pitch, base, vo; lds_t stage; };
DI WSrc w_src(const Ids& id, const void* w, int pitch, int rows_per_wave, int col0, lds_t L) {
  const int r7 = id.lane >> 3;
  return WSrc{mkrs(w, S * F * 2), pitch, id.w * rows_per_wave * pitch + col0, r7 * pitch + (((id.lane & 7) ^ (2 * ((r7 >> 1) & 3))) << 4), L + L_W + id.w * (WSLOT * 4096)};
}
DI void w_piece(const WSrc& ws, int k0, int n, lds_t dst) { dma16<0>(ws.rs, dst + n * 1024, ws.vo, ws.base + (32 * k0 + 8 * n) * ws.pitch); }
template <int N0, int N1> DI void w_issue(const WSrc& ws, int k0, lds_t dst) {
#pragma unroll
  for (int n = N0; n < N1; ++n) w_piece(ws, k0, n, dst);
}
template <int CNT, int O, int N> DI void w_read(const Ids& id, lds_t stage, v4u (&W)[N][4]) {
  const int q = id.l15 >> 2, p = id.l15 & 3, row = 4 * id.g + q, fr = (row >> 1) & 3;
  lds_t b[4];
#pragma unroll
  for (int n = 0; n < 4; ++n) b[n] = stage + row * 128 + 32 * ((n ^ (id.w & 2)) ^ fr) + 8 * p;
#pragma unroll
  for (int ks = 0; ks < CNT; ++ks)
#pragma unroll
    for (int n = 0; n < 4; ++n) W[O + ks][n] = cat(trread(b[n] + ks * 4096), trread(b[n] + ks * 4096 + 2048));
}

template <class Fn> DI void gen_tf(const Ids& id, int j, v4u (&T)[14], Fn side) {
  const int c = 64 * j + 16 * id.w + id.l15;
  const bool dc = c == 447;
  const float kf = dc ? 0.f : (float)((c >> 1) + 1) * (1.0f / 448.0f);
  const float base = __builtin_fmaf((float)(8 * id.g), kf, (c & 1) && !dc ? 0.25f : 0.f);
#pragma unroll
  for (int ks = 0; ks < 14; ++ks) {
    side(ks);
    unsigned u[4];
#pragma unroll
    for (int e = 0; e < 4; ++e)
      u[e] = pk_bf(vcos(__builtin_fmaf((float)(32 * ks + 2 * e), kf, base)), vcos(__builtin_fmaf((float)(32 * ks + 2 * e + 1), kf, base)));
    T[ks] = v4u{u[0], u[1], u[2], u[3]};
    pin1(T[ks]);
  }
}
template <int AUX> DI void f0_issue(const Ids& id, rsrc_t rx, int xbase, int i, lds_t L) {
  const int r7 = id.lane >> 3;
  const int vo = r7 * 896 + (((id.lane & 7) ^ (2 * id.w + ((r7 >> 1) & 1))) << 4);
#pragma unroll
  for (int n = 0; n < 14; ++n) dma16<AUX>(rx, L + L_A + (4 * n + id.w) * 1024, vo, xbase + (64 * i + 32 * (n & 1) + 8 * id.w) * 896 + (n >> 1) * 128);
}
template <bool STATS, int YOUNG> DI void f0_compute(const Ids& id, rsrc_t rw, int layer, int i, int j, lds_t L, const v4u (&T)[14]) {
  constexpr int PD = 8;
  const int rho = id.l15, xr = (rho >> 1) & 7;
  const int rowb = (8 * (rho >> 2) + (rho & 3)) * 128, tw = 4096 * (id.w >> 1) + 512 * (id.w & 1);
  lds_t pa[2][4];
#pragma unroll
  for (int n = 0; n < 4; ++n) {
    const int o = rowb + ((4096 * (n >> 1) + 512 * (n & 1)) ^ tw);
    pa[0][n] = L + L_A + o + ((id.g ^ xr) << 4); pa[1][n] = L + L_A + o + (((id.g ^ xr) << 4) ^ 64);
  }
  vmwait<6 + YOUNG>(); rawbar();
  v4u a[PD];
#pragma unroll
  for (int u = 0; u < PD; ++u) a[u] = ldsr128(pa[(u >> 2) & 1][u & 3] + (u >> 3) * 8192);
  SB();
  f4 acc[4];
  float e0 = 0.f, e1 = 0.f;
#pragma unroll
  for (int u = 0; u < 56; ++u) {
    const int ks = u >> 2, n = u & 3, v = u + PD;
    const v4u cur = a[u % PD];
    acc[n] = mfma_bf(cur, T[ks], ks == 0 ? Z4 : acc[n]);
    if (STATS && n == 0) { e0 = sq_b2(cur[0], e0); e1 = sq_b2(cur[1], e1); e0 = sq_b2(cur[2], e0); e1 = sq_b2(cur[3], e1); }
    if (v == 32) { vmwait<YOUNG>(); rawbar(); }
    if (v < 56) a[u % PD] = ldsr128(pa[(v >> 2) & 1][v & 3] + (v >> 3) * 8192);
    SB();
  }
  const int c = 64 * j + 16 * id.w + id.l15;
  const float sc = c == 447 ? 0.f : (c == 446 ? 0.047245559126153f : 0.066815310478106f);
#pragma unroll
  for (int q = 0; q < 2; ++q) {
    const f4 x = acc[2 * q] * sc, y = acc[2 * q + 1] * sc;
    const unsigned x0 = pk_h(x[0], x[1]), x1 = pk_h(x[2], x[3]), y0 = pk_h(y[0], y[1]), y1 = pk_h(y[2], y[3]);
    const bool sw = id.w & 1;
    st128<SC1>(v4u{sw ? y0 : x0, sw ? y1 : x1, sw ? x0 : y0, sw ? x1 : y1}, rw, c * 128 + 16 * id.g, XH0_OFF + layer * XB + i * 57344 + 64 * (q ^ (id.w >> 1)));
  }
  if (STATS) {
    LAS float* eb = (LAS float*)(L + L_E);
    const float e = sum32(sum16(e0 + e1));
    if (id.g == 0) eb[32 * (id.w >> 1) + 4 * (id.w & 1) + 8 * (rho >> 2) + (rho & 3)] = e;
    if (c == 447) {
#pragma unroll
      for (int n = 0; n < 4; ++n) *(LAS f4*)(eb + 64 + 32 * ((n ^ 3) >> 1) + 4 * ((n ^ 3) & 1) + 8 * id.g) = acc[n];
    }
    wgbar();
    if (id.w == 0) {
      const float m = eb[64 + id.lane] * (1.0f / 448.0f);
      const float r = __builtin_amdgcn_rsqf(__builtin_fmaxf(__builtin_fmaf(-m, m, eb[id.lane] * (1.0f / 448.0f)), 0.f) + EPS);
      __builtin_amdgcn_raw_buffer_store_b32(__builtin_bit_cast(unsigned, m), rw, id.lane * 4, MU_OFF + (layer * S + 64 * i) * 4, SC1);
      __builtin_amdgcn_raw_buffer_store_b16((unsigned short)(pk_h(r, r) & 0xffffu), rw, id.lane * 2, RS_OFF + (layer * S + 64 * i) * 2, SC1);
    }
  }
}
DI void wt_issue(const Params& p, int b, unsigned (&u)[16]) {
  const int k1 = (b & 7) * 8 + ((b >> 3) & 7), g = b >> 6;
  const rsrc_t rr = mkrs(p.wr, S * NKF * 2), ri = mkrs(p.wi, S * NKF * 2);
  const int vo = ((int)threadIdx.x < 224 ? (int)threadIdx.x + 1 : 0) * 2;
#pragma unroll
  for (int m = 0; m < 8; ++m) {
    const int so = (k1 + 512 * g + 64 * m) * NKF * 2;
    asm volatile("buffer_load_ushort %0, %1, %2, %3 offen" : "=v"(u[2 * m]) : "v"(vo), "s"(rr), "s"(so) : "memory");
    asm volatile("buffer_load_ushort %0, %1, %2, %3 offen" : "=v"(u[2 * m + 1]) : "v"(vo), "s"(ri), "s"(so) : "memory");
  }
}
DI void wt_store(rsrc_t rw, int b, unsigned (&u)[16]) {
  pin(u);
  const int k1 = (b & 7) * 8 + ((b >> 3) & 7), g = b >> 6, t = threadIdx.x;
  if (t < 224) {
    const int so = WT_OFF + (k1 >> 4) * 2048 + (g * 16 + (k1 & 15)) * 32;
    st128<SC1>(v4u{u[0] | (u[1] << 16), u[2] | (u[3] << 16), u[4] | (u[5] << 16), u[6] | (u[7] << 16)}, rw, t * 8192, so);
    st128<SC1>(v4u{u[8] | (u[9] << 16), u[10] | (u[11] << 16), u[12] | (u[13] << 16), u[14] | (u[15] << 16)}, rw, t * 8192 + 16, so);
  }
}

struct PC { v4u fr64[4], fi64[4], fr32[4], fi32[4]; float twc[8], tws[8], tic[8], tis[8]; };
DI void cs_tuple(const float (&x)[4], v4u& re, v4u& im) {
  float c[4], s[4];
#pragma unroll
  for (int e = 0; e < 4; ++e) { c[e] = vcos(x[e]); s[e] = vsin(x[e]); }
  const unsigned c0 = pk_h(c[0], c[1]), c1 = pk_h(c[2], c[3]), s0 = pk_h(s[0], s[1]), s1 = pk_h(s[2], s[3]);
  re = v4u{c0, c1, s0, s1}; im = v4u{s0 ^ 0x80008000u, s1 ^ 0x80008000u, c0, c1};
  pin1(re); pin1(im);
}
template <class Fn> DI void gen_pc(const Ids& id, PC& k, Fn side) {
  const float a64 = (float)(16 * id.w + id.l15) * (1.0f / 64.0f), g4 = (float)(4 * id.g);
#pragma unroll
  for (int ks = 0; ks < 4; ++ks) {
    side(ks);
    float x[4];
#pragma unroll
    for (int e = 0; e < 4; ++e) x[e] = (g4 + (float)(16 * ks + e)) * a64;
    cs_tuple(x, k.fr64[ks], k.fi64[ks]);
  }
  const float pl = (float)(8 * (id.l15 >> 2) + (id.l15 & 3)), g8 = (float)(8 * id.g);
#pragma unroll
  for (int t = 0; t < 4; ++t) {
    side(4 + t);
    float x[4];
#pragma unroll
    for (int e = 0; e < 4; ++e) x[e] = (g8 + (float)(4 * (t & 1) + e)) * ((pl + (float)(4 * (t >> 1))) * (1.0f / 32.0f));
    cs_tuple(x, k.fr32[t], k.fi32[t]);
  }
  const float k1a = (float)(16 * id.w + id.l15) * (1.0f / 2048.0f), k1b = (float)(16 * id.w + 4 * id.g);
#pragma unroll
  for (int m = 0; m < 8; ++m) {
    if (!(m & 1)) side(8 + (m >> 1));
    const float x = (g8 + (float)m) * k1a;
    k.twc[m] = vcos(x) * 0.125f; k.tws[m] = vsin(x) * 0.125f;
    const float y = (k1b + (float)(m & 3)) * ((pl + (float)(4 * (m >> 2))) * (1.0f / 2048.0f));
    k.tic[m] = vcos(y) * 0.17677669529663687f; k.tis[m] = vsin(y) * 0.17677669529663687f;
    pin1(k.twc[m]); pin1(k.tws[m]); pin1(k.tic[m]); pin1(k.tis[m]);
  }
}
DI v4u cmul_pack(const f4& a, const f4& b, const float* c, const float* s) {
  float u[4], v[4];
#pragma unroll
  for (int e = 0; e < 4; ++e) { u[e] = a[e] * c[e] + b[e] * s[e]; v[e] = b[e] * c[e] - a[e] * s[e]; }
  return v4u{pk_h(u[0], u[1]), pk_h(u[2], u[3]), pk_h(v[0], v[1]), pk_h(v[2], v[3])};
}
template <class Fn> DI void p_phase(const Ids& id, rsrc_t rw, int layer, int jj, lds_t L, const PC& k, Fn slot) {
  const int s0 = 512 * id.w + 32 * id.l15 + 8 * id.g;
  const int xo = (s0 >> 6) * 57344 + (s0 & 63) * 2 + jj * 256;
  const v4u rs = ld128<SC1>(rw, s0 * 2, RS_OFF + layer * S * 2);
  const v4u xr = ld128<SC1>(rw, xo, XH0_OFF + layer * XB), xi = ld128<SC1>(rw, xo + 128, XH0_OFF + layer * XB);
  v4u wt[2];
  wt[0] = ld128<SC1>(rw, (64 * id.w + id.lane) * 32, WT_OFF + jj * 8192); wt[1] = ld128<SC1>(rw, (64 * id.w + id.lane) * 32 + 16, WT_OFF + jj * 8192);
  SB();
  slot();
  SB();
  lds_t zp = L + L_A + (16 * id.w + id.l15) * 64 + id.g * 16;
  ldsw128(zp, v4u{mul_h2(xr[0], rs[0]), mul_h2(xr[1], rs[1]), mul_h2(xr[2], rs[2]), mul_h2(xr[3], rs[3])});
  ldsw128(zp + 4096, v4u{mul_h2(xi[0], rs[0]), mul_h2(xi[1], rs[1]), mul_h2(xi[2], rs[2]), mul_h2(xi[3], rs[3])});
  wgbar();
  lds_t tp = L + L_A + (4 * id.g + (id.l15 >> 2)) * 64 + (id.l15 & 3) * 16;
  v4u a[8];
#pragma unroll
  for (int u = 0; u < 8; ++u) a[u] = cat(trread(tp + (u & 3) * 1024 + (u >> 2) * 8), trread(tp + 4096 + (u & 3) * 1024 + (u >> 2) * 8));
  float wr[8], wi[8];
#pragma unroll
  for (int m = 0; m < 8; ++m) { const unsigned t = wt[m >> 2][m & 3]; wr[m] = bf_lo(t) * 0.17677669529663687f; wi[m] = bf_hi(t) * -0.17677669529663687f; }
  pin(wr); pin(wi);
  SB();
  f4 ar[2], ai[2];
#pragma unroll
  for (int u = 0; u < 8; ++u) { const int t = u >> 2, ks = u & 3; ar[t] = mfma_h(a[u], k.fr64[ks], ks ? ar[t] : Z4); ai[t] = mfma_h(a[u], k.fi64[ks], ks ? ai[t] : Z4); }
  v4u bq[2];
#pragma unroll
  for (int t = 0; t < 2; ++t) bq[t] = cmul_pack(ar[t], ai[t], k.twc + 4 * t, k.tws + 4 * t);
  v4u yq[2];
  f4 yr[2], yi[2];
#pragma unroll
  for (int tm = 0; tm < 2; ++tm) {
    yr[tm] = mfma_h(k.fr32[2 * tm], bq[0], Z4); yi[tm] = mfma_h(k.fi32[2 * tm], bq[0], Z4);
  }
#pragma unroll
  for (int tm = 0; tm < 2; ++tm) {
    yr[tm] = mfma_h(k.fr32[2 * tm + 1], bq[1], yr[tm]); yi[tm] = mfma_h(k.fi32[2 * tm + 1], bq[1], yi[tm]);
  }
#pragma unroll
  for (int tm = 0; tm < 2; ++tm) {
    float u[4], v[4];
#pragma unroll
    for (int e = 0; e < 4; ++e) { u[e] = yr[tm][e] * wr[4 * tm + e] + yi[tm][e] * wi[4 * tm + e]; v[e] = yr[tm][e] * wi[4 * tm + e] - yi[tm][e] * wr[4 * tm + e]; }
    yq[tm] = v4u{pk_h(u[0], u[1]), pk_h(u[2], u[3]), pk_h(v[0], v[1]), pk_h(v[2], v[3])};
  }
  f4 br[2], bi[2];
#pragma unroll
  for (int t = 0; t < 2; ++t) { br[t] = mfma_h(yq[0], k.fr32[2 * t], Z4); bi[t] = mfma_h(yq[0], k.fi32[2 * t], Z4); }
#pragma unroll
  for (int t = 0; t < 2; ++t) { br[t] = mfma_h(yq[1], k.fr32[2 * t + 1], br[t]); bi[t] = mfma_h(yq[1], k.fi32[2 * t + 1], bi[t]); }
#pragma unroll
  for (int t = 0; t < 2; ++t) ldsw128(L + L_A + 8192 + (2 * id.w + t) * 1024 + id.lane * 16, cmul_pack(br[t], bi[t], k.tic + 4 * t, k.tis + 4 * t));
  wgbar();
#pragma unroll
  for (int u = 0; u < 8; ++u) a[u] = ldsr128(L + L_A + 8192 + (2 * (u & 3) + (u >> 2)) * 1024 + id.lane * 16);
  float xf[16];
#pragma unroll
  for (int e = 0; e < 4; ++e) { xf[2 * e] = h_lo(xr[e]); xf[2 * e + 1] = h_hi(xr[e]); xf[8 + 2 * e] = h_lo(xi[e]); xf[8 + 2 * e + 1] = h_hi(xi[e]); }
  pin(xf);
  SB();
#pragma unroll
  for (int u = 0; u < 8; ++u) { const int t = u >> 2, ks = u & 3; ar[t] = mfma_h(a[u], k.fr64[ks], ks ? ar[t] : Z4); ai[t] = mfma_h(a[u], k.fi64[ks], ks ? ai[t] : Z4); }
  const float si = jj == 223 ? 0.f : -0.125f;
  v4u o0, o1;
#pragma unroll
  for (int e = 0; e < 4; ++e) {
    const int t = e >> 1, c = 2 * (e & 1);
    o0[e] = pk_h(__builtin_fmaf(ar[t][c], 0.125f, xf[2 * e]), __builtin_fmaf(ar[t][c + 1], 0.125f, xf[2 * e + 1]));
    o1[e] = pk_h(__builtin_fmaf(ai[t][c], si, xf[8 + 2 * e]), __builtin_fmaf(ai[t][c + 1], si, xf[8 + 2 * e + 1]));
  }
  st128<SC1>(o0, rw, xo, XH1_OFF + layer * XB); st128<SC1>(o1, rw, xo + 128, XH1_OFF + layer * XB);
}

template <class Fn> DI void gen_ti(const Ids& id, int j, v4u (&T)[14], Fn side) {
  const float df = (float)(64 * j + 16 * id.w + id.l15) * (1.0f / 448.0f);
  const float o = (float)(16 * (id.w & 1));
  const float be = ((float)(2 * id.g + 1) + o) * df, bo = ((float)(2 * id.g + 1) - o) * df;
#pragma unroll
  for (int m = 0; m < 14; ++m) {
    side(m);
    unsigned u[4];
#pragma unroll
    for (int e = 0; e < 4; ++e) {
      const float x = __builtin_fmaf((float)(16 * m + 8 * (e >> 1) + (e & 1)), df, m & 1 ? bo : be);
      float c = vcos(x);
      if (m >= 12 && e == 3) c *= ((m ^ (id.w & 1)) == 13 && id.g == 3) ? 0.70710678118654752f : 1.0f;
      u[e] = pk_h(c, -vsin(x));
    }
    T[m] = v4u{u[0], u[1], u[2], u[3]};
    pin1(T[m]);
  }
}
DI void s3_issue(const Ids& id, rsrc_t rw, int layer, int i, lds_t L) {
  const int r7 = id.lane >> 3;
  const int vo = r7 * 128 + (((id.lane & 7) ^ (2 * ((r7 >> 1) & 3))) << 4);
#pragma unroll
  for (int n = 0; n < 14; ++n) dma16<SC1>(rw, L + L_A + (4 * n + id.w) * 1024, vo, XH1_OFF + layer * XB + i * 57344 + (4 * n + id.w) * 1024);
}
constexpr int s3_m(int u) { return u < 32 ? u >> 2 : (u < 38 ? 8 + ((u - 32) & ~1) : (u < 44 ? 8 + ((u - 38) & ~1) : 9 + 2 * ((u - 44) >> 2))); }
constexpr int s3_n(int u) { return u < 32 ? u & 3 : (u < 38 ? u & 1 : (u < 44 ? 2 + (u & 1) : u & 3)); }
DI void s3_compute(const Ids& id, rsrc_t rw, int layer, int i, int j, lds_t L, const v4u (&T)[14], f4 (&mu)[4], f4 (&x1)[4]) {
  constexpr int PD = 6;
  const int q = id.l15 >> 2, p = id.l15 & 3, row = 4 * id.g + q, fr = (row >> 1) & 3, ko = (id.w & 1) * 4096;
  lds_t tb[2][4];
#pragma unroll
  for (int n = 0; n < 4; ++n) { lds_t t = L + L_A + row * 128 + 32 * ((n ^ (id.w & 2)) ^ fr) + 8 * p; tb[0][n] = t + ko; tb[1][n] = t - ko; }
  vmwait<6>(); rawbar();
  v4u a[PD];
#pragma unroll
  for (int u = 0; u < PD; ++u) { lds_t t = tb[s3_m(u) & 1][s3_n(u)] + s3_m(u) * 4096; a[u] = cat(trread(t), trread(t + 2048)); }
  SB();
  f4 acc[4], rr[4];
  float e0 = 0.f, e1 = 0.f;
  LAS float* eb = (LAS float*)(L + L_E);
  LAS float* rb = (LAS float*)(L + L_R) + 64 * id.w;
  static_for<0, 56>([&](auto U) {
    constexpr int u = U.v, m = s3_m(u), n = s3_n(u), v = u + PD;
    const v4u cur = a[u % PD];
    acc[n] = mfma_h(cur, T[m], m == 0 ? Z4 : acc[n]);
    if constexpr (n == 0 && !(m & 1)) e0 = sq_h2(cur[3], sq_h2(cur[2], sq_h2(cur[1], sq_h2(cur[0], e0))));
    if constexpr (n == 1 && !(m & 1)) e1 = sq_h2(cur[3], sq_h2(cur[2], sq_h2(cur[1], sq_h2(cur[0], e1))));
    if constexpr (v == 32) { vmwait<0>(); rawbar(); pin(mu); }
    if constexpr (u == 38) {
      e0 = sum32(sum16(e0)); e1 = sum32(sum16(e1));
      eb[64 * (id.w & 1) + 16 * ((id.w & 2) + (id.g & 1)) + id.l15] = id.g & 1 ? e1 : e0;
      SB();
    }
    if constexpr (u == 41) { lgwait<6>(); rawbar(); }
    if constexpr (u == 42) {
      const float vr = (eb[id.lane] + eb[64 + id.lane]) * (1.0f / 448.0f);
      const float r1 = __builtin_amdgcn_rsqf(vr + EPS);
      rb[id.lane] = r1 * __builtin_amdgcn_rsqf(vr * r1 * r1 + EPS) * 0.066815310478106f;
    }
    if constexpr (u == 50) {
#pragma unroll
      for (int t = 0; t < 4; ++t) rr[t] = *(LAS f4*)(rb + 16 * (t ^ (id.w & 2)) + 4 * id.g);
    }
    if constexpr (v < 56) { lds_t t = tb[s3_m(v) & 1][s3_n(v)] + s3_m(v) * 4096; a[u % PD] = cat(trread(t), trread(t + 2048)); }
    SB();
  });
#pragma unroll
  for (int qq = 0; qq < 2; ++qq) {
    const f4 x = acc[2 * qq] * rr[2 * qq], y = acc[2 * qq + 1] * rr[2 * qq + 1];
    st128<SC1>(v4u{pk_bf(x[0], x[1]), pk_bf(x[2], x[3]), pk_bf(y[0], y[1]), pk_bf(y[2], y[3])}, rw, id.lane * 16,
               HF_OFF + layer * XB + ((j * 64 + 2 * i + (qq ^ (id.w >> 1))) * 4 + id.w) * 1024);
  }
  SB();
#pragma unroll
  for (int n = 0; n < 4; ++n) x1[n] = acc[n] * 0.066815310478106f + mu[n];
  pin(x1);
}

template <int NKS, int NR, int NW, class Fn>
DI void g_main(const Ids& id, rsrc_t rw, int abase, lds_t L, const v4u (&R)[NR][4], v4u (&W)[NW][4], f4 (&res)[4], Fn slot) {
  constexpr int WIN = GWIN, NU = 4 * NKS;
  v4u a[WIN];
  const int vo = id.lane * 16;
  int sb[4];
#pragma unroll
  for (int n = 0; n < 4; ++n) sb[n] = abase + id.w * NKS * 4096 + ((((n + 1) & 3) ^ id.w) << 10);
#pragma unroll
  for (int u = 0; u < WIN; ++u) a[u] = ld128<SC1>(rw, vo, sb[u / NKS] + (u % NKS) * 4096);
  SB();
  vmwait<WIN>();
  slot();
  SB();
  f4 part[4];
#pragma unroll
  for (int qn = 0; qn < 4; ++qn) {
    f4 acc[4];
#pragma unroll
    for (int ks = 0; ks < NKS; ++ks) {
      const int u = qn * NKS + ks, v = u - 1 + WIN;
      const v4u cur = a[u % WIN];
#pragma unroll
      for (int f = 0; f < 4; ++f) {
        acc[f] = mfma_bf(ks < NR ? R[ks < NR ? ks : 0][f] : W[ks < NR ? 0 : ks - NR][f], cur, ks == 0 ? Z4 : acc[f]);
        if (f == 0 && u >= 1 && v < NU) { SB(); a[(u - 1) % WIN] = ld128<SC1>(rw, vo, sb[v / NKS] + (v % NKS) * 4096); SB(); }
      }
      if (qn == 3 && ks == 4) {
        wgbar();
#pragma unroll
        for (int f = 0; f < 4; ++f) {
          lds_t rp = L + L_A + (id.w * 12 + (f ^ (id.w & 2))) * 1024 + id.lane * 16;
          part[f] = (asf4(ldsr128(rp)) + asf4(ldsr128(rp + 4096))) + asf4(ldsr128(rp + 8192));
        }
      }
      SB();
    }
    if (qn == 0) wgbar();
    if (qn < 3) {
#pragma unroll
      for (int f = 0; f < 4; ++f) ldsw128(L + L_A + (((id.w ^ (qn + 1)) * 3 + qn) * 4 + (f ^ (id.w & 2))) * 1024 + id.lane * 16, asu4(acc[f]));
    } else {
#pragma unroll
      for (int f = 0; f < 4; ++f) res[f] = acc[f] + part[f];
    }
  }
}
DI void load_bias(const Ids& id, const unsigned short* b, int o, v2u (&bb)[4]) {
#pragma unroll
  for (int f = 0; f < 4; ++f) bb[f] = *(const GAS v2u*)(b + o + 16 * (f ^ (id.w & 2)) + 4 * id.g);
}
DI f4 bias4(v2u b) { return f4{bf_lo(b.x), bf_hi(b.x), bf_lo(b.y), bf_hi(b.y)}; }
DI f4 gelu4(f4 u) {
  const f4 z = u * (u * u * (0.044715f * -2.302208198144325f) + -2.302208198144325f);
  f4 r;
#pragma unroll
  for (int e = 0; e < 4; ++e) r[e] = __builtin_amdgcn_rcpf(1.0f + __builtin_amdgcn_exp2f(z[e]));
  return u * r;
}
template <class Fn> DI void g1_epi(const Ids& id, rsrc_t rw, int layer, int j, int m, const f4 (&res)[4], const f4 (&bb)[4], Fn side) {
  v2u o[4];
#pragma unroll
  for (int f = 0; f < 4; ++f) {
    side(f);
    const f4 a = gelu4(res[f] + bb[f]);
    o[f] = v2u{pk_bf(a[0], a[1]), pk_bf(a[2], a[3])};
    pin1(o[f]);
  }
#pragma unroll
  for (int qq = 0; qq < 2; ++qq)
    st128<SC1>(cat(o[2 * qq], o[2 * qq + 1]), rw, id.lane * 16, GF_OFF + layer * GB + ((j * 56 + 2 * m + (qq ^ (id.w >> 1))) * 4 + id.w) * 1024);
}
DI void g2_epi(const Ids& id, rsrc_t ro, int obase, int i, int j, lds_t L, const f4 (&res)[4], const f4 (&bb)[4]) {
  lds_t T = L + L_A + 49152;
  {
    const int d = 16 * id.w + id.l15, hd = id.l15 ^ (id.w & 1) ^ ((id.w >> 1) << 2);
#pragma unroll
    for (int n = 0; n < 4; ++n) {
      const f4 v = res[n] + bb[n];
      *(LAS v2u*)(T + d * 128 + (((4 * (n ^ (id.w & 2)) + id.g) ^ hd) << 3)) = v2u{pk_bf(v[0], v[1]), pk_bf(v[2], v[3])};
    }
  }
  const int q = id.l15 >> 2, p = id.l15 & 3;
  lds_t tp[2][2];
#pragma unroll
  for (int e = 0; e < 2; ++e)
#pragma unroll
    for (int hh = 0; hh < 2; ++hh) {
      const int sc = 4 * id.w + 2 * e + (id.g >> 1), d = 8 * (4 * (id.g & 1) + p) + 4 * hh + q;
      const int hd = (d & 15) ^ ((d >> 4) & 1) ^ (((d >> 5) & 1) << 2);
      tp[e][hh] = T + d * 128 + ((sc ^ hd) << 3);
    }
  const int vo = (16 * id.w + 4 * (id.lane >> 5) + (id.lane & 3)) * 896 + 16 * ((id.lane >> 2) & 7);
  wgbar();
#pragma unroll
  for (int e = 0; e < 2; ++e) st128<SC1>(cat(trread(tp[e][0]), trread(tp[e][1])), ro, vo, obase + (64 * i + 8 * e) * 896 + j * 128);
}

constexpr int C_EP = 0, C_FL = 1, C_F0 = 16, C_P = C_F0 + 2 * 128, C_S3 = C_P + 2 * 128, C_G1 = C_S3 + 2 * 7 * 32, C_G2 = C_G1 + 2 * 7 * 32, C_LINES = C_G2 + 32 * 8;
static_assert(C_LINES * 128 <= CTRL_BYTES, "control block");
constexpr unsigned SPIN_CAP = 1u << 20;
DI unsigned ld_word(unsigned char* ws, int line) { return __hip_atomic_load((gu32*)ws + 32 * line, __ATOMIC_RELAXED, __HIP_MEMORY_SCOPE_AGENT); }
DI void st_word(unsigned char* ws, int line, unsigned v) { __hip_atomic_store((gu32*)ws + 32 * line, v, __ATOMIC_RELAXED, __HIP_MEMORY_SCOPE_AGENT); }
template <int NS, int NR> DI void seam_arrive(const Ids& id, unsigned char* ws, int line0, int shard) {
  vmwait<0>();
  wgbar();
  if (id.w == 0 && id.lane < NR) __hip_atomic_fetch_add((gu32*)ws + 32 * (line0 + id.lane * NS + shard), 1u, __ATOMIC_RELAXED, __HIP_MEMORY_SCOPE_AGENT);
}
template <int NS> DI void seam_wait(const Ids& id, unsigned char* ws, int line0, int rep, unsigned want) {
  if (id.w == 0) {
    const int line = line0 + rep * NS + (id.lane & (NS - 1));
    for (unsigned sp = 0; sp < SPIN_CAP; ++sp)
      if (__all(ld_word(ws, line) >= want)) break;
    __builtin_amdgcn_fence(__ATOMIC_ACQUIRE, "wavefront");
  }
  wgbar();
}
DI void wait_init(const Ids& id, unsigned char* ws, int rep, unsigned tag) {
  if (id.w == 0) {
    for (unsigned sp = 0; sp < SPIN_CAP; ++sp)
      if (__all(ld_word(ws, C_FL + rep) == tag)) break;
    __builtin_amdgcn_fence(__ATOMIC_ACQUIRE, "wavefront");
  }
}

__global__ void __launch_bounds__(256, 1) k_all(Params p) {
  extern __shared__ __attribute__((aligned(16))) unsigned char smem[];
  lds_t L = (lds_t)smem;
  const Ids id0 = get_ids();
  const int b = blockIdx.x, q = b >> 3, x8 = b & 7;
  const rsrc_t rw = mkrs(p.ws, WS_BYTES);
  STAMP(0);
  unsigned tag = 0;
  if (id0.w == 0) tag = ld_word(p.ws, C_EP) + 1u;
  unsigned wu[16];
  if (q >= 28) {
    if (b == 224) {
      for (int n = C_F0 + (int)threadIdx.x; n < C_LINES; n += 256) st_word(p.ws, n, 0u);
      vmwait<0>(); wgbar();
      if (id0.w == 0 && id0.lane < 8) st_word(p.ws, C_FL + id0.lane, tag);
    }
    wt_issue(p, b, wu);
    if (q == 29) {
      const int t = threadIdx.x;
      unsigned s = 0;
      if (t < WS_BYTES >> 17) s += *(const GAS unsigned*)(p.ws + ((size_t)t << 17) + 65536);
      if (t < 56) { s += *(const GAS unsigned*)((const char*)p.w1 + ((size_t)t << 17)); s += *(const GAS unsigned*)((const char*)p.w2 + ((size_t)t << 17)); }
      if (t < 14) s += *(const GAS unsigned*)((const char*)p.out + ((size_t)t << 17));
      if (t == 0) s += *(const GAS unsigned short*)p.b1 + *(const GAS unsigned short*)p.b2;
      asm volatile("" ::"v"(s));
    }
    vmwait<0>();
    wt_store(rw, b, wu);
    wait_init(id0, p.ws, x8, tag);
    seam_arrive<16, 8>(id0, p.ws, C_F0, b & 15);
    if (b == 224) {
      seam_wait<16>(id0, p.ws, C_F0, x8, 16u);
      if (id0.w == 0 && id0.lane < 9) st_word(p.ws, id0.lane == 8 ? C_EP : C_FL + id0.lane, id0.lane == 8 ? tag : tag - 1u);
    }
    return;
  }
  const int xa = x8 >> 2, xb = x8 & 3, sub = q < 24 ? q / 3 : 4 * xa + q - 24;
  const int j = q < 24 ? 4 * xa + q % 3 : 3, i = 8 * xb + sub, m = 7 * xb + sub;
  const bool g1 = sub < 7;
  const int jj = x8 * 28 + q;
#define ID fresh(id0)
#define W1S w_src(ID, p.w1, F * 2, 512, fresh_s(g1 ? m : 0) * 128, L)
#define W2S w_src(ID, p.w2, S * 2, 448, fresh_s(i) * 128, L)
#define STAGE (L + L_W + fresh_s(id0.w) * (WSLOT * 4096))
  v4u R1[NR1][4], R2[NR2][4];
  {
    f0_issue<0>(ID, mkrs(p.x, XB), 0, i, L);
    wt_issue(p, b, wu);
    const WSrc ws = W2S;
    v4u T[14];
    gen_tf(ID, j, T, [&](int ks) { if (ks < 12) { w_piece(ws, 0, 2 * ks, ws.stage); w_piece(ws, 0, 2 * ks + 1, ws.stage); } });
    STAMP(1);
    if (j == 6) f0_compute<true, 40>(ID, rw, 0, i, j, L, T); else f0_compute<false, 40>(ID, rw, 0, i, j, L, T);
    STAMP(2);
    vmwait<0>();
    wt_store(rw, b, wu);
    w_read<NR2, 0>(ID, STAGE, R2);
    wait_init(id0, p.ws, x8, tag);
    STAMP(3);
    seam_arrive<16, 8>(ID, p.ws, C_F0, b & 15);
    STAMP(4);
  }
#pragma unroll 1
  for (int layer = 0; layer < 2; ++layer) {
    const int sb = layer * 30; (void)sb;
    if (layer == 1) {
      v4u T[14];
      gen_tf(ID, j, T, [](int) {});
      STAMP(sb + 0);
      seam_wait<1>(ID, p.ws, C_G2 + i * 8, j, 7u);
      STAMP(sb + 1);
      f0_issue<SC1>(ID, rw, X2_OFF, i, L);
      if (j == 6) f0_compute<true, 0>(ID, rw, 1, i, j, L, T); else f0_compute<false, 0>(ID, rw, 1, i, j, L, T);
      STAMP(sb + 3);
      seam_arrive<16, 8>(ID, p.ws, C_F0 + 128, b & 15);
      STAMP(sb + 4);
    }
    {
      PC k;
      const WSrc ws = W1S;
      gen_pc(ID, k, [&](int n) { if (layer == 0 && n < 8) { w_piece(ws, 0, 2 * n, ws.stage); w_piece(ws, 0, 2 * n + 1, ws.stage); } });
      STAMP(sb + 5);
      seam_wait<16>(ID, p.ws, C_F0 + layer * 128, x8, layer == 0 ? 16u : 14u);
      STAMP(sb + 6);
      p_phase(ID, rw, layer, jj, L, k, [&]() { if (layer == 0) { vmwait<5>(); w_read<NR1, 0>(ID, STAGE, R1); } });
    }
    STAMP(sb + 7);
    seam_arrive<16, 8>(ID, p.ws, C_P + layer * 128, b & 15);
    STAMP(sb + 8);
    v4u W[12][4];
    f4 x1[4];
    {
      f4 mu[4];
      {
        const Ids id = ID;
#pragma unroll
        for (int n = 0; n < 4; ++n) mu[n] = asf4(ld128<SC1>(rw, (16 * (n ^ (id.w & 2)) + 4 * id.g) * 4, MU_OFF + (layer * S + 64 * i) * 4));
      }
      const WSrc ws = W1S;
      v4u T[14];
      gen_ti(ID, j, T, [&](int n) { if (n < 12) { w_piece(ws, NR1, 2 * n, ws.stage); w_piece(ws, NR1, 2 * n + 1, ws.stage); } });
      STAMP(sb + 9);
      seam_wait<16>(ID, p.ws, C_P + layer * 128, x8, 14u);
      STAMP(sb + 10);
      s3_issue(ID, rw, layer, i, L);
      vmwait<14>();
      w_read<6, 0>(ID, STAGE, W);
      STAMP(sb + 11);
      s3_compute(ID, rw, layer, i, j, L, T, mu, x1);
      STAMP(sb + 12);
    }
    seam_arrive<4, 8>(ID, p.ws, C_S3 + (layer * 7 + j) * 32, i & 3);
    STAMP(sb + 13);
    v2u bq[4];
    f4 bb[4], res[4];
    if (g1) {
      load_bias(ID, p.b1, 64 * m, bq);
      { const WSrc ws = W1S; w_issue<0, 24>(ws, NR1 + 6, ws.stage); }
      STAMP(sb + 14);
      seam_wait<4>(ID, p.ws, C_S3 + (layer * 7 + j) * 32, x8, 8u);
#pragma unroll
      for (int f = 0; f < 4; ++f) bb[f] = bias4(bq[f]);
      pin(bb);
      STAMP(sb + 15);
      g_main<16>(ID, rw, HF_OFF + layer * XB + j * 64 * 4096, L, R1, W, res, [&]() { w_read<6, 6>(ID, STAGE, W); });
      STAMP(sb + 16);
      const WSrc ws = W2S;
      g1_epi(ID, rw, layer, j, m, res, bb, [&](int f) { w_piece(ws, NR2, 2 * f, ws.stage); w_piece(ws, NR2, 2 * f + 1, ws.stage); });
      STAMP(sb + 17);
      seam_arrive<4, 8>(ID, p.ws, C_G1 + (layer * 7 + j) * 32, m & 3);
      STAMP(sb + 18);
    } else { const WSrc ws = W2S; w_issue<0, 8>(ws, NR2, ws.stage); }
    load_bias(ID, p.b2, 64 * i, bq);
    { const WSrc ws = W2S; w_issue<8, 24>(ws, NR2, ws.stage); w_issue<0, 8>(ws, NR2 + 6, L + L_A + fresh_s(id0.w) * 8192); }
    STAMP(sb + 19);
    seam_wait<4>(ID, p.ws, C_G1 + (layer * 7 + j) * 32, x8, 7u);
#pragma unroll
    for (int f = 0; f < 4; ++f) bb[f] = bias4(bq[f]) + x1[f];
    pin(bb);
    STAMP(sb + 20);
    g_main<14>(ID, rw, GF_OFF + layer * GB + j * 56 * 4096, L, R2, W, res,
               [&]() { w_read<6, 0>(ID, STAGE, W); w_read<2, 6>(ID, L + L_A + fresh_s(id0.w) * 8192, W); });
    STAMP(sb + 21);
    g2_epi(ID, layer == 0 ? rw : mkrs(p.out, XB), layer == 0 ? X2_OFF : 0, i, j, L, res, bb);
    STAMP(sb + 22);
    if (layer == 0) seam_arrive<1, 7>(ID, p.ws, C_G2 + i * 8, 0);
    STAMP(sb + 23);
  }
}

extern "C" void kernel_launch(void* const* d_in, const int* in_sizes, int n_in, void* d_out, int out_size, void* d_ws, size_t ws_size, hipStream_t stream) {
  Params p;
  p.x = (const unsigned short*)d_in[0]; p.wr = (const unsigned short*)d_in[1]; p.wi = (const unsigned short*)d_in[2];
  p.w1 = (const unsigned short*)d_in[4]; p.b1 = (const unsigned short*)d_in[5]; p.w2 = (const unsigned short*)d_in[6]; p.b2 = (const unsigned short*)d_in[7];
  p.out = (unsigned short*)d_out; p.ws = (unsigned char*)d_ws;
  (void)hipFuncSetAttribute((const void*)k_all, hipFuncAttributeMaxDynamicSharedMemorySize, LDS_BYTES);
  k_all<<<256, 256, LDS_BYTES, stream>>>(p);
}
```

```cpp
#include <hip/hip_runtime.h>
#include <hip/hip_bf16.h>
#include <stdint.h>

#define DI __device__ __forceinline__
#define GAS __attribute__((address_space(1)))
#define LAS __attribute__((address_space(3)))
typedef unsigned v4u __attribute__((ext_vector_type(4)));
typedef unsigned v2u __attribute__((ext_vector_type(2)));
typedef float f4 __attribute__((ext_vector_type(4)));
typedef float f2 __attribute__((ext_vector_type(2)));
typedef _Float16 h8 __attribute__((ext_vector_type(8)));
typedef _Float16 h2 __attribute__((ext_vector_type(2)));
typedef __bf16 b8 __attribute__((ext_vector_type(8)));
typedef __bf16 b2 __attribute__((ext_vector_type(2)));
typedef short s4 __attribute__((ext_vector_type(4)));
typedef __amdgpu_buffer_rsrc_t rsrc_t;
typedef LAS unsigned char* lds_t;
typedef GAS unsigned gu32;

#define MAPB 0
#define GWIN 11
#define TLBWARM 1

constexpr int S = 2048, D = 448, F = 1792, NKF = 225;
constexpr float EPS = 1e-5f;
constexpr int XB = S * D * 2;
constexpr int GB = D * F * 2;
constexpr int SC1 = 16;
constexpr int CTRL_BYTES = 262144;
constexpr int MU_OFF = CTRL_BYTES;
constexpr int RS_OFF = MU_OFF + 2 * S * 4;
constexpr int WT_OFF = RS_OFF + 2 * S * 2;
constexpr int XH0_OFF = WT_OFF + XB;
constexpr int XH1_OFF = XH0_OFF + 2 * XB;
constexpr int HF_OFF = XH1_OFF + 2 * XB;
constexpr int GF_OFF = HF_OFF + 2 * XB;
constexpr int X2_OFF = GF_OFF + 2 * GB;
constexpr int WS_BYTES = X2_OFF + XB;
#define STAMP(n)
constexpr int L_A = 0, L_E = 57344, L_R = L_E + 512, L_W = 59392, WSLOT = 6, LDS_BYTES = L_W + 4 * WSLOT * 4096;
constexpr int NR1 = 4, NR2 = 6;

struct Params {
  const unsigned short* x; const unsigned short* wr; const unsigned short* wi;
  const unsigned short* w1; const unsigned short* b1; const unsigned short* w2; const unsigned short* b2;
  unsigned short* out; unsigned char* ws;
};

DI rsrc_t mkrs(const void* p, int bytes) { return __builtin_amdgcn_make_buffer_rsrc((void*)p, 0, bytes, 0x00020000); }
DI f4 mfma_bf(v4u a, v4u b, f4 c) { return __builtin_amdgcn_mfma_f32_16x16x32_bf16(__builtin_bit_cast(b8, a), __builtin_bit_cast(b8, b), c, 0, 0, 0); }
DI f4 mfma_h(v4u a, v4u b, f4 c) { return __builtin_amdgcn_mfma_f32_16x16x32_f16(__builtin_bit_cast(h8, a), __builtin_bit_cast(h8, b), c, 0, 0, 0); }
DI unsigned pk_bf(float a, float b) { f2 v = {a, b}; return __builtin_bit_cast(unsigned, __builtin_convertvector(v, b2)); }
DI unsigned pk_h(float a, float b) { f2 v = {a, b}; return __builtin_bit_cast(unsigned, __builtin_convertvector(v, h2)); }
DI float h_lo(unsigned u) { return (float)__builtin_bit_cast(h2, u)[0]; }
DI float h_hi(unsigned u) { return (float)__builtin_bit_cast(h2, u)[1]; }
DI float bf_lo(unsigned u) { return __builtin_bit_cast(float, u << 16); }
DI float bf_hi(unsigned u) { return __builtin_bit_cast(float, u & 0xffff0000u); }
DI unsigned mul_h2(unsigned a, unsigned b) { return __builtin_bit_cast(unsigned, __builtin_bit_cast(h2, a) * __builtin_bit_cast(h2, b)); }
DI float sq_h2(unsigned a, float c) { return __builtin_amdgcn_fdot2(__builtin_bit_cast(h2, a), __builtin_bit_cast(h2, a), c, false); }
DI float sq_b2(unsigned a, float c) { return __builtin_amdgcn_fdot2_f32_bf16(__builtin_bit_cast(b2, a), __builtin_bit_cast(b2, a), c, false); }
DI float vcos(float x) { return __builtin_amdgcn_cosf(x); }
DI float vsin(float x) { return __builtin_amdgcn_sinf(x); }
DI v2u trread(lds_t p) { return __builtin_bit_cast(v2u, __builtin_amdgcn_ds_read_tr16_b64_v4i16((LAS s4*)p)); }
DI v4u cat(v2u a, v2u b) { return v4u{a.x, a.y, b.x, b.y}; }
DI v4u ldsr128(lds_t p) { return *(LAS v4u*)p; }
DI void ldsw128(lds_t p, v4u v) { *(LAS v4u*)p = v; }
template <int AUX> DI void dma16(rsrc_t rs, lds_t dst, int voff, int soff) {
  const unsigned la = (unsigned)(size_t)dst;
  if constexpr (AUX == SC1) asm volatile("s_mov_b32 m0, %0\n\ts_nop 0\n\tbuffer_load_dwordx4 %1, %2, %3 offen sc1 lds" ::"s"(la), "v"(voff), "s"(rs), "s"(soff) : "memory");
  else asm volatile("s_mov_b32 m0, %0\n\ts_nop 0\n\tbuffer_load_dwordx4 %1, %2, %3 offen lds" ::"s"(la), "v"(voff), "s"(rs), "s"(soff) : "memory");
}
template <int AUX> DI v4u ld128(rsrc_t rs, int voff, int soff = 0) { return __builtin_amdgcn_raw_buffer_load_b128(rs, voff, soff, AUX); }
template <int AUX> DI void st128(v4u v, rsrc_t rs, int voff, int soff = 0) { __builtin_amdgcn_raw_buffer_store_b128(v, rs, voff + soff, 0, AUX); }
DI f4 asf4(v4u v) { return __builtin_bit_cast(f4, v); }
DI v4u asu4(f4 v) { return __builtin_bit_cast(v4u, v); }
template <int N> DI void vmwait() { asm volatile("s_waitcnt vmcnt(%0)" ::"n"(N) : "memory"); }
template <int N> DI void lgwait() { asm volatile("s_waitcnt lgkmcnt(%0)" ::"n"(N) : "memory"); }
DI void rawbar() { asm volatile("" ::: "memory"); __builtin_amdgcn_s_barrier(); asm volatile("" ::: "memory"); }
DI void wgbar() { lgwait<0>(); rawbar(); }
#define SB() __builtin_amdgcn_sched_barrier(0)
DI float sum16(float v) { const unsigned u = __builtin_bit_cast(unsigned, v); const auto r = __builtin_amdgcn_permlane16_swap(u, u, false, false);
  return __builtin_bit_cast(float, (unsigned)r[0]) + __builtin_bit_cast(float, (unsigned)r[1]); }
DI float sum32(float v) { const unsigned u = __builtin_bit_cast(unsigned, v); const auto r = __builtin_amdgcn_permlane32_swap(u, u, false, false);
  return __builtin_bit_cast(float, (unsigned)r[0]) + __builtin_bit_cast(float, (unsigned)r[1]); }
template <class T> DI void pin1(T& v) { asm volatile("" : "+v"(v)); }
template <class T, int N> DI void pin(T (&a)[N]) {
#pragma unroll
  for (int n = 0; n < N; ++n) pin1(a[n]);
}
constexpr f4 Z4 = {0.f, 0.f, 0.f, 0.f};
template <int V> struct IC { static constexpr int v = V; };
template <int A, int B, class Fn> DI void static_for(Fn&& fn) { if constexpr (A < B) { fn(IC<A>{}); static_for<A + 1, B>(fn); } }

struct Ids { int lane, w, l15, g; };
DI Ids get_ids() { Ids d; d.lane = threadIdx.x & 63; d.w = __builtin_amdgcn_readfirstlane(threadIdx.x >> 6); d.l15 = d.lane & 15; d.g = d.lane >> 4; return d; }
DI Ids fresh(const Ids& a) { Ids d; d.lane = a.lane; d.w = a.w; asm volatile("" : "+v"(d.lane), "+s"(d.w)); d.l15 = d.lane & 15; d.g = d.lane >> 4; return d; }
DI int fresh_s(int v) { asm volatile("" : "+s"(v)); return v; }

struct WSrc { rsrc_t rs; int pitch, base, vo; lds_t stage; };
DI WSrc w_src(const Ids& id, const void* w, int pitch, int rows_per_wave, int col0, lds_t L) {
  const int r7 = id.lane >> 3;
  return WSrc{mkrs(w, S * F * 2), pitch, id.w * rows_per_wave * pitch + col0, r7 * pitch + (((id.lane & 7) ^ (2 * ((r7 >> 1) & 3))) << 4), L + L_W + id.w * (WSLOT * 4096)};
}
DI void w_piece(const WSrc& ws, int k0, int n, lds_t dst) { dma16<0>(ws.rs, dst + n * 1024, ws.vo, ws.base + (32 * k0 + 8 * n) * ws.pitch); }
template <int N0, int N1> DI void w_issue(const WSrc& ws, int k0, lds_t dst) {
#pragma unroll
  for (int n = N0; n < N1; ++n) w_piece(ws, k0, n, dst);
}
template <int CNT, int O, int N> DI void w_read(const Ids& id, lds_t stage, v4u (&W)[N][4]) {
  const int q = id.l15 >> 2, p = id.l15 & 3, row = 4 * id.g + q, fr = (row >> 1) & 3;
  lds_t b[4];
#pragma unroll
  for (int n = 0; n < 4; ++n) b[n] = stage + row * 128 + 32 * ((n ^ (id.w & 2)) ^ fr) + 8 * p;
#pragma unroll
  for (int ks = 0; ks < CNT; ++ks)
#pragma unroll
    for (int n = 0; n < 4; ++n) W[O + ks][n] = cat(trread(b[n] + ks * 4096), trread(b[n] + ks * 4096 + 2048));
}

template <class Fn> DI void gen_tf(const Ids& id, int j, v4u (&T)[14], Fn side) {
  const int c = 64 * j + 16 * id.w + id.l15;
  const bool dc = c == 447;
  const float kf = dc ? 0.f : (float)((c >> 1) + 1) * (1.0f / 448.0f);
  const float base = __builtin_fmaf((float)(8 * id.g), kf, (c & 1) && !dc ? 0.25f : 0.f);
#pragma unroll
  for (int ks = 0; ks < 14; ++ks) {
    side(ks);
    unsigned u[4];
#pragma unroll
    for (int e = 0; e < 4; ++e)
      u[e] = pk_bf(vcos(__builtin_fmaf((float)(32 * ks + 2 * e), kf, base)), vcos(__builtin_fmaf((float)(32 * ks + 2 * e + 1), kf, base)));
    T[ks] = v4u{u[0], u[1], u[2], u[3]};
    pin1(T[ks]);
  }
}
template <int AUX> DI void f0_issue(const Ids& id, rsrc_t rx, int xbase, int i, lds_t L) {
  const int r7 = id.lane >> 3;
  const int vo = r7 * 896 + (((id.lane & 7) ^ (2 * id.w + ((r7 >> 1) & 1))) << 4);
#pragma unroll
  for (int n = 0; n < 14; ++n) dma16<AUX>(rx, L + L_A + (4 * n + id.w) * 1024, vo, xbase + (64 * i + 32 * (n & 1) + 8 * id.w) * 896 + (n >> 1) * 128);
}
template <bool STATS, int YOUNG> DI void f0_compute(const Ids& id, rsrc_t rw, int layer, int i, int j, lds_t L, const v4u (&T)[14]) {
  constexpr int PD = 8;
  const int rho = id.l15, xr = (rho >> 1) & 7;
  const int rowb = (8 * (rho >> 2) + (rho & 3)) * 128, tw = 4096 * (id.w >> 1) + 512 * (id.w & 1);
  lds_t pa[2][4];
#pragma unroll
  for (int n = 0; n < 4; ++n) {
    const int o = rowb + ((4096 * (n >> 1) + 512 * (n & 1)) ^ tw);
    pa[0][n] = L + L_A + o + ((id.g ^ xr) << 4); pa[1][n] = L + L_A + o + (((id.g ^ xr) << 4) ^ 64);
  }
  vmwait<6 + YOUNG>(); rawbar();
  v4u a[PD];
#pragma unroll
  for (int u = 0; u < PD; ++u) a[u] = ldsr128(pa[(u >> 2) & 1][u & 3] + (u >> 3) * 8192);
  SB();
  f4 acc[4];
  float e0 = 0.f, e1 = 0.f;
#pragma unroll
  for (int u = 0; u < 56; ++u) {
    const int ks = u >> 2, n = u & 3, v = u + PD;
    const v4u cur = a[u % PD];
    acc[n] = mfma_bf(cur, T[ks], ks == 0 ? Z4 : acc[n]);
    if (STATS && n == 0) { e0 = sq_b2(cur[0], e0); e1 = sq_b2(cur[1], e1); e0 = sq_b2(cur[2], e0); e1 = sq_b2(cur[3], e1); }
    if (v == 32) { vmwait<YOUNG>(); rawbar(); }
    if (v < 56) a[u % PD] = ldsr128(pa[(v >> 2) & 1][v & 3] + (v >> 3) * 8192);
    SB();
  }
  const int c = 64 * j + 16 * id.w + id.l15;
  const float sc = c == 447 ? 0.f : (c == 446 ? 0.047245559126153f : 0.066815310478106f);
#pragma unroll
  for (int q = 0; q < 2; ++q) {
    const f4 x = acc[2 * q] * sc, y = acc[2 * q + 1] * sc;
    const unsigned x0 = pk_h(x[0], x[1]), x1 = pk_h(x[2], x[3]), y0 = pk_h(y[0], y[1]), y1 = pk_h(y[2], y[3]);
    const bool sw = id.w & 1;
    st128<SC1>(v4u{sw ? y0 : x0, sw ? y1 : x1, sw ? x0 : y0, sw ? x1 : y1}, rw, c * 128 + 16 * id.g, XH0_OFF + layer * XB + i * 57344 + 64 * (q ^ (id.w >> 1)));
  }
  if (STATS) {
    LAS float* eb = (LAS float*)(L + L_E);
    const float e = sum32(sum16(e0 + e1));
    if (id.g == 0) eb[32 * (id.w >> 1) + 4 * (id.w & 1) + 8 * (rho >> 2) + (rho & 3)] = e;
    if (c == 447) {
#pragma unroll
      for (int n = 0; n < 4; ++n) *(LAS f4*)(eb + 64 + 32 * ((n ^ 3) >> 1) + 4 * ((n ^ 3) & 1) + 8 * id.g) = acc[n];
    }
    wgbar();
    if (id.w == 0) {
      const float m = eb[64 + id.lane] * (1.0f / 448.0f);
      const float r = __builtin_amdgcn_rsqf(__builtin_fmaxf(__builtin_fmaf(-m, m, eb[id.lane] * (1.0f / 448.0f)), 0.f) + EPS);
      __builtin_amdgcn_raw_buffer_store_b32(__builtin_bit_cast(unsigned, m), rw, id.lane * 4, MU_OFF + (layer * S + 64 * i) * 4, SC1);
      __builtin_amdgcn_raw_buffer_store_b16((unsigned short)(pk_h(r, r) & 0xffffu), rw, id.lane * 2, RS_OFF + (layer * S + 64 * i) * 2, SC1);
    }
  }
}
DI void wt_issue(const Params& p, int b, unsigned (&u)[16]) {
  const int k1 = (b & 7) * 8 + ((b >> 3) & 7), g = b >> 6;
  const rsrc_t rr = mkrs(p.wr, S * NKF * 2), ri = mkrs(p.wi, S * NKF * 2);
  const int vo = ((int)threadIdx.x < 224 ? (int)threadIdx.x + 1 : 0) * 2;
#pragma unroll
  for (int m = 0; m < 8; ++m) {
    const int so = (k1 + 512 * g + 64 * m) * NKF * 2;
    asm volatile("buffer_load_ushort %0, %1, %2, %3 offen" : "=v"(u[2 * m]) : "v"(vo), "s"(rr), "s"(so) : "memory");
    asm volatile("buffer_load_ushort %0, %1, %2, %3 offen" : "=v"(u[2 * m + 1]) : "v"(vo), "s"(ri), "s"(so) : "memory");
  }
}
DI void wt_store(rsrc_t rw, int b, unsigned (&u)[16]) {
  pin(u);
  const int k1 = (b & 7) * 8 + ((b >> 3) & 7), g = b >> 6, t = threadIdx.x;
  if (t < 224) {
    const int so = WT_OFF + (k1 >> 4) * 2048 + (g * 16 + (k1 & 15)) * 32;
    st128<SC1>(v4u{u[0] | (u[1] << 16), u[2] | (u[3] << 16), u[4] | (u[5] << 16), u[6] | (u[7] << 16)}, rw, t * 8192, so);
    st128<SC1>(v4u{u[8] | (u[9] << 16), u[10] | (u[11] << 16), u[12] | (u[13] << 16), u[14] | (u[15] << 16)}, rw, t * 8192 + 16, so);
  }
}

struct PC { v4u fr64[4], fi64[4], fr32[4], fi32[4]; float twc[8], tws[8], tic[8], tis[8]; };
DI void cs_tuple(const float (&x)[4], v4u& re, v4u& im) {
  float c[4], s[4];
#pragma unroll
  for (int e = 0; e < 4; ++e) { c[e] = vcos(x[e]); s[e] = vsin(x[e]); }
  const unsigned c0 = pk_h(c[0], c[1]), c1 = pk_h(c[2], c[3]), s0 = pk_h(s[0], s[1]), s1 = pk_h(s[2], s[3]);
  re = v4u{c0, c1, s0, s1}; im = v4u{s0 ^ 0x80008000u, s1 ^ 0x80008000u, c0, c1};
  pin1(re); pin1(im);
}
template <class Fn> DI void gen_pc(const Ids& id, PC& k, Fn side) {
  const float a64 = (float)(16 * id.w + id.l15) * (1.0f / 64.0f), g4 = (float)(4 * id.g);
#pragma unroll
  for (int ks = 0; ks < 4; ++ks) {
    side(ks);
    float x[4];
#pragma unroll
    for (int e = 0; e < 4; ++e) x[e] = (g4 + (float)(16 * ks + e)) * a64;
    cs_tuple(x, k.fr64[ks], k.fi64[ks]);
  }
  const float pl = (float)(8 * (id.l15 >> 2) + (id.l15 & 3)), g8 = (float)(8 * id.g);
#pragma unroll
  for (int t = 0; t < 4; ++t) {
    side(4 + t);
    float x[4];
#pragma unroll
    for (int e = 0; e < 4; ++e) x[e] = (g8 + (float)(4 * (t & 1) + e)) * ((pl + (float)(4 * (t >> 1))) * (1.0f / 32.0f));
    cs_tuple(x, k.fr32[t], k.fi32[t]);
  }
  const float k1a = (float)(16 * id.w + id.l15) * (1.0f / 2048.0f), k1b = (float)(16 * id.w + 4 * id.g);
#pragma unroll
  for (int m = 0; m < 8; ++m) {
    if (!(m & 1)) side(8 + (m >> 1));
    const float x = (g8 + (float)m) * k1a;
    k.twc[m] = vcos(x) * 0.125f; k.tws[m] = vsin(x) * 0.125f;
    const float y = (k1b + (float)(m & 3)) * ((pl + (float)(4 * (m >> 2))) * (1.0f / 2048.0f));
    k.tic[m] = vcos(y) * 0.17677669529663687f; k.tis[m] = vsin(y) * 0.17677669529663687f;
    pin1(k.twc[m]); pin1(k.tws[m]); pin1(k.tic[m]); pin1(k.tis[m]);
  }
}
DI v4u cmul_pack(const f4& a, const f4& b, const float* c, const float* s) {
  float u[4], v[4];
#pragma unroll
  for (int e = 0; e < 4; ++e) { u[e] = a[e] * c[e] + b[e] * s[e]; v[e] = b[e] * c[e] - a[e] * s[e]; }
  return v4u{pk_h(u[0], u[1]), pk_h(u[2], u[3]), pk_h(v[0], v[1]), pk_h(v[2], v[3])};
}
template <class Fn> DI void p_phase(const Ids& id, rsrc_t rw, int layer, int jj, lds_t L, const PC& k, Fn slot) {
  const int s0 = 512 * id.w + 32 * id.l15 + 8 * id.g;
  const int xo = (s0 >> 6) * 57344 + (s0 & 63) * 2 + jj * 256;
  const v4u rs = ld128<SC1>(rw, s0 * 2, RS_OFF + layer * S * 2);
  const v4u xr = ld128<SC1>(rw, xo, XH0_OFF + layer * XB), xi = ld128<SC1>(rw, xo + 128, XH0_OFF + layer * XB);
  v4u wt[2];
  wt[0] = ld128<SC1>(rw, (64 * id.w + id.lane) * 32, WT_OFF + jj * 8192); wt[1] = ld128<SC1>(rw, (64 * id.w + id.lane) * 32 + 16, WT_OFF + jj * 8192);
  SB();
  slot();
  SB();
  lds_t zp = L + L_A + (16 * id.w + id.l15) * 64 + id.g * 16;
  ldsw128(zp, v4u{mul_h2(xr[0], rs[0]), mul_h2(xr[1], rs[1]), mul_h2(xr[2], rs[2]), mul_h2(xr[3], rs[3])});
  ldsw128(zp + 4096, v4u{mul_h2(xi[0], rs[0]), mul_h2(xi[1], rs[1]), mul_h2(xi[2], rs[2]), mul_h2(xi[3], rs[3])});
  wgbar();
  lds_t tp = L + L_A + (4 * id.g + (id.l15 >> 2)) * 64 + (id.l15 & 3) * 16;
  v4u a[8];
#pragma unroll
  for (int u = 0; u < 8; ++u) a[u] = cat(trread(tp + (u & 3) * 1024 + (u >> 2) * 8), trread(tp + 4096 + (u & 3) * 1024 + (u >> 2) * 8));
  float wr[8], wi[8];
#pragma unroll
  for (int m = 0; m < 8; ++m) { const unsigned t = wt[m >> 2][m & 3]; wr[m] = bf_lo(t) * 0.17677669529663687f; wi[m] = bf_hi(t) * -0.17677669529663687f; }
  pin(wr); pin(wi);
  SB();
  f4 ar[2], ai[2];
#pragma unroll
  for (int u = 0; u < 8; ++u) { const int t = u >> 2, ks = u & 3; ar[t] = mfma_h(a[u], k.fr64[ks], ks ? ar[t] : Z4); ai[t] = mfma_h(a[u], k.fi64[ks], ks ? ai[t] : Z4); }
  v4u bq[2];
#pragma unroll
  for (int t = 0; t < 2; ++t) bq[t] = cmul_pack(ar[t], ai[t], k.twc + 4 * t, k.tws + 4 * t);
  v4u yq[2];
  f4 yr[2], yi[2];
#pragma unroll
  for (int tm = 0; tm < 2; ++tm) {
    yr[tm] = mfma_h(k.fr32[2 * tm], bq[0], Z4); yi[tm] = mfma_h(k.fi32[2 * tm], bq[0], Z4);
  }
#pragma unroll
  for (int tm = 0; tm < 2; ++tm) {
    yr[tm] = mfma_h(k.fr32[2 * tm + 1], bq[1], yr[tm]); yi[tm] = mfma_h(k.fi32[2 * tm + 1], bq[1], yi[tm]);
  }
#pragma unroll
  for (int tm = 0; tm < 2; ++tm) {
    float u[4], v[4];
#pragma unroll
    for (int e = 0; e < 4; ++e) { u[e] = yr[tm][e] * wr[4 * tm + e] + yi[tm][e] * wi[4 * tm + e]; v[e] = yr[tm][e] * wi[4 * tm + e] - yi[tm][e] * wr[4 * tm + e]; }
    yq[tm] = v4u{pk_h(u[0], u[1]), pk_h(u[2], u[3]), pk_h(v[0], v[1]), pk_h(v[2], v[3])};
  }
  f4 br[2], bi[2];
#pragma unroll
  for (int t = 0; t < 2; ++t) { br[t] = mfma_h(yq[0], k.fr32[2 * t], Z4); bi[t] = mfma_h(yq[0], k.fi32[2 * t], Z4); }
#pragma unroll
  for (int t = 0; t < 2; ++t) { br[t] = mfma_h(yq[1], k.fr32[2 * t + 1], br[t]); bi[t] = mfma_h(yq[1], k.fi32[2 * t + 1], bi[t]); }
#pragma unroll
  for (int t = 0; t < 2; ++t) ldsw128(L + L_A + 8192 + (2 * id.w + t) * 1024 + id.lane * 16, cmul_pack(br[t], bi[t], k.tic + 4 * t, k.tis + 4 * t));
  wgbar();
#pragma unroll
  for (int u = 0; u < 8; ++u) a[u] = ldsr128(L + L_A + 8192 + (2 * (u & 3) + (u >> 2)) * 1024 + id.lane * 16);
  float xf[16];
#pragma unroll
  for (int e = 0; e < 4; ++e) { xf[2 * e] = h_lo(xr[e]); xf[2 * e + 1] = h_hi(xr[e]); xf[8 + 2 * e] = h_lo(xi[e]); xf[8 + 2 * e + 1] = h_hi(xi[e]); }
  pin(xf);
  SB();
#pragma unroll
  for (int u = 0; u < 8; ++u) { const int t = u >> 2, ks = u & 3; ar[t] = mfma_h(a[u], k.fr64[ks], ks ? ar[t] : Z4); ai[t] = mfma_h(a[u], k.fi64[ks], ks ? ai[t] : Z4); }
  const float si = jj == 223 ? 0.f : -0.125f;
  v4u o0, o1;
#pragma unroll
  for (int e = 0; e < 4; ++e) {
    const int t = e >> 1, c = 2 * (e & 1);
    o0[e] = pk_h(__builtin_fmaf(ar[t][c], 0.125f, xf[2 * e]), __builtin_fmaf(ar[t][c + 1], 0.125f, xf[2 * e + 1]));
    o1[e] = pk_h(__builtin_fmaf(ai[t][c], si, xf[8 + 2 * e]), __builtin_fmaf(ai[t][c + 1], si, xf[8 + 2 * e + 1]));
  }
  st128<SC1>(o0, rw, xo, XH1_OFF + layer * XB); st128<SC1>(o1, rw, xo + 128, XH1_OFF + layer * XB);
}

template <class Fn> DI void gen_ti(const Ids& id, int j, v4u (&T)[14], Fn side) {
  const float df = (float)(64 * j + 16 * id.w + id.l15) * (1.0f / 448.0f);
  const float o = (float)(16 * (id.w & 1));
  const float be = ((float)(2 * id.g + 1) + o) * df, bo = ((float)(2 * id.g + 1) - o) * df;
#pragma unroll
  for (int m = 0; m < 14; ++m) {
    side(m);
    unsigned u[4];
#pragma unroll
    for (int e = 0; e < 4; ++e) {
      const float x = __builtin_fmaf((float)(16 * m + 8 * (e >> 1) + (e & 1)), df, m & 1 ? bo : be);
      float c = vcos(x);
      if (m >= 12 && e == 3) c *= ((m ^ (id.w & 1)) == 13 && id.g == 3) ? 0.70710678118654752f : 1.0f;
      u[e] = pk_h(c, -vsin(x));
    }
    T[m] = v4u{u[0], u[1], u[2], u[3]};
    pin1(T[m]);
  }
}
DI void s3_issue(const Ids& id, rsrc_t rw, int layer, int i, lds_t L) {
  const int r7 = id.lane >> 3;
  const int vo = r7 * 128 + (((id.lane & 7) ^ (2 * ((r7 >> 1) & 3))) << 4);
#pragma unroll
  for (int n = 0; n < 14; ++n) dma16<SC1>(rw, L + L_A + (4 * n + id.w) * 1024, vo, XH1_OFF + layer * XB + i * 57344 + (4 * n + id.w) * 1024);
}
constexpr int s3_m(int u) { return u < 32 ? u >> 2 : (u < 38 ? 8 + ((u - 32) & ~1) : (u < 44 ? 8 + ((u - 38) & ~1) : 9 + 2 * ((u - 44) >> 2))); }
constexpr int s3_n(int u) { return u < 32 ? u & 3 : (u < 38 ? u & 1 : (u < 44 ? 2 + (u & 1) : u & 3)); }
DI void s3_compute(const Ids& id, rsrc_t rw, int layer, int i, int j, lds_t L, const v4u (&T)[14], f4 (&mu)[4], f4 (&x1)[4]) {
  constexpr int PD = 6;
  const int q = id.l15 >> 2, p = id.l15 & 3, row = 4 * id.g + q, fr = (row >> 1) & 3, ko = (id.w & 1) * 4096;
  lds_t tb[2][4];
#pragma unroll
  for (int n = 0; n < 4; ++n) { lds_t t = L + L_A + row * 128 + 32 * ((n ^ (id.w & 2)) ^ fr) + 8 * p; tb[0][n] = t + ko; tb[1][n] = t - ko; }
  vmwait<6>(); rawbar();
  v4u a[PD];
#pragma unroll
  for (int u = 0; u < PD; ++u) { lds_t t = tb[s3_m(u) & 1][s3_n(u)] + s3_m(u) * 4096; a[u] = cat(trread(t), trread(t + 2048)); }
  SB();
  f4 acc[4], rr[4];
  float e0 = 0.f, e1 = 0.f;
  LAS float* eb = (LAS float*)(L + L_E);
  LAS float* rb = (LAS float*)(L + L_R) + 64 * id.w;
  static_for<0, 56>([&](auto U) {
    constexpr int u = U.v, m = s3_m(u), n = s3_n(u), v = u + PD;
    const v4u cur = a[u % PD];
    acc[n] = mfma_h(cur, T[m], m == 0 ? Z4 : acc[n]);
    if constexpr (n == 0 && !(m & 1)) e0 = sq_h2(cur[3], sq_h2(cur[2], sq_h2(cur[1], sq_h2(cur[0], e0))));
    if constexpr (n == 1 && !(m & 1)) e1 = sq_h2(cur[3], sq_h2(cur[2], sq_h2(cur[1], sq_h2(cur[0], e1))));
    if constexpr (v == 32) { vmwait<0>(); rawbar(); pin(mu); }
    if constexpr (u == 38) {
      e0 = sum32(sum16(e0)); e1 = sum32(sum16(e1));
      eb[64 * (id.w & 1) + 16 * ((id.w & 2) + (id.g & 1)) + id.l15] = id.g & 1 ? e1 : e0;
      SB();
    }
    if constexpr (u == 41) { lgwait<6>(); rawbar(); }
    if constexpr (u == 42) {
      const float vr = (eb[id.lane] + eb[64 + id.lane]) * (1.0f / 448.0f);
      const float r1 = __builtin_amdgcn_rsqf(vr + EPS);
      rb[id.lane] = r1 * __builtin_amdgcn_rsqf(vr * r1 * r1 + EPS) * 0.066815310478106f;
    }
    if constexpr (u == 50) {
#pragma unroll
      for (int t = 0; t < 4; ++t) rr[t] = *(LAS f4*)(rb + 16 * (t ^ (id.w & 2)) + 4 * id.g);
    }
    if constexpr (v < 56) { lds_t t = tb[s3_m(v) & 1][s3_n(v)] + s3_m(v) * 4096; a[u % PD] = cat(trread(t), trread(t + 2048)); }
    SB();
  });
#pragma unroll
  for (int qq = 0; qq < 2; ++qq) {
    const f4 x = acc[2 * qq] * rr[2 * qq], y = acc[2 * qq + 1] * rr[2 * qq + 1];
    st128<SC1>(v4u{pk_bf(x[0], x[1]), pk_bf(x[2], x[3]), pk_bf(y[0], y[1]), pk_bf(y[2], y[3])}, rw, id.lane * 16,
               HF_OFF + layer * XB + ((j * 64 + 2 * i + (qq ^ (id.w >> 1))) * 4 + id.w) * 1024);
  }
  SB();
#pragma unroll
  for (int n = 0; n < 4; ++n) x1[n] = acc[n] * 0.066815310478106f + mu[n];
  pin(x1);
}

template <int NKS, int NR, int NW, class Fn>
DI void g_main(const Ids& id, rsrc_t rw, int abase, lds_t L, const v4u (&R)[NR][4], v4u (&W)[NW][4], f4 (&res)[4], Fn slot) {
  constexpr int WIN = GWIN, NU = 4 * NKS;
  v4u a[WIN];
  const int vo = id.lane * 16;
  int sb[4];
#pragma unroll
  for (int n = 0; n < 4; ++n) sb[n] = abase + id.w * NKS * 4096 + ((((n + 1) & 3) ^ id.w) << 10);
#pragma unroll
  for (int u = 0; u < WIN; ++u) a[u] = ld128<SC1>(rw, vo, sb[u / NKS] + (u % NKS) * 4096);
  SB();
  vmwait<WIN>();
  slot();
  SB();
  f4 part[4];
#pragma unroll
  for (int qn = 0; qn < 4; ++qn) {
    f4 acc[4];
#pragma unroll
    for (int ks = 0; ks < NKS; ++ks) {
      const int u = qn * NKS + ks, v = u - 1 + WIN;
      const v4u cur = a[u % WIN];
#pragma unroll
      for (int f = 0; f < 4; ++f) {
        acc[f] = mfma_bf(ks < NR ? R[ks < NR ? ks : 0][f] : W[ks < NR ? 0 : ks - NR][f], cur, ks == 0 ? Z4 : acc[f]);
        if (f == 0 && u >= 1 && v < NU) { SB(); a[(u - 1) % WIN] = ld128<SC1>(rw, vo, sb[v / NKS] + (v % NKS) * 4096); SB(); }
      }
      if (qn == 3 && ks == 4) {
        wgbar();
#pragma unroll
        for (int f = 0; f < 4; ++f) {
          lds_t rp = L + L_A + (id.w * 12 + (f ^ (id.w & 2))) * 1024 + id.lane * 16;
          part[f] = (asf4(ldsr128(rp)) + asf4(ldsr128(rp + 4096))) + asf4(ldsr128(rp + 8192));
        }
      }
      SB();
    }
    if (qn == 0) wgbar();
    if (qn < 3) {
#pragma unroll
      for (int f = 0; f < 4; ++f) ldsw128(L + L_A + (((id.w ^ (qn + 1)) * 3 + qn) * 4 + (f ^ (id.w & 2))) * 1024 + id.lane * 16, asu4(acc[f]));
    } else {
#pragma unroll
      for (int f = 0; f < 4; ++f) res[f] = acc[f] + part[f];
    }
  }
}
DI void load_bias(const Ids& id, const unsigned short* b, int o, v2u (&bb)[4]) {
#pragma unroll
  for (int f = 0; f < 4; ++f) bb[f] = *(const GAS v2u*)(b + o + 16 * (f ^ (id.w & 2)) + 4 * id.g);
}
DI f4 bias4(v2u b) { return f4{bf_lo(b.x), bf_hi(b.x), bf_lo(b.y), bf_hi(b.y)}; }
DI f4 gelu4(f4 u) {
  const f4 z = u * (u * u * (0.044715f * -2.302208198144325f) + -2.302208198144325f);
  f4 r;
#pragma unroll
  for (int e = 0; e < 4; ++e) r[e] = __builtin_amdgcn_rcpf(1.0f + __builtin_amdgcn_exp2f(z[e]));
  return u * r;
}
template <class Fn> DI void g1_epi(const Ids& id, rsrc_t rw, int layer, int j, int m, const f4 (&res)[4], const f4 (&bb)[4], Fn side) {
  v2u o[4];
#pragma unroll
  for (int f = 0; f < 4; ++f) {
    side(f);
    const f4 a = gelu4(res[f] + bb[f]);
    o[f] = v2u{pk_bf(a[0], a[1]), pk_bf(a[2], a[3])};
    pin1(o[f]);
  }
#pragma unroll
  for (int qq = 0; qq < 2; ++qq)
    st128<SC1>(cat(o[2 * qq], o[2 * qq + 1]), rw, id.lane * 16, GF_OFF + layer * GB + ((j * 56 + 2 * m + (qq ^ (id.w >> 1))) * 4 + id.w) * 1024);
}
DI void g2_epi(const Ids& id, rsrc_t ro, int obase, int i, int j, lds_t L, const f4 (&res)[4], const f4 (&bb)[4]) {
  lds_t T = L + L_A + 49152;
  {
    const int d = 16 * id.w + id.l15, hd = id.l15 ^ (id.w & 1) ^ ((id.w >> 1) << 2);
#pragma unroll
    for (int n = 0; n < 4; ++n) {
      const f4 v = res[n] + bb[n];
      *(LAS v2u*)(T + d * 128 + (((4 * (n ^ (id.w & 2)) + id.g) ^ hd) << 3)) = v2u{pk_bf(v[0], v[1]), pk_bf(v[2], v[3])};
    }
  }
  const int q = id.l15 >> 2, p = id.l15 & 3;
  lds_t tp[2][2];
#pragma unroll
  for (int e = 0; e < 2; ++e)
#pragma unroll
    for (int hh = 0; hh < 2; ++hh) {
      const int sc = 4 * id.w + 2 * e + (id.g >> 1), d = 8 * (4 * (id.g & 1) + p) + 4 * hh + q;
      const int hd = (d & 15) ^ ((d >> 4) & 1) ^ (((d >> 5) & 1) << 2);
      tp[e][hh] = T + d * 128 + ((sc ^ hd) << 3);
    }
  const int vo = (16 * id.w + 4 * (id.lane >> 5) + (id.lane & 3)) * 896 + 16 * ((id.lane >> 2) & 7);
  wgbar();
#pragma unroll
  for (int e = 0; e < 2; ++e) st128<SC1>(cat(trread(tp[e][0]), trread(tp[e][1])), ro, vo, obase + (64 * i + 8 * e) * 896 + j * 128);
}

constexpr int C_EP = 0, C_FL = 1, C_F0 = 16, C_P = C_F0 + 2 * 128, C_S3 = C_P + 2 * 128, C_G1 = C_S3 + 2 * 7 * 32, C_G2 = C_G1 + 2 * 7 * 32, C_LINES = C_G2 + 32 * 8;
static_assert(C_LINES * 128 <= CTRL_BYTES, "control block");
constexpr unsigned SPIN_CAP = 1u << 20;
DI unsigned ld_word(unsigned char* ws, int line) { return __hip_atomic_load((gu32*)ws + 32 * line, __ATOMIC_RELAXED, __HIP_MEMORY_SCOPE_AGENT); }
DI void st_word(unsigned char* ws, int line, unsigned v) { __hip_atomic_store((gu32*)ws + 32 * line, v, __ATOMIC_RELAXED, __HIP_MEMORY_SCOPE_AGENT); }
template <int NS, int NR> DI void seam_arrive(const Ids& id, unsigned char* ws, int line0, int shard) {
  vmwait<0>();
  wgbar();
  if (id.w == 0 && id.lane < NR) __hip_atomic_fetch_add((gu32*)ws + 32 * (line0 + id.lane * NS + shard), 1u, __ATOMIC_RELAXED, __HIP_MEMORY_SCOPE_AGENT);
}
template <int NS> DI void seam_wait(const Ids& id, unsigned char* ws, int line0, int rep, unsigned want) {
  if (id.w == 0) {
    const int line = line0 + rep * NS + (id.lane & (NS - 1));
    for (unsigned sp = 0; sp < SPIN_CAP; ++sp)
      if (__all(ld_word(ws, line) >= want)) break;
    __builtin_amdgcn_fence(__ATOMIC_ACQUIRE, "wavefront");
  }
  wgbar();
}
DI void wait_init(const Ids& id, unsigned char* ws, int rep, unsigned tag) {
  if (id.w == 0) {
    for (unsigned sp = 0; sp < SPIN_CAP; ++sp)
      if (__all(ld_word(ws, C_FL + rep) == tag)) break;
    __builtin_amdgcn_fence(__ATOMIC_ACQUIRE, "wavefront");
  }
}

__global__ void __launch_bounds__(256, 1) k_all(Params p) {
  extern __shared__ __attribute__((aligned(16))) unsigned char smem[];
  lds_t L = (lds_t)smem;
  const Ids id0 = get_ids();
  const int b = blockIdx.x, q = b >> 3, x8 = b & 7;
  const rsrc_t rw = mkrs(p.ws, WS_BYTES);
  STAMP(0);
  unsigned tag = 0;
  if (id0.w == 0) tag = ld_word(p.ws, C_EP) + 1u;
  unsigned wu[16];
  if (q >= 28) {
    if (b == 224) {
      for (int n = C_F0 + (int)threadIdx.x; n < C_LINES; n += 256) st_word(p.ws, n, 0u);
      vmwait<0>(); wgbar();
      if (id0.w == 0 && id0.lane < 8) st_word(p.ws, C_FL + id0.lane, tag);
    }
    wt_issue(p, b, wu);
    if (q == 29) {
      const int t = threadIdx.x;
      unsigned s = 0;
      if (t < WS_BYTES >> 17) s += *(const GAS unsigned*)(p.ws + ((size_t)t << 17) + 65536);
      if (t < 56) { s += *(const GAS unsigned*)((const char*)p.w1 + ((size_t)t << 17)); s += *(const GAS unsigned*)((const char*)p.w2 + ((size_t)t << 17)); }
      if (t < 14) s += *(const GAS unsigned*)((const char*)p.out + ((size_t)t << 17));
      if (t == 0) s += *(const GAS unsigned short*)p.b1 + *(const GAS unsigned short*)p.b2;
      asm volatile("" ::"v"(s));
    }
    vmwait<0>();
    wt_store(rw, b, wu);
    wait_init(id0, p.ws, x8, tag);
    seam_arrive<16, 8>(id0, p.ws, C_F0, b & 15);
    if (b == 224) {
      seam_wait<16>(id0, p.ws, C_F0, x8, 16u);
      if (id0.w == 0 && id0.lane < 9) st_word(p.ws, id0.lane == 8 ? C_EP : C_FL + id0.lane, id0.lane == 8 ? tag : tag - 1u);
    }
    return;
  }
  const int i = x8 + 8 * (q / 7), j = q % 7, m = i;
  const bool g1 = i < 28;
  const int jj = x8 * 28 + q;
#define ID fresh(id0)
#define W1S w_src(ID, p.w1, F * 2, 512, fresh_s(g1 ? m : 0) * 128, L)
#define W2S w_src(ID, p.w2, S * 2, 448, fresh_s(i) * 128, L)
#define STAGE (L + L_W + fresh_s(id0.w) * (WSLOT * 4096))
  v4u R1[NR1][4], R2[NR2][4];
  {
    f0_issue<0>(ID, mkrs(p.x, XB), 0, i, L);
    wt_issue(p, b, wu);
    const WSrc ws = W2S;
    v4u T[14];
    gen_tf(ID, j, T, [&](int ks) { if (ks < 12) { w_piece(ws, 0, 2 * ks, ws.stage); w_piece(ws, 0, 2 * ks + 1, ws.stage); } });
    STAMP(1);
    if (j == 6) f0_compute<true, 40>(ID, rw, 0, i, j, L, T); else f0_compute<false, 40>(ID, rw, 0, i, j, L, T);
    STAMP(2);
    vmwait<0>();
    wt_store(rw, b, wu);
    w_read<NR2, 0>(ID, STAGE, R2);
    wait_init(id0, p.ws, x8, tag);
    STAMP(3);
    seam_arrive<16, 8>(ID, p.ws, C_F0, b & 15);
    STAMP(4);
  }
#pragma unroll 1
  for (int layer = 0; layer < 2; ++layer) {
    const int sb = layer * 30; (void)sb;
    if (layer == 1) {
      v4u T[14];
      gen_tf(ID, j, T, [](int) {});
      STAMP(sb + 0);
      seam_wait<1>(ID, p.ws, C_G2 + i * 8, j, 7u);
      STAMP(sb + 1);
      f0_issue<SC1>(ID, rw, X2_OFF, i, L);
      if (j == 6) f0_compute<true, 0>(ID, rw, 1, i, j, L, T); else f0_compute<false, 0>(ID, rw, 1, i, j, L, T);
      STAMP(sb + 3);
      seam_arrive<16, 8>(ID, p.ws, C_F0 + 128, b & 15);
      STAMP(sb + 4);
    }
    {
      PC k;
      const WSrc ws = W1S;
      gen_pc(ID, k, [&](int n) { if (layer == 0 && n < 8) { w_piece(ws, 0, 2 * n, ws.stage); w_piece(ws, 0, 2 * n + 1, ws.stage); } });
      STAMP(sb + 5);
      seam_wait<16>(ID, p.ws, C_F0 + layer * 128, x8, layer == 0 ? 16u : 14u);
      STAMP(sb + 6);
      p_phase(ID, rw, layer, jj, L, k, [&]() { if (layer == 0) { vmwait<5>(); w_read<NR1, 0>(ID, STAGE, R1); } });
    }
    STAMP(sb + 7);
    seam_arrive<16, 8>(ID, p.ws, C_P + layer * 128, b & 15);
    STAMP(sb + 8);
    v4u W[12][4];
    f4 x1[4];
    {
      f4 mu[4];
      {
        const Ids id = ID;
#pragma unroll
        for (int n = 0; n < 4; ++n) mu[n] = asf4(ld128<SC1>(rw, (16 * (n ^ (id.w & 2)) + 4 * id.g) * 4, MU_OFF + (layer * S + 64 * i) * 4));
      }
      const WSrc ws = W1S;
      v4u T[14];
      gen_ti(ID, j, T, [&](int n) { if (n < 12) { w_piece(ws, NR1, 2 * n, ws.stage); w_piece(ws, NR1, 2 * n + 1, ws.stage); } });
      STAMP(sb + 9);
      seam_wait<16>(ID, p.ws, C_P + layer * 128, x8, 14u);
      STAMP(sb + 10);
      s3_issue(ID, rw, layer, i, L);
      vmwait<14>();
      w_read<6, 0>(ID, STAGE, W);
      STAMP(sb + 11);
      s3_compute(ID, rw, layer, i, j, L, T, mu, x1);
      STAMP(sb + 12);
    }
    seam_arrive<4, 8>(ID, p.ws, C_S3 + (layer * 7 + j) * 32, i & 3);
    STAMP(sb + 13);
    v2u bq[4];
    f4 bb[4], res[4];
    if (g1) {
      load_bias(ID, p.b1, 64 * m, bq);
      { const WSrc ws = W1S; w_issue<0, 24>(ws, NR1 + 6, ws.stage); }
      STAMP(sb + 14);
      seam_wait<4>(ID, p.ws, C_S3 + (layer * 7 + j) * 32, x8, 8u);
#pragma unroll
      for (int f = 0; f < 4; ++f) bb[f] = bias4(bq[f]);
      pin(bb);
      STAMP(sb + 15);
      g_main<16>(ID, rw, HF_OFF + layer * XB + j * 64 * 4096, L, R1, W, res, [&]() { w_read<6, 6>(ID, STAGE, W); });
      STAMP(sb + 16);
      const WSrc ws = W2S;
      g1_epi(ID, rw, layer, j, m, res, bb, [&](int f) { w_piece(ws, NR2, 2 * f, ws.stage); w_piece(ws, NR2, 2 * f + 1, ws.stage); });
      STAMP(sb + 17);
      seam_arrive<4, 8>(ID, p.ws, C_G1 + (layer * 7 + j) * 32, m & 3);
      STAMP(sb + 18);
    } else { const WSrc ws = W2S; w_issue<0, 8>(ws, NR2, ws.stage); }
    load_bias(ID, p.b2, 64 * i, bq);
    { const WSrc ws = W2S; w_issue<8, 24>(ws, NR2, ws.stage); w_issue<0, 8>(ws, NR2 + 6, L + L_A + fresh_s(id0.w) * 8192); }
    STAMP(sb + 19);
    seam_wait<4>(ID, p.ws, C_G1 + (layer * 7 + j) * 32, x8, 7u);
#pragma unroll
    for (int f = 0; f < 4; ++f) bb[f] = bias4(bq[f]) + x1[f];
    pin(bb);
    STAMP(sb + 20);
    g_main<14>(ID, rw, GF_OFF + layer * GB + j * 56 * 4096, L, R2, W, res,
               [&]() { w_read<6, 0>(ID, STAGE, W); w_read<2, 6>(ID, L + L_A + fresh_s(id0.w) * 8192, W); });
    STAMP(sb + 21);
    g2_epi(ID, layer == 0 ? rw : mkrs(p.out, XB), layer == 0 ? X2_OFF : 0, i, j, L, res, bb);
    STAMP(sb + 22);
    if (layer == 0) seam_arrive<1, 7>(ID, p.ws, C_G2 + i * 8, 0);
    STAMP(sb + 23);
  }
}

extern "C" void kernel_launch(void* const* d_in, const int* in_sizes, int n_in, void* d_out, int out_size, void* d_ws, size_t ws_size, hipStream_t stream) {
  Params p;
  p.x = (const unsigned short*)d_in[0]; p.wr = (const unsigned short*)d_in[1]; p.wi = (const unsigned short*)d_in[2];
  p.w1 = (const unsigned short*)d_in[4]; p.b1 = (const unsigned short*)d_in[5]; p.w2 = (const unsigned short*)d_in[6]; p.b2 = (const unsigned short*)d_in[7];
  p.out = (unsigned short*)d_out; p.ws = (unsigned char*)d_ws;
  (void)hipFuncSetAttribute((const void*)k_all, hipFuncAttributeMaxDynamicSharedMemorySize, LDS_BYTES);
  k_all<<<256, 256, LDS_BYTES, stream>>>(p);
}
```

```cpp
#include <hip/hip_runtime.h>
#include <hip/hip_bf16.h>
#include <stdint.h>

#define DI __device__ __forceinline__
#define GAS __attribute__((address_space(1)))
#define LAS __attribute__((address_space(3)))
typedef unsigned v4u __attribute__((ext_vector_type(4)));
typedef unsigned v2u __attribute__((ext_vector_type(2)));
typedef float f4 __attribute__((ext_vector_type(4)));
typedef float f2 __attribute__((ext_vector_type(2)));
typedef _Float16 h8 __attribute__((ext_vector_type(8)));
typedef _Float16 h2 __attribute__((ext_vector_type(2)));
typedef __bf16 b8 __attribute__((ext_vector_type(8)));
typedef __bf16 b2 __attribute__((ext_vector_type(2)));
typedef short s4 __attribute__((ext_vector_type(4)));
typedef __amdgpu_buffer_rsrc_t rsrc_t;
typedef LAS unsigned char* lds_t;
typedef GAS unsigned gu32;

#define GWIN 11
#define CODEWARM 0
#define TDELAY 0
#define L2WARM 1
#define RETOUCH 1
#define EARLY 1

constexpr int S = 2048, D = 448, F = 1792, NKF = 225;
constexpr float EPS = 1e-5f;
constexpr int XB = S * D * 2;
constexpr int GB = D * F * 2;
constexpr int SC1 = 16;
constexpr int CTRL_BYTES = 262144;
constexpr int MU_OFF = CTRL_BYTES;
constexpr int RS_OFF = MU_OFF + 2 * S * 4;
constexpr int WT_OFF = RS_OFF + 2 * S * 2;
constexpr int XH0_OFF = WT_OFF + XB;
constexpr int XH1_OFF = XH0_OFF + 2 * XB;
constexpr int HF_OFF = XH1_OFF + 2 * XB;
constexpr int GF_OFF = HF_OFF + 2 * XB;
constexpr int X2_OFF = GF_OFF + 2 * GB;
constexpr int WS_BYTES = X2_OFF + XB;
static_assert(HF_OFF % 8192 == 0 && XB % 8192 == 0 && GF_OFF % 8192 == 0 && GB % 8192 == 0, "fragment pairs are addressed by an exclusive or");
#define STAMP(n)
#define STV
#define STA
#define STAMP_OUT()
constexpr int L_A = 0, L_E = 57344, L_R = L_E + 512, L_D = L_R + 1024, L_W = 59392, WSTAGE = 24576, LDS_BYTES = L_W + 4 * WSTAGE;
constexpr int NR1 = 6, NR2 = 5;

struct Params {
  const unsigned short* x; const unsigned short* wr; const unsigned short* wi;
  const unsigned short* w1; const unsigned short* b1; const unsigned short* w2; const unsigned short* b2;
  unsigned short* out; unsigned char* ws;
};

DI rsrc_t mkrs(const void* p, int bytes) { return __builtin_amdgcn_make_buffer_rsrc((void*)p, 0, bytes, 0x00020000); }
DI f4 mfma_bf(v4u a, v4u b, f4 c) { return __builtin_amdgcn_mfma_f32_16x16x32_bf16(__builtin_bit_cast(b8, a), __builtin_bit_cast(b8, b), c, 0, 0, 0); }
DI f4 mfma_h(v4u a, v4u b, f4 c) { return __builtin_amdgcn_mfma_f32_16x16x32_f16(__builtin_bit_cast(h8, a), __builtin_bit_cast(h8, b), c, 0, 0, 0); }
DI unsigned pk_bf(float a, float b) { f2 v = {a, b}; return __builtin_bit_cast(unsigned, __builtin_convertvector(v, b2)); }
DI unsigned pk_h(float a, float b) { f2 v = {a, b}; return __builtin_bit_cast(unsigned, __builtin_convertvector(v, h2)); }
DI float h_lo(unsigned u) { return (float)__builtin_bit_cast(h2, u)[0]; }
DI float h_hi(unsigned u) { return (float)__builtin_bit_cast(h2, u)[1]; }
DI float bf_lo(unsigned u) { return __builtin_bit_cast(float, u << 16); }
DI float bf_hi(unsigned u) { return __builtin_bit_cast(float, u & 0xffff0000u); }
DI unsigned mul_h2(unsigned a, unsigned b) { return __builtin_bit_cast(unsigned, __builtin_bit_cast(h2, a) * __builtin_bit_cast(h2, b)); }
DI float vcos(float x) { return __builtin_amdgcn_cosf(x); }
DI float vsin(float x) { return __builtin_amdgcn_sinf(x); }
DI v2u trread(lds_t p) { return __builtin_bit_cast(v2u, __builtin_amdgcn_ds_read_tr16_b64_v4i16((LAS s4*)p)); }
DI v4u cat(v2u a, v2u b) { return v4u{a.x, a.y, b.x, b.y}; }
DI v4u ldsr128(lds_t p) { return *(LAS v4u*)p; }
DI void ldsw128(lds_t p, v4u v) { *(LAS v4u*)p = v; }
template <int AUX> DI void dma16(rsrc_t rs, lds_t dst, int voff, int soff) {
  const unsigned la = (unsigned)(size_t)dst;
  if constexpr (AUX == SC1) asm volatile("s_mov_b32 m0, %0\n\ts_nop 0\n\tbuffer_load_dwordx4 %1, %2, %3 offen sc1 lds" ::"s"(la), "v"(voff), "s"(rs), "s"(soff) : "memory");
  else asm volatile("s_mov_b32 m0, %0\n\ts_nop 0\n\tbuffer_load_dwordx4 %1, %2, %3 offen lds" ::"s"(la), "v"(voff), "s"(rs), "s"(soff) : "memory");
}
DI void ald128(v4u& o, rsrc_t rs, int voff, int soff) { asm volatile("buffer_load_dwordx4 %0, %1, %2, %3 offen sc1" : "=v"(o) : "v"(voff), "s"(rs), "s"(soff) : "memory"); }
DI void touch(rsrc_t rs, lds_t dummy, int voff, int soff) {
  const unsigned la = (unsigned)(size_t)dummy;
  asm volatile("s_mov_b32 m0, %0\n\ts_nop 0\n\tbuffer_load_dword %1, %2, %3 offen lds" ::"s"(la), "v"(voff), "s"(rs), "s"(soff) : "memory");
}
DI void dma4_sc1(rsrc_t rs, lds_t dst, int voff) {
  const unsigned la = (unsigned)(size_t)dst;
  asm volatile("s_mov_b32 m0, %0\n\ts_nop 0\n\tbuffer_load_dword %1, %2, 0 offen sc1 lds" ::"s"(la), "v"(voff), "s"(rs) : "memory");
}
template <int AUX> DI v4u ld128(rsrc_t rs, int voff, int soff = 0) { return __builtin_amdgcn_raw_buffer_load_b128(rs, voff, soff, AUX); }
template <int AUX> DI void st128(v4u v, rsrc_t rs, int voff, int soff = 0) { __builtin_amdgcn_raw_buffer_store_b128(v, rs, voff + soff, 0, AUX); }
DI f4 asf4(v4u v) { return __builtin_bit_cast(f4, v); }
DI v4u asu4(f4 v) { return __builtin_bit_cast(v4u, v); }
template <int N> DI void vmwait() { asm volatile("s_waitcnt vmcnt(%0)" ::"n"(N) : "memory"); }
template <int N> DI void lgwait() { asm volatile("s_waitcnt lgkmcnt(%0)" ::"n"(N) : "memory"); }
DI void rawbar() { asm volatile("" ::: "memory"); __builtin_amdgcn_s_barrier(); asm volatile("" ::: "memory"); }
DI void wgbar() { lgwait<0>(); rawbar(); }
#define SB() __builtin_amdgcn_sched_barrier(0)
template <class T> DI void pin1(T& v) { asm volatile("" : "+v"(v)); }
template <class T, int N> DI void pin(T (&a)[N]) {
#pragma unroll
  for (int n = 0; n < N; ++n) pin1(a[n]);
}
template <int A, int B, int N> DI void pin_acc(v4u (&W)[N][4]) {
#pragma unroll
  for (int ks = A; ks < B; ++ks)
#pragma unroll
    for (int n = 0; n < 4; ++n) asm volatile("" : "+a"(W[ks][n]));
}
constexpr f4 Z4 = {0.f, 0.f, 0.f, 0.f};
template <int V> struct IC { static constexpr int v = V; };
template <int A, int B, class Fn> DI void static_for(Fn&& fn) { if constexpr (A < B) { fn(IC<A>{}); static_for<A + 1, B>(fn); } }

DI float diag(const f4& c, int l15) { const float a = l15 & 1 ? c[1] : c[0], b = l15 & 1 ? c[3] : c[2]; return l15 & 2 ? b : a; }

struct Ids { int lane, w, l15, g; };
DI Ids get_ids() { Ids d; d.lane = threadIdx.x & 63; d.w = __builtin_amdgcn_readfirstlane(threadIdx.x >> 6); d.l15 = d.lane & 15; d.g = d.lane >> 4; return d; }
DI Ids fresh(const Ids& a) { Ids d; d.lane = a.lane; d.w = a.w; asm volatile("" : "+v"(d.lane), "+s"(d.w)); d.l15 = d.lane & 15; d.g = d.lane >> 4; return d; }
DI int fresh_s(int v) { asm volatile("" : "+s"(v)); return v; }

DI int img_src(const Ids& id, int pitch) { const int r7 = id.lane >> 3; return r7 * pitch + (((id.lane & 7) ^ (r7 & 6)) << 4); }
DI int img_rd(const Ids& id, int n) { const int row = 4 * id.g + (id.l15 >> 2); return row * 128 + 32 * (n ^ id.w ^ ((row >> 1) & 3)) + 8 * (id.l15 & 3); }
DI v4u img_frag(lds_t p) { return cat(trread(p), trread(p + 2048)); }
struct WSrc { rsrc_t rs; int pitch, base, vo; lds_t stage; };
DI WSrc w_src(const Ids& id, const void* w, int bytes, int pitch, int rows_per_wave, int col0, lds_t L) {
  return WSrc{mkrs(w, bytes), pitch, id.w * rows_per_wave * pitch + col0, img_src(id, pitch), L + L_W + id.w * WSTAGE};
}
DI void w_piece(const WSrc& ws, int k0, int n, lds_t dst) { dma16<0>(ws.rs, dst + n * 1024, ws.vo, ws.base + (32 * k0 + 8 * n) * ws.pitch); }
template <int N0, int N1> DI void w_issue(const WSrc& ws, int k0, lds_t dst) {
#pragma unroll
  for (int n = N0; n < N1; ++n) w_piece(ws, k0, n, dst);
}
template <int CNT, int O, int N> DI void w_read(const Ids& id, lds_t stage, v4u (&W)[N][4]) {
  lds_t b[4];
#pragma unroll
  for (int n = 0; n < 4; ++n) b[n] = stage + img_rd(id, n);
#pragma unroll
  for (int ks = 0; ks < CNT; ++ks)
#pragma unroll
    for (int n = 0; n < 4; ++n) W[O + ks][n] = img_frag(b[n] + ks * 4096);
}
DI v4u pair_frag(int w, v2u a, v2u b) { const bool sw = w & 1; return v4u{sw ? b.x : a.x, sw ? b.y : a.y, sw ? a.x : b.x, sw ? a.y : b.y}; }

DI void gen_tf(const Ids& id, int j, v4u (&T)[14]) {
  const int c = 64 * j + 16 * id.w + id.l15;
  const bool dc = c == 447;
  const float kf = dc ? 0.f : (float)((c >> 1) + 1) * (1.0f / 448.0f);
  const float base = __builtin_fmaf((float)(8 * id.g), kf, (c & 1) && !dc ? 0.25f : 0.f);
  const float tc = 2.0f * vcos(32.0f * kf);
  f2 v[2][4];
#pragma unroll
  for (int ks = 0; ks < 14; ++ks) {
    unsigned u[4];
#pragma unroll
    for (int e = 0; e < 4; ++e) {
      if (ks < 2) v[ks][e] = f2{vcos(__builtin_fmaf((float)(32 * ks + 2 * e), kf, base)), vcos(__builtin_fmaf((float)(32 * ks + 2 * e + 1), kf, base))};
      else v[ks & 1][e] = v[~ks & 1][e] * tc - v[ks & 1][e];
      u[e] = pk_bf(v[ks & 1][e][0], v[ks & 1][e][1]);
    }
    T[ks] = v4u{u[0], u[1], u[2], u[3]};
    pin1(T[ks]);
  }
}
template <int AUX> DI void f0_issue(const Ids& id, rsrc_t rx, int xbase, int i, lds_t L) {
  const int r7 = id.lane >> 3;
  const int vo = r7 * 896 + (((id.lane & 7) ^ (2 * id.w + ((r7 >> 1) & 1))) << 4);
#pragma unroll
  for (int n = 0; n < 14; ++n) dma16<AUX>(rx, L + L_A + (4 * n + id.w) * 1024, vo, xbase + (64 * i + 32 * (n & 1) + 8 * id.w) * 896 + (n >> 1) * 128);
}
DI void f0_compute(const Ids& id, rsrc_t rw, int layer, int i, int j, lds_t L, const v4u (&T)[14]) {
  constexpr int YOUNG = 0, YOUNG0 = 3;
  constexpr int PD = 8;
  const int rho = id.l15, xr = (rho >> 1) & 7;
  const int rowb = (8 * (rho >> 2) + (rho & 3)) * 128, tw = 4096 * (id.w >> 1) + 512 * (id.w & 1);
  lds_t pa[2][4];
#pragma unroll
  for (int n = 0; n < 4; ++n) {
    const int o = rowb + ((4096 * (n >> 1) + 512 * (n & 1)) ^ tw);
    pa[0][n] = L + L_A + o + ((id.g ^ xr) << 4); pa[1][n] = L + L_A + o + (((id.g ^ xr) << 4) ^ 64);
  }
  if (layer == 0) vmwait<6 + YOUNG0>(); else vmwait<6 + YOUNG>();
  rawbar();
  v4u a[PD];
#pragma unroll
  for (int u = 0; u < PD; ++u) a[u] = ldsr128(pa[(u >> 2) & 1][u & 3] + (u >> 3) * 8192);
  SB();
  f4 acc[4];
  f4 gram = Z4;
#pragma unroll
  for (int u = 0; u < 56; ++u) {
    const int ks = u >> 2, n = u & 3, v = u + PD;
    const v4u cur = a[u % PD];
    acc[n] = mfma_bf(cur, T[ks], ks == 0 ? Z4 : acc[n]);
    if (n == 0) gram = mfma_bf(cur, cur, gram);
    if (v == 32) { if (layer == 0) vmwait<YOUNG0>(); else vmwait<YOUNG>(); rawbar(); }
    if (v < 56) a[u % PD] = ldsr128(pa[(v >> 2) & 1][v & 3] + (v >> 3) * 8192);
    SB();
  }
  const int c = 64 * j + 16 * id.w + id.l15;
  LAS float* eb = (LAS float*)(L + L_E);
  if (id.g == rho >> 2) eb[32 * (id.w >> 1) + 4 * (id.w & 1) + 8 * (rho >> 2) + (rho & 3)] = diag(gram, rho);
  if (id.w == 3 && rho == 15) {
#pragma unroll
    for (int n = 0; n < 4; ++n) *(LAS f4*)(eb + 64 + 32 * ((n ^ 3) >> 1) + 4 * ((n ^ 3) & 1) + 8 * id.g) = acc[n];
  }
  SB();
  const float sc = c == 447 ? 0.f : (c == 446 ? 0.047245559126153f : 0.066815310478106f);
#pragma unroll
  for (int q = 0; q < 2; ++q) {
    const f4 x = acc[2 * q] * sc, y = acc[2 * q + 1] * sc;
    const unsigned x0 = pk_h(x[0], x[1]), x1 = pk_h(x[2], x[3]), y0 = pk_h(y[0], y[1]), y1 = pk_h(y[2], y[3]);
    const bool sw = id.w & 1;
    st128<SC1>(v4u{sw ? y0 : x0, sw ? y1 : x1, sw ? x0 : y0, sw ? x1 : y1}, rw, c * 128 + 16 * id.g, XH0_OFF + layer * XB + i * 57344 + 64 * (q ^ (id.w >> 1)));
  }
  vmwait<2>();
  wgbar();
  if (j == 6 && id.w == 0) {
    const float m = eb[64 + id.lane] * (1.0f / 448.0f);
    const float r = __builtin_amdgcn_rsqf(__builtin_fmaxf(__builtin_fmaf(-m, m, eb[id.lane] * (1.0f / 448.0f)), 0.f) + EPS);
    __builtin_amdgcn_raw_buffer_store_b32(__builtin_bit_cast(unsigned, m), rw, id.lane * 4, MU_OFF + (layer * S + 64 * i) * 4, SC1);
    __builtin_amdgcn_raw_buffer_store_b16((unsigned short)(pk_h(r, r) & 0xffffu), rw, id.lane * 2, RS_OFF + (layer * S + 64 * i) * 2, SC1);
  }
}
constexpr int WT_LD = 3;
DI void wt_issue(const Params& p, const Ids& id, int kg, int g, int cg, lds_t L) {
  const rsrc_t rs = mkrs(id.w < 2 ? p.wr : p.wi, S * NKF * 2);
#pragma unroll
  for (int n = 0; n < WT_LD; ++n) {
    const int pc = (id.w & 1) * 192 + n * 64 + id.lane, row = pc / 5, ch = pc - 5 * row;
    const int vo = ((((row & 7) + 64 * ((row >> 3) & 7)) * NKF + 1) * 2 & ~3) + 16 * ch;
    dma16<0>(rs, L + L_W + (id.w >> 1) * 8192 + ((id.w & 1) * 3 + n) * 1024, vo, ((8 * kg + 512 * g) * NKF + 32 * cg) * 2);
  }
}
DI void wt_store(rsrc_t rw, int kg, int g, int cg, lds_t L) {
  int t = threadIdx.x;
  pin1(t);
  const LAS unsigned short* q = (const LAS unsigned short*)(L + L_W + (t & 7) * 80 + (t >> 3) * 2 + (~t & 1) * 2);
  unsigned u[8];
#pragma unroll
  for (int m = 0; m < 8; ++m) u[m] = (unsigned)q[320 * m] | ((unsigned)q[320 * m + 4096] << 16);
  const int so = WT_OFF + 32 * cg * 8192 + (kg >> 1) * 1024 + (g * 16 + (kg & 1) * 8) * 16, vo = (t >> 3) * 8192 + (t & 7) * 16;
  st128<SC1>(v4u{u[0], u[1], u[2], u[3]}, rw, vo, so);
  st128<SC1>(v4u{u[4], u[5], u[6], u[7]}, rw, vo + 4096, so);
}

struct PC { v4u fr64[4], fi64[4], fr32[4], fi32[4]; float twc[8], tws[8], tic[8], tis[8]; };
DI void cs_tuple(const float (&x)[4], v4u& re, v4u& im) {
  float c[4], s[4];
#pragma unroll
  for (int e = 0; e < 4; ++e) { c[e] = vcos(x[e]); s[e] = vsin(x[e]); }
  const unsigned c0 = pk_h(c[0], c[1]), c1 = pk_h(c[2], c[3]), s0 = pk_h(s[0], s[1]), s1 = pk_h(s[2], s[3]);
  re = v4u{c0, c1, s0, s1}; im = v4u{s0 ^ 0x80008000u, s1 ^ 0x80008000u, c0, c1};
  pin1(re); pin1(im);
}
DI void gen_pc_a(const Ids& id, PC& k) {
  const float a64 = (float)(16 * id.w + id.l15) * (1.0f / 64.0f), g4 = (float)(4 * id.g);
#pragma unroll
  for (int ks = 0; ks < 4; ++ks) {
    float x[4];
#pragma unroll
    for (int e = 0; e < 4; ++e) x[e] = (g4 + (float)(16 * ks + e)) * a64;
    cs_tuple(x, k.fr64[ks], k.fi64[ks]);
  }
  const float pl = (float)(8 * (id.l15 >> 2) + (id.l15 & 3)), g8 = (float)(8 * id.g);
#pragma unroll
  for (int t = 0; t < 2; ++t) {
    float x[4];
#pragma unroll
    for (int e = 0; e < 4; ++e) x[e] = (g8 + (float)(4 * (t & 1) + e)) * (pl * (1.0f / 32.0f));
    cs_tuple(x, k.fr32[t], k.fi32[t]);
  }
}
DI void gen_pc_b(const Ids& id, PC& k) {
  const float pl = (float)(8 * (id.l15 >> 2) + (id.l15 & 3)), g8 = (float)(8 * id.g);
#pragma unroll
  for (int t = 2; t < 4; ++t) {
    float x[4];
#pragma unroll
    for (int e = 0; e < 4; ++e) x[e] = (g8 + (float)(4 * (t & 1) + e)) * ((pl + 4.0f) * (1.0f / 32.0f));
    cs_tuple(x, k.fr32[t], k.fi32[t]);
  }
  const float k1a = (float)(16 * id.w + id.l15) * (1.0f / 2048.0f), k1b = (float)(16 * id.w + 4 * id.g);
#pragma unroll
  for (int m = 0; m < 8; ++m) {
    const float x = (g8 + (float)m) * k1a;
    k.twc[m] = vcos(x) * 0.125f; k.tws[m] = vsin(x) * 0.125f;
    const float y = (k1b + (float)(m & 3)) * ((pl + (float)(4 * (m >> 2))) * (1.0f / 2048.0f));
    k.tic[m] = vcos(y) * 0.17677669529663687f; k.tis[m] = vsin(y) * 0.17677669529663687f;
    pin1(k.twc[m]); pin1(k.tws[m]); pin1(k.tic[m]); pin1(k.tis[m]);
  }
}
DI v4u cmul_pack(const f4& a, const f4& b, const float* c, const float* s) {
  float u[4], v[4];
#pragma unroll
  for (int e = 0; e < 4; ++e) { u[e] = a[e] * c[e] + b[e] * s[e]; v[e] = b[e] * c[e] - a[e] * s[e]; }
  return v4u{pk_h(u[0], u[1]), pk_h(u[2], u[3]), pk_h(v[0], v[1]), pk_h(v[2], v[3])};
}
template <class Fn> DI void p_phase(const Ids& id, rsrc_t rw, int layer, int jj, lds_t L, const PC& k, Fn slot STV) {
  const int s0 = 512 * id.w + 32 * id.l15 + 8 * id.g;
  const int xo = (s0 >> 6) * 57344 + (s0 & 63) * 2 + jj * 256;
  v4u rs, xr, xi, wt[2];
  ald128(rs, rw, s0 * 2, RS_OFF + layer * S * 2);
  ald128(xr, rw, xo, XH0_OFF + layer * XB); ald128(xi, rw, xo + 128, XH0_OFF + layer * XB);
  ald128(wt[0], rw, id.lane * 16, WT_OFF + jj * 8192 + id.w * 1024); ald128(wt[1], rw, id.lane * 16, WT_OFF + jj * 8192 + id.w * 1024 + 4096);
  const int tr = 4 * id.g + (id.l15 >> 2), to = tr * 64 + (((id.l15 & 3) ^ (((tr >> 1) & 1) | ((tr >> 2) & 2))) << 4), th = (tr << 1) & 8;
  lds_t tp[2] = {L + L_A + to + th, L + L_A + to + (th ^ 8)};
  slot();
  vmwait<2>(); SB();
  STAMP(54);
  pin1(rs); pin1(xr); pin1(xi);
  {
    const int zc = (16 * id.w + id.l15) * 64 + ((id.g ^ (((id.l15 >> 1) & 1) | ((id.l15 >> 2) & 2))) << 4), zh = (id.l15 << 1) & 8;
    LAS v2u* z0 = (LAS v2u*)(L + L_A + zc + zh); LAS v2u* z1 = (LAS v2u*)(L + L_A + zc + (zh ^ 8));
    z0[0] = v2u{mul_h2(xr[0], rs[0]), mul_h2(xr[1], rs[1])}; z1[0] = v2u{mul_h2(xr[2], rs[2]), mul_h2(xr[3], rs[3])};
    z0[512] = v2u{mul_h2(xi[0], rs[0]), mul_h2(xi[1], rs[1])}; z1[512] = v2u{mul_h2(xi[2], rs[2]), mul_h2(xi[3], rs[3])};
  }
  wgbar();
  STAMP(55);
  v4u a[8];
#pragma unroll
  for (int u = 0; u < 8; ++u) a[u] = cat(trread(tp[u >> 2] + (u & 3) * 1024), trread(tp[u >> 2] + 4096 + (u & 3) * 1024));
  SB();
  f4 ar[2], ai[2];
#pragma unroll
  for (int u = 0; u < 8; ++u) { const int t = u >> 2, ks = u & 3; ar[t] = mfma_h(a[u], k.fr64[ks], ks ? ar[t] : Z4); ai[t] = mfma_h(a[u], k.fi64[ks], ks ? ai[t] : Z4); }
  v4u bq[2];
#pragma unroll
  for (int t = 0; t < 2; ++t) bq[t] = cmul_pack(ar[t], ai[t], k.twc + 4 * t, k.tws + 4 * t);
  STAMP(56);
  vmwait<0>(); SB(); pin(wt);
  STAMP(57);
  float wr[8], wi[8];
#pragma unroll
  for (int m = 0; m < 8; ++m) { const unsigned t = wt[m >> 2][m & 3]; wr[m] = bf_lo(t) * 0.17677669529663687f; wi[m] = bf_hi(t) * -0.17677669529663687f; }
  v4u yq[2];
  f4 yr[2], yi[2];
#pragma unroll
  for (int tm = 0; tm < 2; ++tm) {
    yr[tm] = mfma_h(k.fr32[2 * tm], bq[0], Z4); yi[tm] = mfma_h(k.fi32[2 * tm], bq[0], Z4);
  }
#pragma unroll
  for (int tm = 0; tm < 2; ++tm) {
    yr[tm] = mfma_h(k.fr32[2 * tm + 1], bq[1], yr[tm]); yi[tm] = mfma_h(k.fi32[2 * tm + 1], bq[1], yi[tm]);
  }
#pragma unroll
  for (int tm = 0; tm < 2; ++tm) {
    float u[4], v[4];
#pragma unroll
    for (int e = 0; e < 4; ++e) { u[e] = yr[tm][e] * wr[4 * tm + e] + yi[tm][e] * wi[4 * tm + e]; v[e] = yr[tm][e] * wi[4 * tm + e] - yi[tm][e] * wr[4 * tm + e]; }
    yq[tm] = v4u{pk_h(u[0], u[1]), pk_h(u[2], u[3]), pk_h(v[0], v[1]), pk_h(v[2], v[3])};
  }
  f4 br[2], bi[2];
#pragma unroll
  for (int t = 0; t < 2; ++t) { br[t] = mfma_h(yq[0], k.fr32[2 * t], Z4); bi[t] = mfma_h(yq[0], k.fi32[2 * t], Z4); }
#pragma unroll
  for (int t = 0; t < 2; ++t) { br[t] = mfma_h(yq[1], k.fr32[2 * t + 1], br[t]); bi[t] = mfma_h(yq[1], k.fi32[2 * t + 1], bi[t]); }
#pragma unroll
  for (int t = 0; t < 2; ++t) ldsw128(L + L_A + 8192 + (2 * id.w + t) * 1024 + id.lane * 16, cmul_pack(br[t], bi[t], k.tic + 4 * t, k.tis + 4 * t));
  wgbar();
  STAMP(58);
#pragma unroll
  for (int u = 0; u < 8; ++u) a[u] = ldsr128(L + L_A + 8192 + (2 * (u & 3) + (u >> 2)) * 1024 + id.lane * 16);
  float xf[16];
#pragma unroll
  for (int e = 0; e < 4; ++e) { xf[2 * e] = h_lo(xr[e]); xf[2 * e + 1] = h_hi(xr[e]); xf[8 + 2 * e] = h_lo(xi[e]); xf[8 + 2 * e + 1] = h_hi(xi[e]); }
  pin(xf);
  SB();
#pragma unroll
  for (int u = 0; u < 8; ++u) { const int t = u >> 2, ks = u & 3; ar[t] = mfma_h(a[u], k.fr64[ks], ks ? ar[t] : Z4); ai[t] = mfma_h(a[u], k.fi64[ks], ks ? ai[t] : Z4); }
  const float si = jj == 223 ? 0.f : -0.125f;
  v4u o0, o1;
#pragma unroll
  for (int e = 0; e < 4; ++e) {
    const int t = e >> 1, c = 2 * (e & 1);
    o0[e] = pk_h(__builtin_fmaf(ar[t][c], 0.125f, xf[2 * e]), __builtin_fmaf(ar[t][c + 1], 0.125f, xf[2 * e + 1]));
    o1[e] = pk_h(__builtin_fmaf(ai[t][c], si, xf[8 + 2 * e]), __builtin_fmaf(ai[t][c + 1], si, xf[8 + 2 * e + 1]));
  }
  st128<SC1>(o0, rw, xo, XH1_OFF + layer * XB); st128<SC1>(o1, rw, xo + 128, XH1_OFF + layer * XB);
}

DI void gen_ti(const Ids& id, int j, v4u (&T)[14]) {
  const float df = (float)(64 * j + 16 * id.w + id.l15) * (1.0f / 448.0f);
  const float base = (float)(2 * id.g + 1) * df;
  const float tc = 2.0f * vcos(16.0f * df);
  f2 v[2][4];
#pragma unroll
  for (int ks = 0; ks < 14; ++ks) {
    unsigned u[4];
#pragma unroll
    for (int e = 0; e < 4; ++e) {
      if (ks < 2) { const float x = __builtin_fmaf((float)(16 * ks + 8 * (e >> 1) + (e & 1)), df, base); v[ks][e] = f2{vcos(x), -vsin(x)}; }
      else v[ks & 1][e] = v[~ks & 1][e] * tc - v[ks & 1][e];
      float c = v[ks & 1][e][0];
      if (ks == 13 && e == 3) c *= id.g == 3 ? 0.70710678118654752f : 1.0f;
      u[e] = pk_h(c, v[ks & 1][e][1]);
    }
    T[ks] = v4u{u[0], u[1], u[2], u[3]};
    pin1(T[ks]);
  }
}
DI void s3_issue(const Ids& id, rsrc_t rw, int layer, int i, lds_t L) {
  const int vo = img_src(id, 128);
#pragma unroll
  for (int n = 0; n < 14; ++n) dma16<SC1>(rw, L + L_A + (4 * n + id.w) * 1024, vo, XH1_OFF + layer * XB + i * 57344 + (4 * n + id.w) * 1024);
}
constexpr int s3_k(int u) { return u < 36 ? u >> 2 : (u < 41 ? u - 27 : 9 + (u - 41) / 3); }
constexpr int s3_n(int u) { return u < 36 ? u & 3 : (u < 41 ? 0 : 1 + (u - 41) % 3); }
DI void s3_compute(const Ids& id, rsrc_t rw, int layer, int i, int j, lds_t L, const v4u (&T)[14], f4 (&mu)[4], f4 (&x1)[4] STV) {
  constexpr int PD = 5;
  lds_t tb[4];
#pragma unroll
  for (int n = 0; n < 4; ++n) tb[n] = L + L_A + img_rd(id, n);
  LAS float* ew = id.g == id.l15 >> 2 ? (LAS float*)(L + L_E) + 16 * id.w + id.l15 : (LAS float*)(L + L_D) + id.l15;
  LAS float* er[2] = {(LAS float*)(L + L_E) + 16 * id.w + 4 * id.g, (LAS float*)(L + L_E) + 16 * (id.w ^ 1) + 4 * id.g};
  const int e2 = (id.w & 2) ? -32 : 32;
  int so = id.lane * 16 + HF_OFF + layer * XB + ((j * 64 + 2 * i + (id.w >> 1)) * 4 + id.w) * 1024;
  pin1(ew); pin(er); pin1(so);
  vmwait<5>(); rawbar();
  STAMP(59);
  v4u a[PD];
#pragma unroll
  for (int u = 0; u < PD; ++u) a[u] = img_frag(tb[s3_n(u)] + s3_k(u) * 4096);
  SB();
  f4 acc[4], gram, rr[4];
  static_for<0, 56>([&](auto U) {
    constexpr int u = U.v, k = s3_k(u), n = s3_n(u), v = u + PD;
    const v4u cur = a[u % PD];
    if constexpr (n == 0) gram = mfma_h(cur, cur, k == 0 ? Z4 : gram);
    acc[n] = mfma_h(cur, T[k], k == 0 ? Z4 : acc[n]);
    asm volatile("" : "+a"(acc[n]));
    if constexpr (v == 36) { STAMP(60); vmwait<0>(); rawbar(); pin(mu); STAMP(61); }
    if constexpr (v < 56) a[u % PD] = img_frag(tb[s3_n(v)] + s3_k(v) * 4096);
    if constexpr (u == 42) {
      SB();
      const float vr = diag(gram, id.l15) * (1.0f / 448.0f);
      const float r1 = __builtin_amdgcn_rsqf(vr + EPS);
      *ew = r1 * __builtin_amdgcn_rsqf(vr * r1 * r1 + EPS) * 0.066815310478106f;
    }
    if constexpr (u == 47) {
      lgwait<10>(); rawbar();
      rr[0] = *(LAS f4*)er[0]; rr[1] = *(LAS f4*)er[1]; rr[2] = *(LAS f4*)(er[0] + e2); rr[3] = *(LAS f4*)(er[1] + e2);
    }
    SB();
  });
  STAMP(62);
  v2u hq[4];
#pragma unroll
  for (int n = 0; n < 4; ++n) { const f4 x = acc[n] * rr[n]; hq[n] = v2u{pk_bf(x[0], x[1]), pk_bf(x[2], x[3])}; }
  st128<SC1>(pair_frag(id.w, hq[0], hq[1]), rw, so);
  st128<SC1>(pair_frag(id.w, hq[2], hq[3]), rw, so ^ 4096);
  SB();
#pragma unroll
  for (int n = 0; n < 4; ++n) x1[n] = acc[n] * 0.066815310478106f + mu[n];
  pin(x1);
}

template <int NKS, int W0, int SO, int NS, int NX, int PRE, int NR, class Fi, class Fs>
DI void g_main(const Ids& id, rsrc_t rw, int abase, lds_t L, const v4u (&R)[NR][4], v4u (&W)[10][4], f4 (&res)[4], Fi init, Fs slot) {
  constexpr int WIN = GWIN, NU = 4 * NKS;
  v4u a[WIN];
  const int vo = id.lane * 16;
  int sb[4];
#pragma unroll
  for (int n = 0; n < 4; ++n) sb[n] = abase + id.w * NKS * 4096 + ((((n + 1) & 3) ^ id.w) << 10);
#pragma unroll
  for (int u = 0; u < WIN; ++u) a[u] = ld128<SC1>(rw, vo, sb[u / NKS] + (u % NKS) * 4096);
  lds_t wa[4], wb[4], rd[4];
#pragma unroll
  for (int n = 0; n < 4; ++n) {
    const int o = img_rd(id, n);
    wa[n] = L + L_W + id.w * WSTAGE + SO + o; wb[n] = L + L_A + id.w * 12288 + o; rd[n] = L + L_A + ((n ^ id.w) << 10) + vo;
  }
  const auto wrd = [&](int fi) { const int k = fi >> 2, n = fi & 3; W[W0 + k][n] = img_frag(k < NS ? wa[n] + k * 4096 : wb[n] + (k - NS) * 4096); };
  SB();
  vmwait<WIN>();
#pragma unroll
  for (int fi = 0; fi < 4 * PRE; ++fi) wrd(fi);
  slot();
  SB();
  f4 part[4], acc[4];
  v4u t0, t1, t2;
  static_for<0, NU>([&](auto U) {
    constexpr int u = U.v, qn = u / NKS, ks = u % NKS, v = u - 1 + WIN;
    const v4u cur = a[u % WIN];
    const auto mm = [&](int f) { if constexpr (ks < NR) acc[f] = mfma_bf(R[ks][f], cur, ks == 0 ? Z4 : acc[f]); else acc[f] = mfma_bf(W[ks - NR][f], cur, acc[f]); };
    mm(0);
    if constexpr (u >= 1 && v < NU) { SB(); a[(u - 1) % WIN] = ld128<SC1>(rw, vo, sb[v / NKS] + (v % NKS) * 4096); SB(); }
    mm(1); mm(2); mm(3);
    asm volatile("" : "+a"(acc[0]), "+a"(acc[1]), "+a"(acc[2]), "+a"(acc[3]));
    if constexpr (4 * PRE + 2 * u < 4 * (NS + NX)) { wrd(4 * PRE + 2 * u); wrd(4 * PRE + 2 * u + 1); }
    if constexpr (qn == 3 && ks == 3) wgbar();
    if constexpr (qn == 3 && ks >= 4 && ks < 8) { part[ks - 4] = (asf4(t0) + asf4(t1)) + (asf4(t2) + init(ks - 4)); pin1(part[ks - 4]); }
    if constexpr (qn == 3 && ks >= 3 && ks < 7) { lds_t rp = rd[ks - 3] + id.w * 12288; t0 = ldsr128(rp); t1 = ldsr128(rp + 4096); t2 = ldsr128(rp + 8192); }
    if constexpr (ks == NKS - 1 && qn < 3) {
      if constexpr (qn == 0) wgbar();
#pragma unroll
      for (int f = 0; f < 4; ++f) ldsw128(rd[f] + ((id.w ^ (qn + 1)) * 3 + qn) * 4096, asu4(acc[f]));
    }
    SB();
  });
#pragma unroll
  for (int f = 0; f < 4; ++f) res[f] = acc[f] + part[f];
}
DI void load_bias(const Ids& id, const unsigned short* b, int o, v2u (&bb)[4]) {
#pragma unroll
  for (int f = 0; f < 4; ++f) bb[f] = *(const GAS v2u*)(b + o + 16 * (f ^ id.w) + 4 * id.g);
}
DI f4 bias4(v2u b) { return f4{bf_lo(b.x), bf_hi(b.x), bf_lo(b.y), bf_hi(b.y)}; }
DI f4 gelu4(f4 u) {
  const f4 z = u * (u * u * (0.044715f * -2.302208198144325f) + -2.302208198144325f);
  f4 r;
#pragma unroll
  for (int e = 0; e < 4; ++e) r[e] = __builtin_amdgcn_rcpf(1.0f + __builtin_amdgcn_exp2f(z[e]));
  return u * r;
}
template <class Fn> DI void g1_epi(const Ids& id, rsrc_t rw, int layer, int j, int m, const f4 (&res)[4], Fn side) {
  v2u o[4];
#pragma unroll
  for (int q = 0; q < 4; ++q) {
    side(q);
    const f4 a = gelu4(res[q]);
    o[q] = v2u{pk_bf(a[0], a[1]), pk_bf(a[2], a[3])};
    pin1(o[q]);
  }
  const int so = id.lane * 16 + GF_OFF + layer * GB + ((j * 56 + 2 * m + (id.w >> 1)) * 4 + id.w) * 1024;
  st128<SC1>(pair_frag(id.w, o[0], o[1]), rw, so);
  st128<SC1>(pair_frag(id.w, o[2], o[3]), rw, so ^ 4096);
}
struct G2A { lds_t wp[4], tp[4]; int vo; };
DI G2A g2_addr(const Ids& id, lds_t L) {
  G2A a;
  lds_t T = L + L_A + 49152;
  const int d0 = 16 * id.w + id.l15, h0 = (d0 & 15) ^ ((d0 >> 4) & 1) ^ (((d0 >> 5) & 1) << 2), q = id.l15 >> 2, p = id.l15 & 3;
#pragma unroll
  for (int n = 0; n < 4; ++n) {
    a.wp[n] = T + d0 * 128 + (((4 * (n ^ id.w) + id.g) ^ h0) << 3);
    const int sc = 4 * id.w + (n & 2) + (id.g >> 1), d = 8 * (4 * (id.g & 1) + p) + 4 * (n & 1) + q;
    a.tp[n] = T + d * 128 + ((sc ^ (d & 15) ^ ((d >> 4) & 1) ^ (((d >> 5) & 1) << 2)) << 3);
  }
  a.vo = (16 * id.w + 4 * (id.lane >> 5) + (id.lane & 3)) * 896 + 16 * ((id.lane >> 2) & 7);
  pin(a.wp); pin(a.tp); pin1(a.vo);
  return a;
}
DI void g2_epi(const G2A& a, rsrc_t ro, int obase, const f4 (&res)[4]) {
#pragma unroll
  for (int q = 0; q < 4; ++q) *(LAS v2u*)a.wp[q] = v2u{pk_bf(res[q][0], res[q][1]), pk_bf(res[q][2], res[q][3])};
  wgbar();
  const v4u o0 = cat(trread(a.tp[0]), trread(a.tp[1])), o1 = cat(trread(a.tp[2]), trread(a.tp[3]));
  SB();
  st128<SC1>(o0, ro, a.vo, obase); st128<SC1>(o1, ro, a.vo, obase + 8 * 896);
}

constexpr int C_EP = 0, C_FL = 1, C_H = 15, C_F0 = 16, C_P = C_F0 + 2 * 128, C_S3 = C_P + 2 * 128, C_G1 = C_S3 + 2 * 7 * 32, C_G2 = C_G1 + 2 * 7 * 32, C_LINES = C_G2 + 32 * 8;
static_assert(C_LINES * 128 <= CTRL_BYTES && C_LINES <= C_F0 + 128 + 7 * 256, "control block");
constexpr unsigned SPIN_CAP = 1u << 20;
DI unsigned ld_word(unsigned char* ws, int line) { return __hip_atomic_load((gu32*)ws + 32 * line, __ATOMIC_RELAXED, __HIP_MEMORY_SCOPE_AGENT); }
DI void st_word(unsigned char* ws, int line, unsigned v) { __hip_atomic_store((gu32*)ws + 32 * line, v, __ATOMIC_RELAXED, __HIP_MEMORY_SCOPE_AGENT); }
template <int NS, int NR, int YOUNG = 0, class Fn> DI void seam_arrive(const Ids& id, unsigned char* ws, int line0, int shard, Fn work, bool on = true) {
  SB(); work(); SB();
  vmwait<YOUNG>();
  wgbar();
  if (on && id.w == 0 && id.lane < NR) __hip_atomic_fetch_add((gu32*)ws + 32 * (line0 + id.lane * NS + shard), 1u, __ATOMIC_RELAXED, __HIP_MEMORY_SCOPE_AGENT);
}
template <int NS> DI void seam_wait(const Ids& id, unsigned char* ws, int line0, int rep, unsigned want) {
  if (id.w == 0) {
    const int line = line0 + rep * NS + (id.lane & (NS - 1));
    for (unsigned sp = 0; sp < SPIN_CAP; ++sp)
      if (__all(ld_word(ws, line) >= want)) break;
    __builtin_amdgcn_fence(__ATOMIC_ACQUIRE, "wavefront");
  }
  wgbar();
}
template <int NS> DI void poll_early(const Ids& id, rsrc_t rw, lds_t L, int line0, int rep) {
  if (EARLY && id.w == 0) dma4_sc1(rw, L + L_D, 128 * (line0 + rep * NS + (id.lane & (NS - 1))));
}
template <int NS, int YOUNG> DI void seam_wait_early(const Ids& id, unsigned char* ws, lds_t L, int line0, int rep, unsigned want) {
  if (id.w == 0) {
    bool ok = false;
    if (EARLY) { vmwait<YOUNG>(); ok = __all(((volatile LAS unsigned*)(L + L_D))[id.lane] >= want); }
    if (!ok) {
      const int line = line0 + rep * NS + (id.lane & (NS - 1));
      for (unsigned sp = 0; sp < SPIN_CAP; ++sp)
        if (__all(ld_word(ws, line) >= want)) break;
    }
    __builtin_amdgcn_fence(__ATOMIC_ACQUIRE, "wavefront");
  }
  wgbar();
}
DI void wait_init(const Ids& id, unsigned char* ws, int rep, unsigned tag) {
  if (id.w == 0) {
    for (unsigned sp = 0; sp < SPIN_CAP; ++sp)
      if (__all(ld_word(ws, C_FL + rep) == tag)) break;
    __builtin_amdgcn_fence(__ATOMIC_ACQUIRE, "wavefront");
  }
}

DI void poll_ge(unsigned char* ws, int line, unsigned want) {
  for (unsigned sp = 0; sp < SPIN_CAP; ++sp) {
    if (__all(ld_word(ws, line) >= want)) break;
    __builtin_amdgcn_s_sleep(32);
  }
}

__global__ void __launch_bounds__(256, 1) k_all(Params p) {
  extern __shared__ __attribute__((aligned(16))) unsigned char smem[];
  const unsigned long long pc0 = __builtin_amdgcn_s_getpc();
  lds_t L = (lds_t)smem;
  const Ids id0 = get_ids();
  const int b = blockIdx.x, q = b >> 3, x8 = b & 7;
  const rsrc_t rw = mkrs(p.ws, WS_BYTES);
  const auto nop = []() {};
  STAMP(0);
  const unsigned want0 = (id0.lane & 15) < 8 ? 15u : 14u;
  if (q == 28) {
    const unsigned tag = ld_word(p.ws, C_EP) + 1u;
    if (b == 224) {
      if (id0.w == 0) {
        st_word(p.ws, C_F0 + id0.lane, 0u); st_word(p.ws, C_F0 + 64 + id0.lane, 0u);
        if (id0.lane == 0) st_word(p.ws, C_H, 0u);
        vmwait<0>();
        STAMP(27);
        if (id0.lane < 8) st_word(p.ws, C_FL + id0.lane, tag);
        vmwait<0>();
        if (id0.lane < 8) __hip_atomic_fetch_add((gu32*)p.ws + 32 * (C_F0 + id0.lane * 16), 1u, __ATOMIC_RELAXED, __HIP_MEMORY_SCOPE_AGENT);
        __builtin_amdgcn_s_sleep(96);
        poll_ge(p.ws, C_F0 + (id0.lane & 15), want0);
        poll_ge(p.ws, C_H, 24u);
        if (id0.lane < 9) st_word(p.ws, id0.lane == 8 ? C_EP : C_FL + id0.lane, id0.lane == 8 ? tag : tag - 1u);
      }
    } else {
      const int n = C_F0 + 128 + (b - 225) * 256 + (int)threadIdx.x;
      if (n < C_LINES) st_word(p.ws, n, 0u);
      wait_init(id0, p.ws, x8, tag);
      seam_arrive<16, 8>(id0, p.ws, C_F0, x8, nop);
    }
    STAMP(26);
    STAMP_OUT();
    return;
  }
  if (q > 28) {
    const unsigned tag = ld_word(p.ws, C_EP) + 1u;
    if (CODEWARM && q == 29) {
      const rsrc_t rc = mkrs((const void*)(pc0 & ~127ull), CODEWARM);
      for (int n = id0.w; n < CODEWARM / 8192; n += 4) touch(rc, L + L_D, id0.lane * 128, n * 8192);
    }
    for (int n = 0; n < TDELAY; ++n) __builtin_amdgcn_s_sleep(16);
    const rsrc_t r2 = mkrs(p.w2, S * F * 2), r1 = mkrs(p.w1, x8 < 7 ? S * F * 2 : 0);
    const int h = 4 * (q - 29) + id0.w;
#pragma unroll 1
    for (int pass = 0; pass < 2; ++pass) {
      if (L2WARM) {
        for (int n = h + (pass ? 48 : 0); n < 4 * 32; n += 12) touch(r1, L + L_D, id0.lane * (F * 2), ((n >> 2 & 3) * 512 + (n >> 4) * 64) * F * 2 + (4 * x8 + (n & 3)) * 128);
        for (int n = h + (pass ? 32 : 0); n < 4 * 28; n += 12) touch(r2, L + L_D, id0.lane * (S * 2), ((n >> 2 & 3) * 448 + (n >> 4) * 64) * S * 2 + (4 * x8 + (n & 3)) * 128);
      }
      vmwait<0>();
      if (pass) break;
      STAMP(26);
      if (id0.w == 0) {
        wait_init(id0, p.ws, x8, tag);
        if (id0.lane == 0) __hip_atomic_fetch_add((gu32*)p.ws + 32 * C_H, 1u, __ATOMIC_RELAXED, __HIP_MEMORY_SCOPE_AGENT);
        if (RETOUCH) {
          poll_ge(p.ws, C_F0 + x8 * 16 + (id0.lane & 15), want0);
          poll_ge(p.ws, C_G2 + 4 * x8 * 8 + 7, 7u);
        }
      }
      if (!RETOUCH) break;
      wgbar();
    }
    STAMP_OUT();
    return;
  }
  const int i = 4 * x8 + q / 7, j = q % 7, m = i, jj = x8 * 28 + q;
  const bool g1 = i < 28;
#define ID fresh(id0)
#define W1S w_src(ID, p.w1, fresh_s(g1 ? S * F * 2 : 0), F * 2, 512, fresh_s(m) * 128, L)
#define W2S w_src(ID, p.w2, S * F * 2, S * 2, 448, fresh_s(i) * 128, L)
#define STAGE (L + L_W + fresh_s(id0.w) * WSTAGE)
#define ASTAGE (L + L_A + fresh_s(id0.w) * 12288)
  f0_issue<0>(id0, mkrs(p.x, XB), 0, i, L);
  STAMP(24);
  wt_issue(p, id0, x8, q / 7, q % 7, L);
  if (threadIdx.x == 0) dma4_sc1(rw, L + L_D + 256, C_EP * 128);
  STAMP(25);
  SB();
  v4u R1[NR1][4], R2[NR2][4];
#pragma unroll 1
  for (int layer = 0; layer < 2; ++layer) {
    const int sb = layer * 30; (void)sb;
    v4u T[14];
    gen_tf(ID, j, T);
    if (layer == 1) {
      seam_arrive<1, 8>(ID, p.ws, C_G2 + i * 8, 0, nop);
      STAMP(23);
    }
    {
      PC k;
      gen_pc_a(ID, k);
      if (layer == 1) {
        seam_wait<1>(ID, p.ws, C_G2 + i * 8, j, 7u);
        f0_issue<SC1>(ID, rw, X2_OFF, i, L);
      }
      STAMP(sb + 1);
      f0_compute(ID, rw, layer, i, j, L, T);
      STAMP(sb + 2);
      if (layer == 0) {
        if (threadIdx.x == 0) dma4_sc1(rw, L + L_D + 260, (C_FL + x8) * 128);
        wt_store(rw, x8, q / 7, q % 7, L);
      }
      STAMP(sb + 3);
      seam_arrive<16, 8>(ID, p.ws, C_F0 + layer * 128, b & 15, [&]() {
        gen_pc_b(ID, k);
        if (layer == 0 && id0.w == 0) {
          vmwait<0>();
          const unsigned tag = ((const volatile LAS unsigned*)(L + L_D))[64] + 1u, fl = ((const volatile LAS unsigned*)(L + L_D))[65];
          if (__builtin_amdgcn_readfirstlane(fl != tag)) wait_init(id0, p.ws, x8, tag);
        } });
      STAMP(sb + 4);
      if (layer == 0) { const WSrc ws = W1S; w_issue<0, 4 * NR1>(ws, 0, ws.stage); }
      else __builtin_amdgcn_s_sleep(10);
      STAMP(sb + 5);
      seam_wait<16>(ID, p.ws, C_F0 + layer * 128, x8, layer == 0 ? want0 : 14u);
      STAMP(sb + 6);
      p_phase(ID, rw, layer, jj, L, k, [&]() {
        if (layer == 0) { vmwait<5>(); w_read<NR1, 0>(ID, STAGE, R1); lgwait<0>(); pin_acc<0, NR1>(R1); }
        gen_ti(ID, j, T); } STA);
    }
    STAMP(sb + 7);
    v4u W[10][4];
    f4 x1[4];
    {
      seam_arrive<16, 8, 12>(ID, p.ws, C_P + layer * 128, b & 15, [&]() { const WSrc ws = W1S; w_issue<0, 12>(ws, NR1, ws.stage); });
      STAMP(sb + 8);
      f4 mu[4];
      {
        const Ids id = ID;
#pragma unroll
        for (int n = 0; n < 4; ++n) mu[n] = asf4(ld128<SC1>(rw, (16 * (n ^ id.w) + 4 * id.g) * 4, MU_OFF + (layer * S + 64 * i) * 4));
      }
      { const WSrc ws = W1S; w_issue<12, 19>(ws, NR1, ws.stage); poll_early<16>(ID, rw, L, C_P + layer * 128, x8); w_issue<19, 24>(ws, NR1, ws.stage); }
      STAMP(sb + 9);
      seam_wait_early<16, 5>(ID, p.ws, L, C_P + layer * 128, x8, 14u);
      STAMP(sb + 10);
      s3_issue(ID, rw, layer, i, L);
      vmwait<14>();
      w_read<6, 0>(ID, STAGE, W);
      STAMP(sb + 11);
      s3_compute(ID, rw, layer, i, j, L, T, mu, x1 STA);
      pin_acc<0, 3>(W);
      STAMP(sb + 12);
    }
    if (layer == 0) { const WSrc ws = W2S; w_issue<0, 8>(ws, 0, ws.stage); }
    seam_arrive<4, 8, 12>(ID, p.ws, C_S3 + (layer * 7 + j) * 32, i & 3, [&]() {
      { const WSrc ws = W1S; w_issue<0, 4>(ws, NR1 + 6, ws.stage + 20480); }
      if (layer == 1) vmwait<4>(); });
    STAMP(sb + 13);
    v2u bq[4];
    f4 res[4];
    {
      const rsrc_t rg = mkrs(p.ws, fresh_s(g1 ? WS_BYTES : 0));
      load_bias(ID, p.b1, g1 ? 64 * m : 0, bq);
      if (layer == 0) { const WSrc ws = W2S; w_issue<8, 4 * NR2>(ws, 0, ws.stage); }
      { const WSrc ws = W1S; lds_t as = ASTAGE; w_issue<0, 7>(ws, NR1 + 7, as); poll_early<4>(ID, rw, L, C_S3 + (layer * 7 + j) * 32, x8); w_issue<7, 12>(ws, NR1 + 7, as); }
      STAMP(sb + 14);
      seam_wait_early<4, 5>(ID, p.ws, L, C_S3 + (layer * 7 + j) * 32, x8, 8u);
      pin(bq);
      STAMP(sb + 15);
      g_main<16, 6, 20480, 1, 3, 0>(ID, rg, HF_OFF + layer * XB + j * 64 * 4096, L, R1, W, res, [&](int f) { return bias4(bq[f]); }, nop);
      STAMP(sb + 16);
      if (layer == 0) { w_read<NR2, 0>(ID, STAGE, R2); lgwait<0>(); pin_acc<0, NR2>(R2); }
      const WSrc ws = W2S;
      g1_epi(ID, rg, layer, j, m, res, [&](int f) { w_piece(ws, NR2, 2 * f, ws.stage); w_piece(ws, NR2, 2 * f + 1, ws.stage); });
      STAMP(sb + 17);
      seam_arrive<4, 8, 10>(ID, p.ws, C_G1 + (layer * 7 + j) * 32, m & 3, [&]() { w_issue<8, 18>(ws, NR2, ws.stage); }, g1);
      STAMP(sb + 18);
    }
    load_bias(ID, p.b2, 64 * i, bq);
    { const WSrc ws = W2S; lds_t as = ASTAGE; w_issue<18, 24>(ws, NR2, ws.stage); w_issue<0, 2>(ws, NR2 + 6, as);
      poll_early<4>(ID, rw, L, C_G1 + (layer * 7 + j) * 32, x8); w_issue<2, 12>(ws, NR2 + 6, as); }
    STAMP(sb + 19);
    seam_wait_early<4, 10>(ID, p.ws, L, C_G1 + (layer * 7 + j) * 32, x8, 7u);
    pin(bq);
    STAMP(sb + 20);
    G2A ga;
    g_main<14, 0, 0, 6, 3, 4>(ID, rw, GF_OFF + layer * GB + j * 56 * 4096, L, R2, W, res, [&](int f) { return bias4(bq[f]) + x1[f]; }, [&]() { ga = g2_addr(ID, L); });
    STAMP(sb + 21);
    g2_epi(ga, layer == 0 ? rw : mkrs(p.out, XB), (layer == 0 ? X2_OFF : 0) + 64 * i * 896 + j * 128, res);
    STAMP(sb + 22);
  }
  STAMP(53);
  STAMP_OUT();
}

extern "C" void kernel_launch(void* const* d_in, const int* in_sizes, int n_in, void* d_out, int out_size, void* d_ws, size_t ws_size, hipStream_t stream) {
  Params p;
  p.x = (const unsigned short*)d_in[0]; p.wr = (const unsigned short*)d_in[1]; p.wi = (const unsigned short*)d_in[2];
  p.w1 = (const unsigned short*)d_in[4]; p.b1 = (const unsigned short*)d_in[5]; p.w2 = (const unsigned short*)d_in[6]; p.b2 = (const unsigned short*)d_in[7];
  p.out = (unsigned short*)d_out; p.ws = (unsigned char*)d_ws;
  (void)hipFuncSetAttribute((const void*)k_all, hipFuncAttributeMaxDynamicSharedMemorySize, LDS_BYTES);
  k_all<<<256, 256, LDS_BYTES, stream>>>(p);
}
```

```cpp
#include <hip/hip_runtime.h>
#include <hip/hip_bf16.h>
#include <stdint.h>

#define DI __device__ __forceinline__
#define GAS __attribute__((address_space(1)))
#define LAS __attribute__((address_space(3)))
typedef unsigned v4u __attribute__((ext_vector_type(4)));
typedef unsigned v2u __attribute__((ext_vector_type(2)));
typedef float f4 __attribute__((ext_vector_type(4)));
typedef float f2 __attribute__((ext_vector_type(2)));
typedef _Float16 h8 __attribute__((ext_vector_type(8)));
typedef _Float16 h2 __attribute__((ext_vector_type(2)));
typedef __bf16 b8 __attribute__((ext_vector_type(8)));
typedef __bf16 b2 __attribute__((ext_vector_type(2)));
typedef short s4 __attribute__((ext_vector_type(4)));
typedef __amdgpu_buffer_rsrc_t rsrc_t;
typedef LAS unsigned char* lds_t;
typedef GAS unsigned gu32;

#define GWIN 11
#define EPI_D 8
#define DRN_D 10
#define G2PRE 3
#define TRIG 1u
#define EARLY 1

constexpr int S = 2048, D = 448, F = 1792, NKF = 225;
constexpr float EPS = 1e-5f;
constexpr int XB = S * D * 2;
constexpr int GB = D * F * 2;
constexpr int SC1 = 16;
constexpr int CTRL_BYTES = 262144;
constexpr int MU_OFF = CTRL_BYTES;
constexpr int RS_OFF = MU_OFF + 2 * S * 4;
constexpr int WT_OFF = RS_OFF + 2 * S * 2;
constexpr int XH0_OFF = WT_OFF + XB;
constexpr int XH1_OFF = XH0_OFF + 2 * XB;
constexpr int HF_OFF = XH1_OFF + 2 * XB;
constexpr int GF_OFF = HF_OFF + 2 * XB;
constexpr int X2_OFF = GF_OFF + 2 * GB;
constexpr int WS_BYTES = X2_OFF + XB;
static_assert(HF_OFF % 8192 == 0 && XB % 8192 == 0 && GF_OFF % 8192 == 0 && GB % 8192 == 0, "fragment pairs are addressed by an exclusive or");
#define STAMP(n)
#define STV
#define STA
#define STAMP_OUT()
constexpr int L_A = 0, L_E = 57344, L_R = L_E + 512, L_D = L_R + 1024, L_W = 59392, WSTAGE = 24576, LDS_BYTES = L_W + 4 * WSTAGE;
constexpr int NR1 = 6, NR2 = 5;

struct Params {
  const unsigned short* x; const unsigned short* wr; const unsigned short* wi;
  const unsigned short* w1; const unsigned short* b1; const unsigned short* w2; const unsigned short* b2;
  unsigned short* out; unsigned char* ws;
};

DI rsrc_t mkrs(const void* p, int bytes) { return __builtin_amdgcn_make_buffer_rsrc((void*)p, 0, bytes, 0x00020000); }
DI f4 mfma_bf(v4u a, v4u b, f4 c) { return __builtin_amdgcn_mfma_f32_16x16x32_bf16(__builtin_bit_cast(b8, a), __builtin_bit_cast(b8, b), c, 0, 0, 0); }
DI f4 mfma_h(v4u a, v4u b, f4 c) { return __builtin_amdgcn_mfma_f32_16x16x32_f16(__builtin_bit_cast(h8, a), __builtin_bit_cast(h8, b), c, 0, 0, 0); }
DI unsigned pk_bf(float a, float b) { f2 v = {a, b}; return __builtin_bit_cast(unsigned, __builtin_convertvector(v, b2)); }
DI unsigned pk_h(float a, float b) { f2 v = {a, b}; return __builtin_bit_cast(unsigned, __builtin_convertvector(v, h2)); }
DI float h_lo(unsigned u) { return (float)__builtin_bit_cast(h2, u)[0]; }
DI float h_hi(unsigned u) { return (float)__builtin_bit_cast(h2, u)[1]; }
DI float bf_lo(unsigned u) { return __builtin_bit_cast(float, u << 16); }
DI float bf_hi(unsigned u) { return __builtin_bit_cast(float, u & 0xffff0000u); }
DI unsigned mul_h2(unsigned a, unsigned b) { return __builtin_bit_cast(unsigned, __builtin_bit_cast(h2, a) * __builtin_bit_cast(h2, b)); }
DI float vcos(float x) { return __builtin_amdgcn_cosf(x); }
DI float vsin(float x) { return __builtin_amdgcn_sinf(x); }
DI v2u trread(lds_t p) { return __builtin_bit_cast(v2u, __builtin_amdgcn_ds_read_tr16_b64_v4i16((LAS s4*)p)); }
DI v4u cat(v2u a, v2u b) { return v4u{a.x, a.y, b.x, b.y}; }
DI v4u ldsr128(lds_t p) { return *(LAS v4u*)p; }
DI void ldsw128(lds_t p, v4u v) { *(LAS v4u*)p = v; }
template <int AUX> DI void dma16(rsrc_t rs, lds_t dst, int voff, int soff) {
  const unsigned la = (unsigned)(size_t)dst;
  if constexpr (AUX == SC1) asm volatile("s_mov_b32 m0, %0\n\ts_nop 0\n\tbuffer_load_dwordx4 %1, %2, %3 offen sc1 lds" ::"s"(la), "v"(voff), "s"(rs), "s"(soff) : "memory");
  else asm volatile("s_mov_b32 m0, %0\n\ts_nop 0\n\tbuffer_load_dwordx4 %1, %2, %3 offen lds" ::"s"(la), "v"(voff), "s"(rs), "s"(soff) : "memory");
}
DI void ald128(v4u& o, rsrc_t rs, int voff, int soff) { asm volatile("buffer_load_dwordx4 %0, %1, %2, %3 offen sc1" : "=v"(o) : "v"(voff), "s"(rs), "s"(soff) : "memory"); }
DI void touch(rsrc_t rs, lds_t dummy, int voff, int soff) {
  const unsigned la = (unsigned)(size_t)dummy;
  asm volatile("s_mov_b32 m0, %0\n\ts_nop 0\n\tbuffer_load_dword %1, %2, %3 offen lds" ::"s"(la), "v"(voff), "s"(rs), "s"(soff) : "memory");
}
DI void dma4_sc1(rsrc_t rs, lds_t dst, int voff) {
  const unsigned la = (unsigned)(size_t)dst;
  asm volatile("s_mov_b32 m0, %0\n\ts_nop 0\n\tbuffer_load_dword %1, %2, 0 offen sc1 lds" ::"s"(la), "v"(voff), "s"(rs) : "memory");
}
template <int AUX> DI v4u ld128(rsrc_t rs, int voff, int soff = 0) { return __builtin_amdgcn_raw_buffer_load_b128(rs, voff, soff, AUX); }
template <int AUX> DI void st128(v4u v, rsrc_t rs, int voff, int soff = 0) { __builtin_amdgcn_raw_buffer_store_b128(v, rs, voff + soff, 0, AUX); }
DI f4 asf4(v4u v) { return __builtin_bit_cast(f4, v); }
DI v4u asu4(f4 v) { return __builtin_bit_cast(v4u, v); }
template <int N> DI void vmwait() { asm volatile("s_waitcnt vmcnt(%0)" ::"n"(N) : "memory"); }
template <int N> DI void lgwait() { asm volatile("s_waitcnt lgkmcnt(%0)" ::"n"(N) : "memory"); }
DI void rawbar() { asm volatile("" ::: "memory"); __builtin_amdgcn_s_barrier(); asm volatile("" ::: "memory"); }
DI void wgbar() { lgwait<0>(); rawbar(); }
#define SB() __builtin_amdgcn_sched_barrier(0)
template <class T> DI void pin1(T& v) { asm volatile("" : "+v"(v)); }
template <class T, int N> DI void pin(T (&a)[N]) {
#pragma unroll
  for (int n = 0; n < N; ++n) pin1(a[n]);
}
template <int A, int B, int N> DI void pin_acc(v4u (&W)[N][4]) {
#pragma unroll
  for (int ks = A; ks < B; ++ks)
#pragma unroll
    for (int n = 0; n < 4; ++n) asm volatile("" : "+a"(W[ks][n]));
}
constexpr f4 Z4 = {0.f, 0.f, 0.f, 0.f};
template <int V> struct IC { static constexpr int v = V; };
template <int A, int B, class Fn> DI void static_for(Fn&& fn) { if constexpr (A < B) { fn(IC<A>{}); static_for<A + 1, B>(fn); } }

DI float diag(const f4& c, int l15) { const float a = l15 & 1 ? c[1] : c[0], b = l15 & 1 ? c[3] : c[2]; return l15 & 2 ? b : a; }

struct Ids { int lane, w, l15, g; };
DI Ids get_ids() { Ids d; d.lane = threadIdx.x & 63; d.w = __builtin_amdgcn_readfirstlane(threadIdx.x >> 6); d.l15 = d.lane & 15; d.g = d.lane >> 4; return d; }
DI Ids fresh(const Ids& a) { Ids d; d.lane = a.lane; d.w = a.w; asm volatile("" : "+v"(d.lane), "+s"(d.w)); d.l15 = d.lane & 15; d.g = d.lane >> 4; return d; }
DI int fresh_s(int v) { asm volatile("" : "+s"(v)); return v; }

DI int img_src(const Ids& id, int pitch) { const int r7 = id.lane >> 3; return r7 * pitch + (((id.lane & 7) ^ (r7 & 6)) << 4); }
DI int img_rd(const Ids& id, int n) { const int row = 4 * id.g + (id.l15 >> 2); return row * 128 + 32 * (n ^ id.w ^ ((row >> 1) & 3)) + 8 * (id.l15 & 3); }
DI v4u img_frag(lds_t p) { return cat(trread(p), trread(p + 2048)); }
struct WSrc { rsrc_t rs; int pitch, base, vo; lds_t stage; };
DI WSrc w_src(const Ids& id, const void* w, int bytes, int pitch, int rows_per_wave, int col0, lds_t L) {
  return WSrc{mkrs(w, bytes), pitch, id.w * rows_per_wave * pitch + col0, img_src(id, pitch), L + L_W + id.w * WSTAGE};
}
DI void w_piece(const WSrc& ws, int k0, int n, lds_t dst) { dma16<0>(ws.rs, dst + n * 1024, ws.vo, ws.base + (32 * k0 + 8 * n) * ws.pitch); }
template <int N0, int N1> DI void w_issue(const WSrc& ws, int k0, lds_t dst) {
#pragma unroll
  for (int n = N0; n < N1; ++n) w_piece(ws, k0, n, dst);
}
template <int CNT, int O, int N> DI void w_read(const Ids& id, lds_t stage, v4u (&W)[N][4]) {
  lds_t b[4];
#pragma unroll
  for (int n = 0; n < 4; ++n) b[n] = stage + img_rd(id, n);
#pragma unroll
  for (int ks = 0; ks < CNT; ++ks)
#pragma unroll
    for (int n = 0; n < 4; ++n) W[O + ks][n] = img_frag(b[n] + ks * 4096);
}
DI v4u pair_frag(int w, v2u a, v2u b) { const bool sw = w & 1; return v4u{sw ? b.x : a.x, sw ? b.y : a.y, sw ? a.x : b.x, sw ? a.y : b.y}; }

DI void gen_tf(const Ids& id, int j, v4u (&T)[14]) {
  const int c = 64 * j + 16 * id.w + id.l15;
  const bool dc = c == 447;
  const float kf = dc ? 0.f : (float)((c >> 1) + 1) * (1.0f / 448.0f);
  const float base = __builtin_fmaf((float)(8 * id.g), kf, (c & 1) && !dc ? 0.25f : 0.f);
  const float tc = 2.0f * vcos(32.0f * kf);
  f2 v[2][4];
#pragma unroll
  for (int ks = 0; ks < 14; ++ks) {
    unsigned u[4];
#pragma unroll
    for (int e = 0; e < 4; ++e) {
      if (ks < 2) v[ks][e] = f2{vcos(__builtin_fmaf((float)(32 * ks + 2 * e), kf, base)), vcos(__builtin_fmaf((float)(32 * ks + 2 * e + 1), kf, base))};
      else v[ks & 1][e] = v[~ks & 1][e] * tc - v[ks & 1][e];
      u[e] = pk_bf(v[ks & 1][e][0], v[ks & 1][e][1]);
    }
    T[ks] = v4u{u[0], u[1], u[2], u[3]};
    pin1(T[ks]);
  }
}
template <int AUX> DI void f0_issue(const Ids& id, rsrc_t rx, int xbase, int i, lds_t L) {
  const int r7 = id.lane >> 3;
  const int vo = r7 * 896 + (((id.lane & 7) ^ (2 * id.w + ((r7 >> 1) & 1))) << 4);
#pragma unroll
  for (int n = 0; n < 14; ++n) dma16<AUX>(rx, L + L_A + (4 * n + id.w) * 1024, vo, xbase + (64 * i + 32 * (n & 1) + 8 * id.w) * 896 + (n >> 1) * 128);
}
DI void f0_compute(const Ids& id, rsrc_t rw, int layer, int i, int j, lds_t L, const v4u (&T)[14]) {
  constexpr int YOUNG = 0, YOUNG0 = 3;
  constexpr int PD = 8;
  const int rho = id.l15, xr = (rho >> 1) & 7;
  const int rowb = (8 * (rho >> 2) + (rho & 3)) * 128, tw = 4096 * (id.w >> 1) + 512 * (id.w & 1);
  lds_t pa[2][4];
#pragma unroll
  for (int n = 0; n < 4; ++n) {
    const int o = rowb + ((4096 * (n >> 1) + 512 * (n & 1)) ^ tw);
    pa[0][n] = L + L_A + o + ((id.g ^ xr) << 4); pa[1][n] = L + L_A + o + (((id.g ^ xr) << 4) ^ 64);
  }
  if (layer == 0) vmwait<6 + YOUNG0>(); else vmwait<6 + YOUNG>();
  rawbar();
  v4u a[PD];
#pragma unroll
  for (int u = 0; u < PD; ++u) a[u] = ldsr128(pa[(u >> 2) & 1][u & 3] + (u >> 3) * 8192);
  SB();
  f4 acc[4];
  f4 gram = Z4;
#pragma unroll
  for (int u = 0; u < 56; ++u) {
    const int ks = u >> 2, n = u & 3, v = u + PD;
    const v4u cur = a[u % PD];
    acc[n] = mfma_bf(cur, T[ks], ks == 0 ? Z4 : acc[n]);
    if (n == 0) gram = mfma_bf(cur, cur, gram);
    if (v == 32) { if (layer == 0) vmwait<YOUNG0>(); else vmwait<YOUNG>(); rawbar(); }
    if (v < 56) a[u % PD] = ldsr128(pa[(v >> 2) & 1][v & 3] + (v >> 3) * 8192);
    SB();
  }
  const int c = 64 * j + 16 * id.w + id.l15;
  LAS float* eb = (LAS float*)(L + L_E);
  if (id.g == rho >> 2) eb[32 * (id.w >> 1) + 4 * (id.w & 1) + 8 * (rho >> 2) + (rho & 3)] = diag(gram, rho);
  if (id.w == 3 && rho == 15) {
#pragma unroll
    for (int n = 0; n < 4; ++n) *(LAS f4*)(eb + 64 + 32 * ((n ^ 3) >> 1) + 4 * ((n ^ 3) & 1) + 8 * id.g) = acc[n];
  }
  SB();
  const float sc = c == 447 ? 0.f : (c == 446 ? 0.047245559126153f : 0.066815310478106f);
#pragma unroll
  for (int q = 0; q < 2; ++q) {
    const f4 x = acc[2 * q] * sc, y = acc[2 * q + 1] * sc;
    const unsigned x0 = pk_h(x[0], x[1]), x1 = pk_h(x[2], x[3]), y0 = pk_h(y[0], y[1]), y1 = pk_h(y[2], y[3]);
    const bool sw = id.w & 1;
    st128<SC1>(v4u{sw ? y0 : x0, sw ? y1 : x1, sw ? x0 : y0, sw ? x1 : y1}, rw, c * 128 + 16 * id.g, XH0_OFF + layer * XB + i * 57344 + 64 * (q ^ (id.w >> 1)));
  }
  vmwait<2>();
  wgbar();
  if (j == 6 && id.w == 0) {
    const float m = eb[64 + id.lane] * (1.0f / 448.0f);
    const float r = __builtin_amdgcn_rsqf(__builtin_fmaxf(__builtin_fmaf(-m, m, eb[id.lane] * (1.0f / 448.0f)), 0.f) + EPS);
    __builtin_amdgcn_raw_buffer_store_b32(__builtin_bit_cast(unsigned, m), rw, id.lane * 4, MU_OFF + (layer * S + 64 * i) * 4, SC1);
    __builtin_amdgcn_raw_buffer_store_b16((unsigned short)(pk_h(r, r) & 0xffffu), rw, id.lane * 2, RS_OFF + (layer * S + 64 * i) * 2, SC1);
  }
}
constexpr int WT_LD = 3;
DI void wt_issue(const Params& p, const Ids& id, int kg, int g, int cg, lds_t L) {
  const rsrc_t rs = mkrs(id.w < 2 ? p.wr : p.wi, S * NKF * 2);
#pragma unroll
  for (int n = 0; n < WT_LD; ++n) {
    const int pc = (id.w & 1) * 192 + n * 64 + id.lane, row = pc / 5, ch = pc - 5 * row;
    const int vo = ((((row & 7) + 64 * ((row >> 3) & 7)) * NKF + 1) * 2 & ~3) + 16 * ch;
    dma16<0>(rs, L + L_W + fresh_s(id.w * 3072) + (id.w & 2) * 1024 + n * 1024, vo, ((8 * kg + 512 * g) * NKF + 32 * cg) * 2);
  }
}
DI void wt_store(rsrc_t rw, int kg, int g, int cg, lds_t L) {
  int t = threadIdx.x;
  pin1(t);
  const LAS unsigned short* q = (const LAS unsigned short*)(L + L_W + (t & 7) * 80 + (t >> 3) * 2 + (~t & 1) * 2);
  unsigned u[8];
#pragma unroll
  for (int m = 0; m < 8; ++m) u[m] = (unsigned)q[320 * m] | ((unsigned)q[320 * m + 4096] << 16);
  const int so = WT_OFF + 32 * cg * 8192 + (kg >> 1) * 1024 + (g * 16 + (kg & 1) * 8) * 16, vo = (t >> 3) * 8192 + (t & 7) * 16;
  st128<SC1>(v4u{u[0], u[1], u[2], u[3]}, rw, vo, so);
  st128<SC1>(v4u{u[4], u[5], u[6], u[7]}, rw, vo + 4096, so);
}

struct PC { v4u fr64[4], fi64[4], fr32[4], fi32[4]; float twc[8], tws[8], tic[8], tis[8]; };
DI void cs_tuple(const float (&x)[4], v4u& re, v4u& im) {
  float c[4], s[4];
#pragma unroll
  for (int e = 0; e < 4; ++e) { c[e] = vcos(x[e]); s[e] = vsin(x[e]); }
  const unsigned c0 = pk_h(c[0], c[1]), c1 = pk_h(c[2], c[3]), s0 = pk_h(s[0], s[1]), s1 = pk_h(s[2], s[3]);
  re = v4u{c0, c1, s0, s1}; im = v4u{s0 ^ 0x80008000u, s1 ^ 0x80008000u, c0, c1};
  pin1(re); pin1(im);
}
DI void gen_pc_a(const Ids& id, PC& k) {
  const float a64 = (float)(16 * id.w + id.l15) * (1.0f / 64.0f), g4 = (float)(4 * id.g);
#pragma unroll
  for (int ks = 0; ks < 4; ++ks) {
    float x[4];
#pragma unroll
    for (int e = 0; e < 4; ++e) x[e] = (g4 + (float)(16 * ks + e)) * a64;
    cs_tuple(x, k.fr64[ks], k.fi64[ks]);
  }
  const float pl = (float)(8 * (id.l15 >> 2) + (id.l15 & 3)), g8 = (float)(8 * id.g);
#pragma unroll
  for (int t = 0; t < 2; ++t) {
    float x[4];
#pragma unroll
    for (int e = 0; e < 4; ++e) x[e] = (g8 + (float)(4 * (t & 1) + e)) * (pl * (1.0f / 32.0f));
    cs_tuple(x, k.fr32[t], k.fi32[t]);
  }
}
DI void gen_pc_b(const Ids& id, PC& k) {
  const float pl = (float)(8 * (id.l15 >> 2) + (id.l15 & 3)), g8 = (float)(8 * id.g);
#pragma unroll
  for (int t = 2; t < 4; ++t) {
    float x[4];
#pragma unroll
    for (int e = 0; e < 4; ++e) x[e] = (g8 + (float)(4 * (t & 1) + e)) * ((pl + 4.0f) * (1.0f / 32.0f));
    cs_tuple(x, k.fr32[t], k.fi32[t]);
  }
  const float k1a = (float)(16 * id.w + id.l15) * (1.0f / 2048.0f), k1b = (float)(16 * id.w + 4 * id.g);
#pragma unroll
  for (int m = 0; m < 8; ++m) {
    const float x = (g8 + (float)m) * k1a;
    k.twc[m] = vcos(x) * 0.125f; k.tws[m] = vsin(x) * 0.125f;
    const float y = (k1b + (float)(m & 3)) * ((pl + (float)(4 * (m >> 2))) * (1.0f / 2048.0f));
    k.tic[m] = vcos(y) * 0.17677669529663687f; k.tis[m] = vsin(y) * 0.17677669529663687f;
    pin1(k.twc[m]); pin1(k.tws[m]); pin1(k.tic[m]); pin1(k.tis[m]);
  }
}
DI v4u cmul_pack(const f4& a, const f4& b, const float* c, const float* s) {
  float u[4], v[4];
#pragma unroll
  for (int e = 0; e < 4; ++e) { u[e] = a[e] * c[e] + b[e] * s[e]; v[e] = b[e] * c[e] - a[e] * s[e]; }
  return v4u{pk_h(u[0], u[1]), pk_h(u[2], u[3]), pk_h(v[0], v[1]), pk_h(v[2], v[3])};
}
template <class Fn> DI void p_phase(const Ids& id, rsrc_t rw, int layer, int jj, lds_t L, const PC& k, Fn slot STV) {
  const int s0 = 512 * id.w + 32 * id.l15 + 8 * id.g;
  const int xo = (s0 >> 6) * 57344 + (s0 & 63) * 2 + jj * 256;
  v4u rs, xr, xi, wt[2];
  ald128(rs, rw, s0 * 2, RS_OFF + layer * S * 2);
  ald128(xr, rw, xo, XH0_OFF + layer * XB); ald128(xi, rw, xo + 128, XH0_OFF + layer * XB);
  ald128(wt[0], rw, id.lane * 16, WT_OFF + jj * 8192 + id.w * 1024); ald128(wt[1], rw, id.lane * 16, WT_OFF + jj * 8192 + id.w * 1024 + 4096);
  const int tr = 4 * id.g + (id.l15 >> 2), to = tr * 64 + (((id.l15 & 3) ^ (((tr >> 1) & 1) | ((tr >> 2) & 2))) << 4), th = (tr << 1) & 8;
  lds_t tp[2] = {L + L_A + to + th, L + L_A + to + (th ^ 8)};
  slot();
  vmwait<2>(); SB();
  STAMP(54);
  pin1(rs); pin1(xr); pin1(xi);
  {
    const int zc = (16 * id.w + id.l15) * 64 + ((id.g ^ (((id.l15 >> 1) & 1) | ((id.l15 >> 2) & 2))) << 4), zh = (id.l15 << 1) & 8;
    LAS v2u* z0 = (LAS v2u*)(L + L_A + zc + zh); LAS v2u* z1 = (LAS v2u*)(L + L_A + zc + (zh ^ 8));
    z0[0] = v2u{mul_h2(xr[0], rs[0]), mul_h2(xr[1], rs[1])}; z1[0] = v2u{mul_h2(xr[2], rs[2]), mul_h2(xr[3], rs[3])};
    z0[512] = v2u{mul_h2(xi[0], rs[0]), mul_h2(xi[1], rs[1])}; z1[512] = v2u{mul_h2(xi[2], rs[2]), mul_h2(xi[3], rs[3])};
  }
  wgbar();
  STAMP(55);
  v4u a[8];
#pragma unroll
  for (int u = 0; u < 8; ++u) a[u] = cat(trread(tp[u >> 2] + (u & 3) * 1024), trread(tp[u >> 2] + 4096 + (u & 3) * 1024));
  SB();
  f4 ar[2], ai[2];
#pragma unroll
  for (int u = 0; u < 8; ++u) { const int t = u >> 2, ks = u & 3; ar[t] = mfma_h(a[u], k.fr64[ks], ks ? ar[t] : Z4); ai[t] = mfma_h(a[u], k.fi64[ks], ks ? ai[t] : Z4); }
  v4u bq[2];
#pragma unroll
  for (int t = 0; t < 2; ++t) bq[t] = cmul_pack(ar[t], ai[t], k.twc + 4 * t, k.tws + 4 * t);
  STAMP(56);
  vmwait<0>(); SB(); pin(wt);
  STAMP(57);
  float wr[8], wi[8];
#pragma unroll
  for (int m = 0; m < 8; ++m) { const unsigned t = wt[m >> 2][m & 3]; wr[m] = bf_lo(t) * 0.17677669529663687f; wi[m] = bf_hi(t) * -0.17677669529663687f; }
  v4u yq[2];
  f4 yr[2], yi[2];
#pragma unroll
  for (int tm = 0; tm < 2; ++tm) {
    yr[tm] = mfma_h(k.fr32[2 * tm], bq[0], Z4); yi[tm] = mfma_h(k.fi32[2 * tm], bq[0], Z4);
  }
#pragma unroll
  for (int tm = 0; tm < 2; ++tm) {
    yr[tm] = mfma_h(k.fr32[2 * tm + 1], bq[1], yr[tm]); yi[tm] = mfma_h(k.fi32[2 * tm + 1], bq[1], yi[tm]);
  }
#pragma unroll
  for (int tm = 0; tm < 2; ++tm) {
    float u[4], v[4];
#pragma unroll
    for (int e = 0; e < 4; ++e) { u[e] = yr[tm][e] * wr[4 * tm + e] + yi[tm][e] * wi[4 * tm + e]; v[e] = yr[tm][e] * wi[4 * tm + e] - yi[tm][e] * wr[4 * tm + e]; }
    yq[tm] = v4u{pk_h(u[0], u[1]), pk_h(u[2], u[3]), pk_h(v[0], v[1]), pk_h(v[2], v[3])};
  }
  f4 br[2], bi[2];
#pragma unroll
  for (int t = 0; t < 2; ++t) { br[t] = mfma_h(yq[0], k.fr32[2 * t], Z4); bi[t] = mfma_h(yq[0], k.fi32[2 * t], Z4); }
#pragma unroll
  for (int t = 0; t < 2; ++t) { br[t] = mfma_h(yq[1], k.fr32[2 * t + 1], br[t]); bi[t] = mfma_h(yq[1], k.fi32[2 * t + 1], bi[t]); }
#pragma unroll
  for (int t = 0; t < 2; ++t) ldsw128(L + L_A + 8192 + (2 * id.w + t) * 1024 + id.lane * 16, cmul_pack(br[t], bi[t], k.tic + 4 * t, k.tis + 4 * t));
  wgbar();
  STAMP(58);
#pragma unroll
  for (int u = 0; u < 8; ++u) a[u] = ldsr128(L + L_A + 8192 + (2 * (u & 3) + (u >> 2)) * 1024 + id.lane * 16);
  float xf[16];
#pragma unroll
  for (int e = 0; e < 4; ++e) { xf[2 * e] = h_lo(xr[e]); xf[2 * e + 1] = h_hi(xr[e]); xf[8 + 2 * e] = h_lo(xi[e]); xf[8 + 2 * e + 1] = h_hi(xi[e]); }
  pin(xf);
  SB();
#pragma unroll
  for (int u = 0; u < 8; ++u) { const int t = u >> 2, ks = u & 3; ar[t] = mfma_h(a[u], k.fr64[ks], ks ? ar[t] : Z4); ai[t] = mfma_h(a[u], k.fi64[ks], ks ? ai[t] : Z4); }
  const float si = jj == 223 ? 0.f : -0.125f;
  v4u o0, o1;
#pragma unroll
  for (int e = 0; e < 4; ++e) {
    const int t = e >> 1, c = 2 * (e & 1);
    o0[e] = pk_h(__builtin_fmaf(ar[t][c], 0.125f, xf[2 * e]), __builtin_fmaf(ar[t][c + 1], 0.125f, xf[2 * e + 1]));
    o1[e] = pk_h(__builtin_fmaf(ai[t][c], si, xf[8 + 2 * e]), __builtin_fmaf(ai[t][c + 1], si, xf[8 + 2 * e + 1]));
  }
  st128<SC1>(o0, rw, xo, XH1_OFF + layer * XB); st128<SC1>(o1, rw, xo + 128, XH1_OFF + layer * XB);
}

DI void gen_ti(const Ids& id, int j, v4u (&T)[14]) {
  const float df = (float)(64 * j + 16 * id.w + id.l15) * (1.0f / 448.0f);
  const float base = (float)(2 * id.g + 1) * df;
  const float tc = 2.0f * vcos(16.0f * df);
  f2 v[2][4];
#pragma unroll
  for (int ks = 0; ks < 14; ++ks) {
    unsigned u[4];
#pragma unroll
    for (int e = 0; e < 4; ++e) {
      if (ks < 2) { const float x = __builtin_fmaf((float)(16 * ks + 8 * (e >> 1) + (e & 1)), df, base); v[ks][e] = f2{vcos(x), -vsin(x)}; }
      else v[ks & 1][e] = v[~ks & 1][e] * tc - v[ks & 1][e];
      float c = v[ks & 1][e][0];
      if (ks == 13 && e == 3) c *= id.g == 3 ? 0.70710678118654752f : 1.0f;
      u[e] = pk_h(c, v[ks & 1][e][1]);
    }
    T[ks] = v4u{u[0], u[1], u[2], u[3]};
    pin1(T[ks]);
  }
}
DI void s3_issue(const Ids& id, rsrc_t rw, int layer, int i, lds_t L) {
  const int vo = img_src(id, 128);
#pragma unroll
  for (int n = 0; n < 14; ++n) dma16<SC1>(rw, L + L_A + (4 * n + id.w) * 1024, vo, XH1_OFF + layer * XB + i * 57344 + (4 * n + id.w) * 1024);
}
constexpr int s3_k(int u) { return u < 36 ? u >> 2 : (u < 41 ? u - 27 : 9 + (u - 41) / 3); }
constexpr int s3_n(int u) { return u < 36 ? u & 3 : (u < 41 ? 0 : 1 + (u - 41) % 3); }
DI void s3_compute(const Ids& id, rsrc_t rw, int layer, int i, int j, lds_t L, const v4u (&T)[14], f4 (&mu)[4], f4 (&x1)[4] STV) {
  constexpr int PD = 5;
  lds_t tb[4];
#pragma unroll
  for (int n = 0; n < 4; ++n) tb[n] = L + L_A + img_rd(id, n);
  LAS float* ew = id.g == id.l15 >> 2 ? (LAS float*)(L + L_E) + 16 * id.w + id.l15 : (LAS float*)(L + L_D) + id.l15;
  LAS float* er[2] = {(LAS float*)(L + L_E) + 16 * id.w + 4 * id.g, (LAS float*)(L + L_E) + 16 * (id.w ^ 1) + 4 * id.g};
  const int e2 = (id.w & 2) ? -32 : 32;
  int so = id.lane * 16 + HF_OFF + layer * XB + ((j * 64 + 2 * i + (id.w >> 1)) * 4 + id.w) * 1024;
  pin1(ew); pin(er); pin1(so);
  vmwait<5>(); rawbar();
  STAMP(59);
  v4u a[PD];
#pragma unroll
  for (int u = 0; u < PD; ++u) a[u] = img_frag(tb[s3_n(u)] + s3_k(u) * 4096);
  SB();
  f4 acc[4], gram, rr[4];
  static_for<0, 56>([&](auto U) {
    constexpr int u = U.v, k = s3_k(u), n = s3_n(u), v = u + PD;
    const v4u cur = a[u % PD];
    if constexpr (n == 0) gram = mfma_h(cur, cur, k == 0 ? Z4 : gram);
    acc[n] = mfma_h(cur, T[k], k == 0 ? Z4 : acc[n]);
    asm volatile("" : "+a"(acc[n]));
    if constexpr (v == 36) { STAMP(60); vmwait<0>(); rawbar(); pin(mu); STAMP(61); }
    if constexpr (v < 56) a[u % PD] = img_frag(tb[s3_n(v)] + s3_k(v) * 4096);
    if constexpr (u == 42) {
      SB();
      const float vr = diag(gram, id.l15) * (1.0f / 448.0f);
      const float r1 = __builtin_amdgcn_rsqf(vr + EPS);
      *ew = r1 * __builtin_amdgcn_rsqf(vr * r1 * r1 + EPS) * 0.066815310478106f;
    }
    if constexpr (u == 47) {
      lgwait<10>(); rawbar();
      rr[0] = *(LAS f4*)er[0]; rr[1] = *(LAS f4*)er[1]; rr[2] = *(LAS f4*)(er[0] + e2); rr[3] = *(LAS f4*)(er[1] + e2);
    }
    SB();
  });
  STAMP(62);
  v2u hq[4];
#pragma unroll
  for (int n = 0; n < 4; ++n) { const f4 x = acc[n] * rr[n]; hq[n] = v2u{pk_bf(x[0], x[1]), pk_bf(x[2], x[3])}; }
  st128<SC1>(pair_frag(id.w, hq[0], hq[1]), rw, so);
  st128<SC1>(pair_frag(id.w, hq[2], hq[3]), rw, so ^ 4096);
  SB();
#pragma unroll
  for (int n = 0; n < 4; ++n) x1[n] = acc[n] * 0.066815310478106f + mu[n];
  pin(x1);
}

template <int NKS, int W0, int SO, int NS, int NX, int PRE, int NR, class Fi, class Fs>
DI void g_main(const Ids& id, rsrc_t rw, int abase, lds_t L, const v4u (&R)[NR][4], v4u (&W)[10][4], f4 (&res)[4], Fi init, Fs slot) {
  constexpr int WIN = GWIN, NU = 4 * NKS;
  v4u a[WIN];
  const int vo = id.lane * 16;
  int sb[4];
#pragma unroll
  for (int n = 0; n < 4; ++n) sb[n] = abase + id.w * NKS * 4096 + ((((n + 1) & 3) ^ id.w) << 10);
#pragma unroll
  for (int u = 0; u < WIN; ++u) a[u] = ld128<SC1>(rw, vo, sb[u / NKS] + (u % NKS) * 4096);
  lds_t wa[4], wb[4], rd[4];
#pragma unroll
  for (int n = 0; n < 4; ++n) {
    const int o = img_rd(id, n);
    wa[n] = L + L_W + id.w * WSTAGE + SO + o; wb[n] = L + L_A + id.w * 12288 + o; rd[n] = L + L_A + ((n ^ id.w) << 10) + vo;
  }
  const auto wrd = [&](int fi) { const int k = fi >> 2, n = fi & 3; W[W0 + k][n] = img_frag(k < NS ? wa[n] + k * 4096 : wb[n] + (k - NS) * 4096); };
  SB();
  vmwait<WIN>();
#pragma unroll
  for (int fi = 0; fi < 4 * PRE; ++fi) wrd(fi);
  slot();
  SB();
  f4 part[4], acc[4];
  v4u t0, t1, t2;
  static_for<0, NU>([&](auto U) {
    constexpr int u = U.v, qn = u / NKS, ks = u % NKS, v = u - 1 + WIN;
    const v4u cur = a[u % WIN];
    const auto mm = [&](int f) { if constexpr (ks < NR) acc[f] = mfma_bf(R[ks][f], cur, ks == 0 ? Z4 : acc[f]); else acc[f] = mfma_bf(W[ks - NR][f], cur, acc[f]); };
    mm(0);
    if constexpr (u >= 1 && v < NU) { SB(); a[(u - 1) % WIN] = ld128<SC1>(rw, vo, sb[v / NKS] + (v % NKS) * 4096); SB(); }
    mm(1); mm(2); mm(3);
    asm volatile("" : "+a"(acc[0]), "+a"(acc[1]), "+a"(acc[2]), "+a"(acc[3]));
    if constexpr (4 * PRE + 2 * u < 4 * (NS + NX)) { wrd(4 * PRE + 2 * u); wrd(4 * PRE + 2 * u + 1); }
    if constexpr (qn == 3 && ks == 3) wgbar();
    if constexpr (qn == 3 && ks >= 4 && ks < 8) { part[ks - 4] = (asf4(t0) + asf4(t1)) + (asf4(t2) + init(ks - 4)); pin1(part[ks - 4]); }
    if constexpr (qn == 3 && ks >= 3 && ks < 7) { lds_t rp = rd[ks - 3] + id.w * 12288; t0 = ldsr128(rp); t1 = ldsr128(rp + 4096); t2 = ldsr128(rp + 8192); }
    if constexpr (ks == NKS - 1 && qn < 3) {
      if constexpr (qn == 0) wgbar();
#pragma unroll
      for (int f = 0; f < 4; ++f) ldsw128(rd[f] + ((id.w ^ (qn + 1)) * 3 + qn) * 4096, asu4(acc[f]));
    }
    SB();
  });
#pragma unroll
  for (int f = 0; f < 4; ++f) res[f] = acc[f] + part[f];
}
DI void load_bias(const Ids& id, const unsigned short* b, int o, v2u (&bb)[4]) {
#pragma unroll
  for (int f = 0; f < 4; ++f) bb[f] = *(const GAS v2u*)(b + o + 16 * (f ^ id.w) + 4 * id.g);
}
DI f4 bias4(v2u b) { return f4{bf_lo(b.x), bf_hi(b.x), bf_lo(b.y), bf_hi(b.y)}; }
DI f4 gelu4(f4 u) {
  const f4 z = u * (u * u * (0.044715f * -2.302208198144325f) + -2.302208198144325f);
  f4 r;
#pragma unroll
  for (int e = 0; e < 4; ++e) r[e] = __builtin_amdgcn_rcpf(1.0f + __builtin_amdgcn_exp2f(z[e]));
  return u * r;
}
template <class Fn> DI void g1_epi(const Ids& id, rsrc_t rw, int layer, int j, int m, const f4 (&res)[4], Fn side) {
  v2u o[4];
#pragma unroll
  for (int q = 0; q < 4; ++q) {
    side(q);
    const f4 a = gelu4(res[q]);
    o[q] = v2u{pk_bf(a[0], a[1]), pk_bf(a[2], a[3])};
    pin1(o[q]);
  }
  const int so = id.lane * 16 + GF_OFF + layer * GB + ((j * 56 + 2 * m + (id.w >> 1)) * 4 + id.w) * 1024;
  st128<SC1>(pair_frag(id.w, o[0], o[1]), rw, so);
  st128<SC1>(pair_frag(id.w, o[2], o[3]), rw, so ^ 4096);
}
struct G2A { lds_t wp[4], tp[4]; int vo; };
DI G2A g2_addr(const Ids& id, lds_t L) {
  G2A a;
  lds_t T = L + L_A + 49152;
  const int d0 = 16 * id.w + id.l15, h0 = (d0 & 15) ^ ((d0 >> 4) & 1) ^ (((d0 >> 5) & 1) << 2), q = id.l15 >> 2, p = id.l15 & 3;
#pragma unroll
  for (int n = 0; n < 4; ++n) {
    a.wp[n] = T + d0 * 128 + (((4 * (n ^ id.w) + id.g) ^ h0) << 3);
    const int sc = 4 * id.w + (n & 2) + (id.g >> 1), d = 8 * (4 * (id.g & 1) + p) + 4 * (n & 1) + q;
    a.tp[n] = T + d * 128 + ((sc ^ (d & 15) ^ ((d >> 4) & 1) ^ (((d >> 5) & 1) << 2)) << 3);
  }
  a.vo = (16 * id.w + 4 * (id.lane >> 5) + (id.lane & 3)) * 896 + 16 * ((id.lane >> 2) & 7);
  pin(a.wp); pin(a.tp); pin1(a.vo);
  return a;
}
DI void g2_epi(const G2A& a, rsrc_t ro, int obase, const f4 (&res)[4]) {
#pragma unroll
  for (int q = 0; q < 4; ++q) *(LAS v2u*)a.wp[q] = v2u{pk_bf(res[q][0], res[q][1]), pk_bf(res[q][2], res[q][3])};
  wgbar();
  const v4u o0 = cat(trread(a.tp[0]), trread(a.tp[1])), o1 = cat(trread(a.tp[2]), trread(a.tp[3]));
  SB();
  st128<SC1>(o0, ro, a.vo, obase); st128<SC1>(o1, ro, a.vo, obase + 8 * 896);
}

constexpr int C_EP = 0, C_FL = 1, C_H = 15, C_F0 = 16, C_P = C_F0 + 2 * 128, C_S3 = C_P + 2 * 128, C_G1 = C_S3 + 2 * 7 * 32, C_G2 = C_G1 + 2 * 7 * 32, C_LINES = C_G2 + 32 * 8;
static_assert(C_LINES * 128 <= CTRL_BYTES && C_LINES <= C_F0 + 128 + 7 * 256, "control block");
constexpr unsigned SPIN_CAP = 1u << 20;
DI unsigned ld_word(unsigned char* ws, int line) { return __hip_atomic_load((gu32*)ws + 32 * line, __ATOMIC_RELAXED, __HIP_MEMORY_SCOPE_AGENT); }
DI void st_word(unsigned char* ws, int line, unsigned v) { __hip_atomic_store((gu32*)ws + 32 * line, v, __ATOMIC_RELAXED, __HIP_MEMORY_SCOPE_AGENT); }
template <int NS, int NR, int YOUNG = 0, class Fn> DI void seam_arrive(const Ids& id, unsigned char* ws, int line0, int shard, Fn work, bool on = true) {
  SB(); work(); SB();
  vmwait<YOUNG>();
  wgbar();
  if (on && id.w == 0 && id.lane < NR) __hip_atomic_fetch_add((gu32*)ws + 32 * (line0 + id.lane * NS + shard), 1u, __ATOMIC_RELAXED, __HIP_MEMORY_SCOPE_AGENT);
}
template <int NS> DI void seam_wait(const Ids& id, unsigned char* ws, int line0, int rep, unsigned want) {
  if (id.w == 0) {
    const int line = line0 + rep * NS + (id.lane & (NS - 1));
    for (unsigned sp = 0; sp < SPIN_CAP; ++sp)
      if (__all(ld_word(ws, line) >= want)) break;
    __builtin_amdgcn_fence(__ATOMIC_ACQUIRE, "wavefront");
  }
  wgbar();
}
template <int NS> DI void poll_early(const Ids& id, rsrc_t rw, lds_t L, int line0, int rep) {
  if (EARLY && id.w == 0) dma4_sc1(rw, L + L_D, 128 * (line0 + rep * NS + (id.lane & (NS - 1))));
}
template <int NS, int YOUNG> DI void seam_wait_early(const Ids& id, unsigned char* ws, lds_t L, int line0, int rep, unsigned want) {
  if (id.w == 0) {
    bool ok = false;
    if (EARLY) { vmwait<YOUNG>(); ok = __all(((volatile LAS unsigned*)(L + L_D))[id.lane] >= want); }
    if (!ok) {
      const int line = line0 + rep * NS + (id.lane & (NS - 1));
      for (unsigned sp = 0; sp < SPIN_CAP; ++sp)
        if (__all(ld_word(ws, line) >= want)) break;
    }
    __builtin_amdgcn_fence(__ATOMIC_ACQUIRE, "wavefront");
  }
  wgbar();
}
DI void wait_init(const Ids& id, unsigned char* ws, int rep, unsigned tag) {
  if (id.w == 0) {
    for (unsigned sp = 0; sp < SPIN_CAP; ++sp)
      if (__all(ld_word(ws, C_FL + rep) == tag)) break;
    __builtin_amdgcn_fence(__ATOMIC_ACQUIRE, "wavefront");
  }
}

DI void poll_ge(unsigned char* ws, int line, unsigned want) {
  for (unsigned sp = 0; sp < SPIN_CAP; ++sp) {
    if (__all(ld_word(ws, line) >= want)) break;
    __builtin_amdgcn_s_sleep(12);
  }
}

template <class T> DI T* fresh_p(T* v) { asm volatile("" : "+s"(v)); return v; }
DI Params fresh_p(const Params& a) { return Params{fresh_p(a.x), fresh_p(a.wr), fresh_p(a.wi), fresh_p(a.w1), fresh_p(a.b1), fresh_p(a.w2), fresh_p(a.b2), fresh_p(a.out), fresh_p(a.ws)}; }

DI void helper(const Params& p, lds_t L, const Ids& id0 STV) {
  const int b = blockIdx.x, q = b >> 3, x8 = b & 7;
  const auto nop = []() {};
  const unsigned want0 = (id0.lane & 15) < 8 ? 15u : 14u;
  if (q == 28) {
    const unsigned tag = ld_word(p.ws, C_EP) + 1u;
    if (b == 224) {
      if (id0.w == 0) {
        st_word(p.ws, C_F0 + id0.lane, 0u); st_word(p.ws, C_F0 + 64 + id0.lane, 0u);
        if (id0.lane == 0) st_word(p.ws, C_H, 0u);
        vmwait<0>();
        STAMP(27);
        if (id0.lane < 8) st_word(p.ws, C_FL + id0.lane, tag);
        vmwait<0>();
        if (id0.lane < 8) __hip_atomic_fetch_add((gu32*)p.ws + 32 * (C_F0 + id0.lane * 16), 1u, __ATOMIC_RELAXED, __HIP_MEMORY_SCOPE_AGENT);
        __builtin_amdgcn_s_sleep(96);
        poll_ge(p.ws, C_F0 + (id0.lane & 15), want0);
        poll_ge(p.ws, C_H, 24u);
        if (id0.lane < 9) st_word(p.ws, id0.lane == 8 ? C_EP : C_FL + id0.lane, id0.lane == 8 ? tag : tag - 1u);
      }
    } else {
      const int n = C_F0 + 128 + (b - 225) * 256 + (int)threadIdx.x;
      if (n < C_LINES) st_word(p.ws, n, 0u);
      wait_init(id0, p.ws, x8, tag);
      seam_arrive<16, 8>(id0, p.ws, C_F0, x8, nop);
    }
    STAMP(26);
    STAMP_OUT();
    return;
  }
  {
    const unsigned tag = ld_word(p.ws, C_EP) + 1u;
    const rsrc_t r2 = mkrs(p.w2, S * F * 2), r1 = mkrs(p.w1, x8 < 7 ? S * F * 2 : 0);
    const int h = 4 * (q - 29) + id0.w;
    const auto t1 = [&](int n0, int n1) { for (int n = h + n0; n < n1; n += 12) touch(r1, L + L_D, id0.lane * (F * 2), ((n >> 2 & 3) * 512 + (n >> 4) * 64) * F * 2 + (4 * x8 + (n & 3)) * 128); };
    const auto t2 = [&](int n0, int n1) { for (int n = h + n0; n < n1; n += 12) touch(r2, L + L_D, id0.lane * (S * 2), ((n >> 2 & 3) * 448 + (n >> 4) * 64) * S * 2 + (4 * x8 + (n & 3)) * 128); };
    t1(0, 96); t2(0, 48); t1(96, 128);
    vmwait<0>();
    STAMP(26);
    if (id0.w == 0) {
      wait_init(id0, p.ws, x8, tag);
      if (id0.lane == 0) __hip_atomic_fetch_add((gu32*)p.ws + 32 * C_H, 1u, __ATOMIC_RELAXED, __HIP_MEMORY_SCOPE_AGENT);
      poll_ge(p.ws, C_F0 + x8 * 16 + (id0.lane & 15), want0);
    }
#pragma unroll 1
    for (int st = 0; st < 3; ++st) {
      if (id0.w == 0) poll_ge(p.ws, st == 1 ? C_G2 + 4 * x8 * 8 + 7 : C_S3 + (st >> 1) * 7 * 32 + x8 * 4, st == 1 ? 7u : TRIG);
      wgbar();
      if (st == 1) t1(48, 128); else t2(32, 112);
      vmwait<0>();
    }
    STAMP_OUT();
  }
}

__global__ void __launch_bounds__(256, 1) k_all(Params p0) {
  extern __shared__ __attribute__((aligned(16))) unsigned char smem[];
  lds_t L = (lds_t)smem;
  const Ids id0 = get_ids();
  const auto nop = []() {};
  STAMP(0);
  if (blockIdx.x >= 224) { helper(p0, L, id0 STA); return; }
#define ID fresh(id0)
#define W1S w_src(ID, p.w1, fresh_s(g1 ? S * F * 2 : 0), F * 2, 512, fresh_s(m) * 128, L)
#define W2S w_src(ID, p.w2, S * F * 2, S * 2, 448, fresh_s(i) * 128, L)
#define STAGE (L + L_W + fresh_s(id0.w) * WSTAGE)
#define ASTAGE (L + L_A + fresh_s(id0.w) * 12288)
  v4u R1[NR1][4], R2[NR2][4];
#pragma unroll 1
  for (int layer = 0; layer < 2; ++layer) {
    const int sb = layer * 30; (void)sb;
    const int b = fresh_s(blockIdx.x), q = b >> 3, x8 = b & 7, i = 4 * x8 + q / 7, j = q % 7, m = i, jj = x8 * 28 + q;
    const bool g1 = i < 28;
    PC k;
    gen_pc_a(ID, k);
    SB();
    const Params p = fresh_p(p0);
    const rsrc_t rw = mkrs(p.ws, WS_BYTES);
    if (layer == 0) {
      STAMP(28);
      f0_issue<0>(ID, mkrs(p.x, XB), 0, i, L);
      STAMP(24);
      wt_issue(p, ID, x8, q / 7, j, L);
      if (threadIdx.x == 0) dma4_sc1(rw, L + L_D + 256, C_EP * 128);
      STAMP(25);
    }
    if (layer == 1) {
      seam_arrive<1, 8>(ID, p.ws, C_G2 + i * 8, 0, nop);
      STAMP(23);
    }
    v4u T[14];
    {
      gen_tf(ID, j, T);
      if (layer == 1) {
        seam_wait<1>(ID, p.ws, C_G2 + i * 8, j, 7u);
        f0_issue<SC1>(ID, rw, X2_OFF, i, L);
      }
      STAMP(sb + 1);
      f0_compute(ID, rw, layer, i, j, L, T);
      STAMP(sb + 2);
      if (layer == 0) {
        if (threadIdx.x == 0) dma4_sc1(rw, L + L_D + 260, (C_FL + x8) * 128);
        wt_store(rw, x8, q / 7, q % 7, L);
      }
      STAMP(sb + 3);
      seam_arrive<16, 8>(ID, p.ws, C_F0 + layer * 128, b & 15, [&]() {
        gen_pc_b(ID, k);
        if (layer == 0 && id0.w == 0) {
          vmwait<0>();
          const unsigned tag = ((const volatile LAS unsigned*)(L + L_D))[64] + 1u, fl = ((const volatile LAS unsigned*)(L + L_D))[65];
          if (__builtin_amdgcn_readfirstlane(fl != tag)) wait_init(id0, p.ws, x8, tag);
        } });
      STAMP(sb + 4);
      if (layer == 0) { const WSrc ws = W1S; w_issue<0, 4 * NR1>(ws, 0, ws.stage); }
      else __builtin_amdgcn_s_sleep(10);
      STAMP(sb + 5);
      seam_wait<16>(ID, p.ws, C_F0 + layer * 128, x8, layer == 0 && (ID.lane & 15) < 8 ? 15u : 14u);
      STAMP(sb + 6);
      p_phase(ID, rw, layer, jj, L, k, [&]() {
        if (layer == 0) { vmwait<5>(); w_read<NR1, 0>(ID, STAGE, R1); lgwait<0>(); pin_acc<0, NR1>(R1); }
        gen_ti(ID, j, T); } STA);
    }
    STAMP(sb + 7);
    v4u W[10][4];
    f4 x1[4];
    {
      seam_arrive<16, 8, 12>(ID, p.ws, C_P + layer * 128, b & 15, [&]() { const WSrc ws = W1S; w_issue<0, 12>(ws, NR1, ws.stage); });
      STAMP(sb + 8);
      f4 mu[4];
      {
        const Ids id = ID;
#pragma unroll
        for (int n = 0; n < 4; ++n) mu[n] = asf4(ld128<SC1>(rw, (16 * (n ^ id.w) + 4 * id.g) * 4, MU_OFF + (layer * S + 64 * i) * 4));
      }
      { const WSrc ws = W1S; w_issue<12, 19>(ws, NR1, ws.stage); poll_early<16>(ID, rw, L, C_P + layer * 128, x8); w_issue<19, 24>(ws, NR1, ws.stage); }
      STAMP(sb + 9);
      seam_wait_early<16, 5>(ID, p.ws, L, C_P + layer * 128, x8, 14u);
      STAMP(sb + 10);
      s3_issue(ID, rw, layer, i, L);
      vmwait<14>();
      w_read<6, 0>(ID, STAGE, W);
      STAMP(sb + 11);
      s3_compute(ID, rw, layer, i, j, L, T, mu, x1 STA);
      pin_acc<0, 3>(W);
      STAMP(sb + 12);
    }
    if (layer == 0) { const WSrc ws = W2S; w_issue<0, 8>(ws, 0, ws.stage); }
    seam_arrive<4, 8, 12>(ID, p.ws, C_S3 + (layer * 7 + j) * 32, i & 3, [&]() {
      { const WSrc ws = W1S; w_issue<0, 4>(ws, NR1 + 6, ws.stage + 20480); }
      if (layer == 1) vmwait<4>(); });
    STAMP(sb + 13);
    v2u bq[4];
    f4 res[4];
    {
      const rsrc_t rg = mkrs(p.ws, fresh_s(g1 ? WS_BYTES : 0));
      load_bias(ID, p.b1, g1 ? 64 * m : 0, bq);
      if (layer == 0) { const WSrc ws = W2S; w_issue<8, 4 * NR2>(ws, 0, ws.stage); }
      { const WSrc ws = W1S; lds_t as = ASTAGE; w_issue<0, 7>(ws, NR1 + 7, as); poll_early<4>(ID, rw, L, C_S3 + (layer * 7 + j) * 32, x8); w_issue<7, 12>(ws, NR1 + 7, as); }
      STAMP(sb + 14);
      seam_wait_early<4, 5>(ID, p.ws, L, C_S3 + (layer * 7 + j) * 32, x8, 8u);
      pin(bq);
      STAMP(sb + 15);
      g_main<16, 6, 20480, 1, 3, 0>(ID, rg, HF_OFF + layer * XB + j * 64 * 4096, L, R1, W, res, [&](int f) { return bias4(bq[f]); }, nop);
      STAMP(sb + 16);
      if (layer == 0) { w_read<NR2, 0>(ID, STAGE, R2); lgwait<0>(); pin_acc<0, NR2>(R2); }
      const WSrc ws = W2S;
      g1_epi(ID, rg, layer, j, m, res, [&](int f) {
#pragma unroll
        for (int n = 0; n < EPI_D / 4; ++n) w_piece(ws, NR2, EPI_D / 4 * f + n, ws.stage); });
      STAMP(sb + 17);
      seam_arrive<4, 8, DRN_D>(ID, p.ws, C_G1 + (layer * 7 + j) * 32, m & 3, [&]() { w_issue<EPI_D, EPI_D + DRN_D>(ws, NR2, ws.stage); }, g1);
      STAMP(sb + 18);
    }
    load_bias(ID, p.b2, 64 * i, bq);
    { const WSrc ws = W2S; lds_t as = ASTAGE; w_issue<EPI_D + DRN_D, 24>(ws, NR2, ws.stage); w_issue<0, 2>(ws, NR2 + 6, as);
      poll_early<4>(ID, rw, L, C_G1 + (layer * 7 + j) * 32, x8); w_issue<2, 12>(ws, NR2 + 6, as); }
    const G2A ga = g2_addr(ID, L);
    STAMP(sb + 19);
    seam_wait_early<4, 10>(ID, p.ws, L, C_G1 + (layer * 7 + j) * 32, x8, 7u);
    pin(bq);
    STAMP(sb + 20);
    g_main<14, 0, 0, 6, 3, G2PRE>(ID, rw, GF_OFF + layer * GB + j * 56 * 4096, L, R2, W, res, [&](int f) { return bias4(bq[f]) + x1[f]; }, nop);
    STAMP(sb + 21);
    g2_epi(ga, layer == 0 ? rw : mkrs(p.out, XB), (layer == 0 ? X2_OFF : 0) + 64 * i * 896 + j * 128, res);
    STAMP(sb + 22);
  }
  STAMP(53);
  const Params& p = p0; (void)p;
  STAMP_OUT();
}

extern "C" void kernel_launch(void* const* d_in, const int* in_sizes, int n_in, void* d_out, int out_size, void* d_ws, size_t ws_size, hipStream_t stream) {
  Params p;
  p.x = (const unsigned short*)d_in[0]; p.wr = (const unsigned short*)d_in[1]; p.wi = (const unsigned short*)d_in[2];
  p.w1 = (const unsigned short*)d_in[4]; p.b1 = (const unsigned short*)d_in[5]; p.w2 = (const unsigned short*)d_in[6]; p.b2 = (const unsigned short*)d_in[7];
  p.out = (unsigned short*)d_out; p.ws = (unsigned char*)d_ws;
  (void)hipFuncSetAttribute((const void*)k_all, hipFuncAttributeMaxDynamicSharedMemorySize, LDS_BYTES);
  k_all<<<256, 256, LDS_BYTES, stream>>>(p);
}
```

```cpp
#include <hip/hip_runtime.h>
#include <hip/hip_bf16.h>
#include <stdint.h>

#define DI __device__ __forceinline__
#define GAS __attribute__((address_space(1)))
#define LAS __attribute__((address_space(3)))
typedef unsigned v4u __attribute__((ext_vector_type(4)));
typedef unsigned v2u __attribute__((ext_vector_type(2)));
typedef float f4 __attribute__((ext_vector_type(4)));
typedef float f2 __attribute__((ext_vector_type(2)));
typedef _Float16 h8 __attribute__((ext_vector_type(8)));
typedef _Float16 h2 __attribute__((ext_vector_type(2)));
typedef __bf16 b8 __attribute__((ext_vector_type(8)));
typedef __bf16 b2 __attribute__((ext_vector_type(2)));
typedef short s4 __attribute__((ext_vector_type(4)));
typedef __amdgpu_buffer_rsrc_t rsrc_t;
typedef LAS unsigned char* lds_t;
typedef GAS unsigned gu32;

#define GWIN 11
#define DRN_D 9
#define G2PRE 3
#define PD_D 6
#define PP_D 15
#define CH3 1
#define HMODE 0
#define F0P 15
#define G2P 3
#define S3PRE 3
#define PCA 1
#define F0SL 13
#define TRIG 1u
#define EARLY 1

constexpr int S = 2048, D = 448, F = 1792, NKF = 225;
constexpr float EPS = 1e-5f;
constexpr int XB = S * D * 2;
constexpr int GB = D * F * 2;
constexpr int SC1 = 16;
constexpr int CTRL_BYTES = 262144;
constexpr int MU_OFF = CTRL_BYTES;
constexpr int RS_OFF = MU_OFF + 2 * S * 4;
constexpr int WT_OFF = RS_OFF + 2 * S * 2;
constexpr int XH0_OFF = WT_OFF + XB;
constexpr int XH1_OFF = XH0_OFF + 2 * XB;
constexpr int HF_OFF = XH1_OFF + 2 * XB;
constexpr int GF_OFF = HF_OFF + 2 * XB;
constexpr int X2_OFF = GF_OFF + 2 * GB;
constexpr int WS_BYTES = X2_OFF + XB;
static_assert(HF_OFF % 8192 == 0 && XB % 8192 == 0 && GF_OFF % 8192 == 0 && GB % 8192 == 0, "fragment pairs are addressed by an exclusive or");
#define STAMP(n)
#define STAMPE(n)
#define STV
#define STA
#define STAMP_OUT()
constexpr int L_A = 0, L_E = 57344, L_R = L_E + 512, L_D = L_R + 1024, L_W = 59392, WSTAGE = 24576, LDS_BYTES = L_W + 4 * WSTAGE;
constexpr int NR1 = 6, NR2 = 5;

struct Params {
  const unsigned short* x; const unsigned short* wr; const unsigned short* wi;
  const unsigned short* w1; const unsigned short* b1; const unsigned short* w2; const unsigned short* b2;
  unsigned short* out; unsigned char* ws;
};

DI rsrc_t mkrs(const void* p, int bytes) { return __builtin_amdgcn_make_buffer_rsrc((void*)p, 0, bytes, 0x00020000); }
DI f4 mfma_bf(v4u a, v4u b, f4 c) { return __builtin_amdgcn_mfma_f32_16x16x32_bf16(__builtin_bit_cast(b8, a), __builtin_bit_cast(b8, b), c, 0, 0, 0); }
DI f4 mfma_h(v4u a, v4u b, f4 c) { return __builtin_amdgcn_mfma_f32_16x16x32_f16(__builtin_bit_cast(h8, a), __builtin_bit_cast(h8, b), c, 0, 0, 0); }
DI unsigned pk_bf(float a, float b) { f2 v = {a, b}; return __builtin_bit_cast(unsigned, __builtin_convertvector(v, b2)); }
DI unsigned pk_h(float a, float b) { f2 v = {a, b}; return __builtin_bit_cast(unsigned, __builtin_convertvector(v, h2)); }
DI float h_lo(unsigned u) { return (float)__builtin_bit_cast(h2, u)[0]; }
DI float h_hi(unsigned u) { return (float)__builtin_bit_cast(h2, u)[1]; }
DI float bf_lo(unsigned u) { return __builtin_bit_cast(float, u << 16); }
DI float bf_hi(unsigned u) { return __builtin_bit_cast(float, u & 0xffff0000u); }
DI unsigned mul_h2(unsigned a, unsigned b) { return __builtin_bit_cast(unsigned, __builtin_bit_cast(h2, a) * __builtin_bit_cast(h2, b)); }
DI float vcos(float x) { return __builtin_amdgcn_cosf(x); }
DI float vsin(float x) { return __builtin_amdgcn_sinf(x); }
DI v2u trread(lds_t p) { return __builtin_bit_cast(v2u, __builtin_amdgcn_ds_read_tr16_b64_v4i16((LAS s4*)p)); }
DI v4u cat(v2u a, v2u b) { return v4u{a.x, a.y, b.x, b.y}; }
DI v4u ldsr128(lds_t p) { return *(LAS v4u*)p; }
DI void ldsw128(lds_t p, v4u v) { *(LAS v4u*)p = v; }
template <int AUX> DI void dma16(rsrc_t rs, lds_t dst, int voff, int soff) {
  const unsigned la = (unsigned)(size_t)dst;
  if constexpr (AUX == SC1) asm volatile("s_mov_b32 m0, %0\n\ts_nop 0\n\tbuffer_load_dwordx4 %1, %2, %3 offen sc1 lds" ::"s"(la), "v"(voff), "s"(rs), "s"(soff) : "memory");
  else asm volatile("s_mov_b32 m0, %0\n\ts_nop 0\n\tbuffer_load_dwordx4 %1, %2, %3 offen lds" ::"s"(la), "v"(voff), "s"(rs), "s"(soff) : "memory");
}
template <int N> DI void dma_run(rsrc_t rs, lds_t dst, int voff, int soff, int sstep) {
  constexpr int R = N % 3, Q = N / 3;
#pragma unroll
  for (int n = 0; n < R; ++n) dma16<0>(rs, dst + n * 1024, voff, soff + n * sstep);
  if constexpr (Q > 0) {
    const unsigned la = (unsigned)(size_t)dst + R * 1024;
    int so = soff + R * sstep;
    unsigned cnt;
    asm volatile("s_mov_b32 m0, %2\n\ts_mov_b32 %1, %7\n1:\n\tbuffer_load_dwordx4 %3, %4, %0 offen lds\n\ts_add_u32 %0, %0, %5\n\t"
                 "buffer_load_dwordx4 %3, %4, %0 offen offset:1024 lds\n\ts_add_u32 %0, %0, %5\n\tbuffer_load_dwordx4 %3, %4, %0 offen offset:2048 lds\n\t"
                 "s_add_u32 m0, m0, 3072\n\ts_add_u32 %0, %0, %6\n\ts_sub_u32 %1, %1, 1\n\ts_cbranch_scc0 1b"
                 : "+s"(so), "=&s"(cnt) : "s"(la), "v"(voff), "s"(rs), "s"(sstep - 1024), "s"(sstep + 2048), "n"(Q - 1) : "memory", "scc");
  }
}
template <int S0, int S1> DI void dma_tile(rsrc_t rs, lds_t dst, int voff, int so) {
  const unsigned la = (unsigned)(size_t)dst;
  unsigned cnt;
  asm volatile("s_mov_b32 m0, %2\n\ts_mov_b32 %1, 6\n1:\n\tbuffer_load_dwordx4 %3, %4, %0 offen sc1 lds\n\ts_add_u32 m0, m0, 4096\n\ts_add_u32 %0, %0, %5\n\t"
               "buffer_load_dwordx4 %3, %4, %0 offen sc1 lds\n\ts_add_u32 m0, m0, 4096\n\ts_add_u32 %0, %0, %6\n\ts_sub_u32 %1, %1, 1\n\ts_cbranch_scc0 1b"
               : "+s"(so), "=&s"(cnt) : "s"(la), "v"(voff), "s"(rs), "n"(S0), "n"(S1) : "memory", "scc");
}
DI void ald128(v4u& o, rsrc_t rs, int voff, int soff) { asm volatile("buffer_load_dwordx4 %0, %1, %2, %3 offen sc1" : "=v"(o) : "v"(voff), "s"(rs), "s"(soff) : "memory"); }
DI void touch(rsrc_t rs, lds_t dummy, int voff, int soff) {
  const unsigned la = (unsigned)(size_t)dummy;
  asm volatile("s_mov_b32 m0, %0\n\ts_nop 0\n\tbuffer_load_dword %1, %2, %3 offen lds" ::"s"(la), "v"(voff), "s"(rs), "s"(soff) : "memory");
}
DI void dma4_sc1(rsrc_t rs, lds_t dst, int voff) {
  const unsigned la = (unsigned)(size_t)dst;
  asm volatile("s_mov_b32 m0, %0\n\ts_nop 0\n\tbuffer_load_dword %1, %2, 0 offen sc1 lds" ::"s"(la), "v"(voff), "s"(rs) : "memory");
}
template <int AUX> DI v4u ld128(rsrc_t rs, int voff, int soff = 0) { return __builtin_amdgcn_raw_buffer_load_b128(rs, voff, soff, AUX); }
template <int AUX> DI void st128(v4u v, rsrc_t rs, int voff, int soff = 0) { __builtin_amdgcn_raw_buffer_store_b128(v, rs, voff + soff, 0, AUX); }
DI f4 asf4(v4u v) { return __builtin_bit_cast(f4, v); }
DI v4u asu4(f4 v) { return __builtin_bit_cast(v4u, v); }
template <int N> DI void vmwait() { asm volatile("s_waitcnt vmcnt(%0)" ::"n"(N) : "memory"); }
template <int N> DI void lgwait() { asm volatile("s_waitcnt lgkmcnt(%0)" ::"n"(N) : "memory"); }
DI void rawbar() { asm volatile("" ::: "memory"); __builtin_amdgcn_s_barrier(); asm volatile("" ::: "memory"); }
DI void wgbar() { lgwait<0>(); rawbar(); }
#define SB() __builtin_amdgcn_sched_barrier(0)
template <class T> DI void pin1(T& v) { asm volatile("" : "+v"(v)); }
template <class T, int N> DI void pin(T (&a)[N]) {
#pragma unroll
  for (int n = 0; n < N; ++n) pin1(a[n]);
}
template <int A, int B, int N> DI void pin_acc(v4u (&W)[N][4]) {
#pragma unroll
  for (int ks = A; ks < B; ++ks)
#pragma unroll
    for (int n = 0; n < 4; ++n) asm volatile("" : "+a"(W[ks][n]));
}
constexpr f4 Z4 = {0.f, 0.f, 0.f, 0.f};
template <int V> struct IC { static constexpr int v = V; };
template <int A, int B, class Fn> DI void static_for(Fn&& fn) { if constexpr (A < B) { fn(IC<A>{}); static_for<A + 1, B>(fn); } }

DI float diag(const f4& c, int l15) { const float a = l15 & 1 ? c[1] : c[0], b = l15 & 1 ? c[3] : c[2]; return l15 & 2 ? b : a; }

struct Ids { int lane, w, l15, g; };
DI Ids get_ids() { Ids d; d.lane = threadIdx.x & 63; d.w = __builtin_amdgcn_readfirstlane(threadIdx.x >> 6); d.l15 = d.lane & 15; d.g = d.lane >> 4; return d; }
DI Ids fresh(const Ids& a) { Ids d; d.lane = a.lane; d.w = a.w; asm volatile("" : "+v"(d.lane), "+s"(d.w)); d.l15 = d.lane & 15; d.g = d.lane >> 4; return d; }
DI int fresh_s(int v) { asm volatile("" : "+s"(v)); return v; }

DI int img_src(const Ids& id, int pitch) { const int r7 = id.lane >> 3; return r7 * pitch + (((id.lane & 7) ^ (r7 & 6)) << 4); }
DI int img_rd(const Ids& id, int n) { const int row = 4 * id.g + (id.l15 >> 2); return row * 128 + 32 * (n ^ id.w ^ ((row >> 1) & 3)) + 8 * (id.l15 & 3); }
DI v4u img_frag(lds_t p) { return cat(trread(p), trread(p + 2048)); }
struct WSrc { rsrc_t rs; int pitch, base, vo; lds_t stage; };
DI WSrc w_src(const Ids& id, const void* w, int bytes, int pitch, int rows_per_wave, int col0, lds_t L) {
  return WSrc{mkrs(w, bytes), pitch, id.w * rows_per_wave * pitch + col0, img_src(id, pitch), L + L_W + id.w * WSTAGE};
}
DI void w_piece(const WSrc& ws, int k0, int n, lds_t dst) { dma16<0>(ws.rs, dst + n * 1024, ws.vo, ws.base + (32 * k0 + 8 * n) * ws.pitch); }
template <int N0, int N1> DI void w_issue(const WSrc& ws, int k0, lds_t dst) {
  dma_run<N1 - N0>(ws.rs, dst + N0 * 1024, ws.vo, ws.base + (32 * k0 + 8 * N0) * ws.pitch, 8 * ws.pitch);
}
template <int CNT, int O, int N> DI void w_read(const Ids& id, lds_t stage, v4u (&W)[N][4]) {
  lds_t b[4];
#pragma unroll
  for (int n = 0; n < 4; ++n) b[n] = stage + img_rd(id, n);
#pragma unroll
  for (int ks = 0; ks < CNT; ++ks)
#pragma unroll
    for (int n = 0; n < 4; ++n) W[O + ks][n] = img_frag(b[n] + ks * 4096);
}
DI v4u pair_frag(int w, v2u a, v2u b) { const bool sw = w & 1; return v4u{sw ? b.x : a.x, sw ? b.y : a.y, sw ? a.x : b.x, sw ? a.y : b.y}; }

template <class Fn> DI void gen_tf(const Ids& id, int j, v4u (&T)[14], Fn mid) {
  const int c = 64 * j + 16 * id.w + id.l15;
  const bool dc = c == 447;
  const float kf = dc ? 0.f : (float)((c >> 1) + 1) * (1.0f / 448.0f);
  const float base = __builtin_fmaf((float)(8 * id.g), kf, (c & 1) && !dc ? 0.25f : 0.f);
  const float tc = 2.0f * vcos(32.0f * kf);
  f2 v[2][4];
#pragma unroll
  for (int ks = 0; ks < 14; ++ks) {
    unsigned u[4];
#pragma unroll
    for (int e = 0; e < 4; ++e) {
      if (ks < 2) v[ks][e] = f2{vcos(__builtin_fmaf((float)(32 * ks + 2 * e), kf, base)), vcos(__builtin_fmaf((float)(32 * ks + 2 * e + 1), kf, base))};
      else v[ks & 1][e] = v[~ks & 1][e] * tc - v[ks & 1][e];
      u[e] = pk_bf(v[ks & 1][e][0], v[ks & 1][e][1]);
    }
    T[ks] = v4u{u[0], u[1], u[2], u[3]};
    pin1(T[ks]);
    if (ks == 7) mid();
  }
}
DI void f0_issue(const Ids& id, rsrc_t rx, int xbase, int i, lds_t L) {
  const int r7 = id.lane >> 3;
  const int vo = r7 * 896 + (((id.lane & 7) ^ (2 * id.w + ((r7 >> 1) & 1))) << 4);
  dma_tile<28672, -28544>(rx, L + L_A + id.w * 1024, vo, xbase + (64 * i + 8 * id.w) * 896);
}
DI void f0_compute(const Ids& id, rsrc_t rw, int layer, int i, int j, lds_t L, const v4u (&T)[14]) {
  constexpr int YOUNG = 0, YOUNG0 = 3;
  constexpr int PD = 8;
  const int rho = id.l15, xr = (rho >> 1) & 7;
  const int rowb = (8 * (rho >> 2) + (rho & 3)) * 128, tw = 4096 * (id.w >> 1) + 512 * (id.w & 1);
  lds_t pa[2][4];
#pragma unroll
  for (int n = 0; n < 4; ++n) {
    const int o = rowb + ((4096 * (n >> 1) + 512 * (n & 1)) ^ tw);
    pa[0][n] = L + L_A + o + ((id.g ^ xr) << 4); pa[1][n] = L + L_A + o + (((id.g ^ xr) << 4) ^ 64);
  }
  if (layer == 0) vmwait<10 + YOUNG0>(); else vmwait<10 + YOUNG>();
  rawbar();
  v4u a[PD];
#pragma unroll
  for (int u = 0; u < PD; ++u) a[u] = ldsr128(pa[(u >> 2) & 1][u & 3] + (u >> 3) * 8192);
  SB();
  f4 acc[4];
  f4 gram = Z4;
#pragma unroll
  for (int u = 0; u < 56; ++u) {
    const int ks = u >> 2, n = u & 3, v = u + PD;
    const v4u cur = a[u % PD];
    acc[n] = mfma_bf(cur, T[ks], ks == 0 ? Z4 : acc[n]);
    if (n == 0) gram = mfma_bf(cur, cur, gram);
    if (v == 16) { if (layer == 0) vmwait<4 + YOUNG0>(); else vmwait<4 + YOUNG>(); rawbar(); }
    if (v == 40) { if (layer == 0) vmwait<YOUNG0>(); else vmwait<YOUNG>(); rawbar(); }
    if (v < 56) a[u % PD] = ldsr128(pa[(v >> 2) & 1][v & 3] + (v >> 3) * 8192);
    SB();
  }
  const int c = 64 * j + 16 * id.w + id.l15;
  LAS float* eb = (LAS float*)(L + L_E);
  if (id.g == rho >> 2) eb[32 * (id.w >> 1) + 4 * (id.w & 1) + 8 * (rho >> 2) + (rho & 3)] = diag(gram, rho);
  if (id.w == 3 && rho == 15) {
#pragma unroll
    for (int n = 0; n < 4; ++n) *(LAS f4*)(eb + 64 + 32 * ((n ^ 3) >> 1) + 4 * ((n ^ 3) & 1) + 8 * id.g) = acc[n];
  }
  SB();
  const float sc = c == 447 ? 0.f : (c == 446 ? 0.047245559126153f : 0.066815310478106f);
#pragma unroll
  for (int q = 0; q < 2; ++q) {
    const f4 x = acc[2 * q] * sc, y = acc[2 * q + 1] * sc;
    const unsigned x0 = pk_h(x[0], x[1]), x1 = pk_h(x[2], x[3]), y0 = pk_h(y[0], y[1]), y1 = pk_h(y[2], y[3]);
    const bool sw = id.w & 1;
    st128<SC1>(v4u{sw ? y0 : x0, sw ? y1 : x1, sw ? x0 : y0, sw ? x1 : y1}, rw, c * 128 + 16 * id.g, XH0_OFF + layer * XB + i * 57344 + 64 * (q ^ (id.w >> 1)));
  }
  vmwait<2>();
  wgbar();
  if (j == 6 && id.w == 0) {
    const float m = eb[64 + id.lane] * (1.0f / 448.0f);
    const float r = __builtin_amdgcn_rsqf(__builtin_fmaxf(__builtin_fmaf(-m, m, eb[id.lane] * (1.0f / 448.0f)), 0.f) + EPS);
    __builtin_amdgcn_raw_buffer_store_b32(__builtin_bit_cast(unsigned, m), rw, id.lane * 4, MU_OFF + (layer * S + 64 * i) * 4, SC1);
    __builtin_amdgcn_raw_buffer_store_b16((unsigned short)(pk_h(r, r) & 0xffffu), rw, id.lane * 2, RS_OFF + (layer * S + 64 * i) * 2, SC1);
  }
}
constexpr int WT_LD = 3;
DI void wt_issue(const Params& p, const Ids& id, int kg, int g, int cg, lds_t L) {
  const rsrc_t rs = mkrs(id.w < 2 ? p.wr : p.wi, S * NKF * 2);
#pragma unroll
  for (int n = 0; n < WT_LD; ++n) {
    const int pc = (id.w & 1) * 192 + n * 64 + id.lane, row = pc / 5, ch = pc - 5 * row;
    const int vo = ((((row & 7) + 64 * ((row >> 3) & 7)) * NKF + 1) * 2 & ~3) + 16 * ch;
    dma16<0>(rs, L + L_W + fresh_s(id.w * 3072) + (id.w & 2) * 1024 + n * 1024, vo, ((8 * kg + 512 * g) * NKF + 32 * cg) * 2);
  }
}
DI void wt_store(rsrc_t rw, int kg, int g, int cg, lds_t L) {
  int t = threadIdx.x;
  pin1(t);
  const LAS unsigned short* q = (const LAS unsigned short*)(L + L_W + (t & 7) * 80 + (t >> 3) * 2 + (~t & 1) * 2);
  unsigned u[8];
#pragma unroll
  for (int m = 0; m < 8; ++m) u[m] = (unsigned)q[320 * m] | ((unsigned)q[320 * m + 4096] << 16);
  const int so = WT_OFF + 32 * cg * 8192 + (kg >> 1) * 1024 + (g * 16 + (kg & 1) * 8) * 16, vo = (t >> 3) * 8192 + (t & 7) * 16;
  st128<SC1>(v4u{u[0], u[1], u[2], u[3]}, rw, vo, so);
  st128<SC1>(v4u{u[4], u[5], u[6], u[7]}, rw, vo + 4096, so);
}

struct PC { v4u fr64[4], fi64[4], fr32[4], fi32[4]; v4u twc, tws, tic, tis; };
DI void tuple(unsigned c0, unsigned c1, unsigned s0, unsigned s1, v4u& re, v4u& im) {
  re = v4u{c0, c1, s0, s1}; im = v4u{s0 ^ 0x80008000u, s1 ^ 0x80008000u, c0, c1};
  pin1(re); pin1(im);
}
DI void cs4(float x0, float dx, unsigned& c0, unsigned& c1, unsigned& s0, unsigned& s1) {
  const float x1 = x0 + dx, x2 = x1 + dx, x3 = x2 + dx;
  c0 = pk_h(vcos(x0), vcos(x1)); c1 = pk_h(vcos(x2), vcos(x3)); s0 = pk_h(vsin(x0), vsin(x1)); s1 = pk_h(vsin(x2), vsin(x3));
}
DI void gen_pc_a(const Ids& id, PC& k) {
  unsigned c0, c1, s0, s1;
  {
    const float a = (float)(16 * id.w + id.l15) * (1.0f / 64.0f);
    cs4((float)(4 * id.g) * a, a, c0, c1, s0, s1);
    const bool odd = id.l15 & 1;
    const unsigned mc = ((id.l15 + 1) & 2) ? 0x80008000u : 0u, ms = (id.l15 & 2) ? 0x80008000u : 0u;
#pragma unroll
    for (int ks = 0; ks < 4; ++ks) {
      tuple(c0, c1, s0, s1, k.fr64[ks], k.fi64[ks]);
      const unsigned a0 = (odd ? s0 : c0) ^ mc, a1 = (odd ? s1 : c1) ^ mc, b0 = (odd ? c0 : s0) ^ ms, b1 = (odd ? c1 : s1) ^ ms;
      c0 = a0; c1 = a1; s0 = b0; s1 = b1;
    }
  }
  const float pl = (float)(8 * (id.l15 >> 2) + (id.l15 & 3)), g8 = (float)(8 * id.g);
#pragma unroll
  for (int t = 0; t < PCA; ++t) {
    const float a = (pl + (float)(4 * (t >> 1))) * (1.0f / 32.0f);
    cs4((g8 + (float)(4 * (t & 1))) * a, a, c0, c1, s0, s1);
    tuple(c0, c1, s0, s1, k.fr32[t], k.fi32[t]);
  }
}
DI void gen_pc_b(const Ids& id, PC& k) {
  const float pl = (float)(8 * (id.l15 >> 2) + (id.l15 & 3)), a = (float)(16 * id.w + id.l15) * (1.0f / 2048.0f), kb = (float)(16 * id.w + 4 * id.g);
  unsigned c[4], s[4], d[4], e[4];
#pragma unroll
  for (int t = PCA; t < 4; ++t) {
    const float a3 = (pl + (float)(4 * (t >> 1))) * (1.0f / 32.0f);
    cs4((float)(8 * id.g + 4 * (t & 1)) * a3, a3, c[0], c[1], s[0], s[1]);
    tuple(c[0], c[1], s[0], s[1], k.fr32[t], k.fi32[t]);
  }
#pragma unroll
  for (int t = 0; t < 2; ++t) {
    cs4((float)(8 * id.g + 4 * t) * a, a, c[2 * t], c[2 * t + 1], s[2 * t], s[2 * t + 1]);
    const float b = (pl + (float)(4 * t)) * (1.0f / 2048.0f);
    cs4(kb * b, b, d[2 * t], d[2 * t + 1], e[2 * t], e[2 * t + 1]);
  }
#pragma unroll
  for (int n = 0; n < 4; ++n) {
    k.twc[n] = mul_h2(c[n], 0x30003000u); k.tws[n] = mul_h2(s[n], 0x30003000u); k.tic[n] = mul_h2(d[n], 0x30003000u); k.tis[n] = mul_h2(e[n], 0x30003000u);
  }
  pin1(k.twc); pin1(k.tws); pin1(k.tic); pin1(k.tis);
}
template <bool FIRST> DI void mf1(f4& d, v4u a, v4u b) {
  if constexpr (FIRST) asm volatile("v_mfma_f32_16x16x32_f16 %0, %1, %2, 0" : "=&v"(d) : "v"(a), "v"(b));
  else asm volatile("v_mfma_f32_16x16x32_f16 %0, %1, %2, %0" : "+v"(d) : "v"(a), "v"(b));
  SB();
}
template <int N> DI void snop() { asm volatile("s_nop %0" ::"n"(N - 1)); SB(); }
struct Q4 { unsigned d[4]; DI v4u v() const { return v4u{d[0], d[1], d[2], d[3]}; } };
struct CM { unsigned a, b, u, v; };
DI void cm_cvt(CM& m, const f4& re, const f4& im, int h) {
  asm volatile("v_cvt_pk_f16_f32 %0, %2, %3\n\tv_cvt_pk_f16_f32 %1, %4, %5" : "=&v"(m.a), "=v"(m.b) : "v"(re[2 * h]), "v"(re[2 * h + 1]), "v"(im[2 * h]), "v"(im[2 * h + 1])); SB(); }
DI void cm_mul(CM& m, unsigned x, unsigned xs, unsigned y, unsigned ys) {
  asm volatile("v_pk_mul_f16 %0, %2, %3\n\tv_pk_mul_f16 %1, %4, %5" : "=&v"(m.u), "=v"(m.v) : "v"(x), "v"(xs), "v"(y), "v"(ys)); SB(); }
DI void cm_fma(unsigned& o0, unsigned& o1, const CM& m, unsigned x, unsigned xs, unsigned y, unsigned ys) {
  asm volatile("v_pk_fma_f16 %0, %2, %3, %6\n\tv_pk_fma_f16 %1, %4, %5, %7 neg_lo:[0,0,1] neg_hi:[0,0,1]" : "=&v"(o0), "=v"(o1) : "v"(x), "v"(xs), "v"(y), "v"(ys), "v"(m.u), "v"(m.v)); SB(); }
template <bool CONJW> DI void cmul(int n, CM& m, Q4& o, const f4& re, const f4& im, const v4u& c, const v4u& s, int t) {
  const int h = n / 3;
  const unsigned cc = c[2 * t + h], ss = s[2 * t + h];
  if (n % 3 == 0) cm_cvt(m, re, im, h);
  else if (n % 3 == 1) { if (CONJW) cm_mul(m, m.b, ss, m.b, cc); else cm_mul(m, m.b, ss, m.a, ss); }
  else { if (CONJW) cm_fma(o.d[h], o.d[2 + h], m, m.a, cc, m.a, ss); else cm_fma(o.d[h], o.d[2 + h], m, m.a, cc, m.b, cc); }
}
template <bool CONJW> DI void cmul_all(Q4& o, const f4& re, const f4& im, const v4u& c, const v4u& s, int t) {
  unsigned a0, b0, a1, b1;
#define CM_HEAD "v_cvt_pk_f16_f32 %4, %8, %9\n\tv_cvt_pk_f16_f32 %5, %12, %13\n\tv_cvt_pk_f16_f32 %6, %10, %11\n\tv_cvt_pk_f16_f32 %7, %14, %15\n\tv_pk_mul_f16 %0, %5, %18\n\tv_pk_mul_f16 %1, %7, %19\n\t"
#define CM_OPS : "=&v"(o.d[0]), "=&v"(o.d[1]), "=&v"(o.d[2]), "=&v"(o.d[3]), "=&v"(a0), "=&v"(b0), "=&v"(a1), "=&v"(b1) \
               : "v"(re[0]), "v"(re[1]), "v"(re[2]), "v"(re[3]), "v"(im[0]), "v"(im[1]), "v"(im[2]), "v"(im[3]), "v"(c[2 * t]), "v"(c[2 * t + 1]), "v"(s[2 * t]), "v"(s[2 * t + 1])
  if constexpr (CONJW)
    asm volatile(CM_HEAD "v_pk_mul_f16 %2, %5, %16\n\tv_pk_mul_f16 %3, %7, %17\n\tv_pk_fma_f16 %0, %4, %16, %0\n\tv_pk_fma_f16 %1, %6, %17, %1\n\t"
                 "v_pk_fma_f16 %2, %4, %18, %2 neg_lo:[0,0,1] neg_hi:[0,0,1]\n\tv_pk_fma_f16 %3, %6, %19, %3 neg_lo:[0,0,1] neg_hi:[0,0,1]" CM_OPS);
  else
    asm volatile(CM_HEAD "v_pk_mul_f16 %2, %4, %18\n\tv_pk_mul_f16 %3, %6, %19\n\tv_pk_fma_f16 %0, %4, %16, %0\n\tv_pk_fma_f16 %1, %6, %17, %1\n\t"
                 "v_pk_fma_f16 %2, %5, %16, %2 neg_lo:[0,0,1] neg_hi:[0,0,1]\n\tv_pk_fma_f16 %3, %7, %17, %3 neg_lo:[0,0,1] neg_hi:[0,0,1]" CM_OPS);
#undef CM_HEAD
#undef CM_OPS
  SB();
}
template <class Fn> DI void p_phase(const Ids& id, rsrc_t rw, int layer, int jj, lds_t L, const PC& k, Fn slot STV) {
  const int s0 = 512 * id.w + 32 * id.l15 + 8 * id.g;
  const int xo = (s0 >> 6) * 57344 + (s0 & 63) * 2 + jj * 256;
  v4u rs, xr, xi, wt[2];
  ald128(rs, rw, s0 * 2, RS_OFF + layer * S * 2);
  ald128(xr, rw, xo, XH0_OFF + layer * XB); ald128(xi, rw, xo + 128, XH0_OFF + layer * XB);
  ald128(wt[0], rw, id.lane * 16, WT_OFF + jj * 8192 + id.w * 1024); ald128(wt[1], rw, id.lane * 16, WT_OFF + jj * 8192 + id.w * 1024 + 4096);
  const int tr = 4 * id.g + (id.l15 >> 2), to = tr * 64 + (((id.l15 & 3) ^ (((tr >> 1) & 1) | ((tr >> 2) & 2))) << 4), th = (tr << 1) & 8;
  lds_t tp[2] = {L + L_A + to + th, L + L_A + to + (th ^ 8)};
  const int zc = (16 * id.w + id.l15) * 64 + ((id.g ^ (((id.l15 >> 1) & 1) | ((id.l15 >> 2) & 2))) << 4), zh = (id.l15 << 1) & 8;
  LAS v2u* z0 = (LAS v2u*)(L + L_A + zc + zh); LAS v2u* z1 = (LAS v2u*)(L + L_A + zc + (zh ^ 8));
  lds_t xp = L + L_A + 8192 + 2 * id.w * 1024 + id.lane * 16, xq = L + L_A + 8192 + id.lane * 16;
  int so = xo + XH1_OFF + layer * XB;
  pin(tp); pin1(z0); pin1(z1); pin1(xp); pin1(xq); pin1(so);
  slot();
  vmwait<10>(); SB();
  STAMP(54);
  pin1(rs); pin1(xr); pin1(xi);
  {
    z0[0] = v2u{mul_h2(xr[0], rs[0]), mul_h2(xr[1], rs[1])}; z1[0] = v2u{mul_h2(xr[2], rs[2]), mul_h2(xr[3], rs[3])};
    z0[512] = v2u{mul_h2(xi[0], rs[0]), mul_h2(xi[1], rs[1])}; z1[512] = v2u{mul_h2(xi[2], rs[2]), mul_h2(xi[3], rs[3])};
  }
  wgbar();
  STAMP(55);
  v4u a[8];
#pragma unroll
  for (int u = 0; u < 8; ++u) a[u] = cat(trread(tp[u >> 2] + (u & 3) * 1024), trread(tp[u >> 2] + 4096 + (u & 3) * 1024));
  vmwait<0>(); SB(); pin(wt);
  v4u wr, wi;
#pragma unroll
  for (int m = 0; m < 4; ++m) {
    const unsigned t0 = wt[m >> 1][2 * (m & 1)], t1 = wt[m >> 1][2 * (m & 1) + 1];
    wr[m] = mul_h2(pk_h(bf_lo(t0), bf_lo(t1)), 0x30003000u); wi[m] = mul_h2(pk_h(bf_hi(t0), bf_hi(t1)), 0xb000b000u);
  }
  pin1(wr); pin1(wi);
  SB();
  f4 r0, i0, r1, i1, r2, i2, r3, i3;
  Q4 q0, q1;
  CM m;
#define DFT64(R, I, U) mf1<(U & 3) == 0>(R, a[U], k.fr64[U & 3]); mf1<(U & 3) == 0>(I, a[U], k.fi64[U & 3])
#define TW1(n) cmul<false>(n, m, q0, r0, i0, k.twc, k.tws, 0)
  DFT64(r0, i0, 0); DFT64(r0, i0, 1); DFT64(r0, i0, 2); DFT64(r0, i0, 3);
  DFT64(r1, i1, 4); snop<6>();
  TW1(0); mf1<false>(r1, a[5], k.fr64[1]); TW1(1); mf1<false>(i1, a[5], k.fi64[1]); TW1(2); mf1<false>(r1, a[6], k.fr64[2]);
  TW1(3); mf1<false>(i1, a[6], k.fi64[2]); TW1(4); mf1<false>(r1, a[7], k.fr64[3]); TW1(5); mf1<false>(i1, a[7], k.fi64[3]);
#define TW2(n) cmul<false>(n, m, q1, r1, i1, k.twc, k.tws, 1)
  snop<1>();
  mf1<true>(r2, k.fr32[0], q0.v()); mf1<true>(i2, k.fi32[0], q0.v()); snop<5>();
  TW2(0); TW2(1); mf1<true>(r3, k.fr32[2], q0.v()); TW2(2); TW2(3); mf1<true>(i3, k.fi32[2], q0.v()); TW2(4); TW2(5);
  snop<2>();
  mf1<false>(r2, k.fr32[1], q1.v()); mf1<false>(i2, k.fi32[1], q1.v()); mf1<false>(r3, k.fr32[3], q1.v()); mf1<false>(i3, k.fi32[3], q1.v());
  snop<6>();
#define WM2(n) cmul<true>(n, m, q1, r3, i3, wr, wi, 1)
  cmul_all<true>(q0, r2, i2, wr, wi, 0);
  WM2(0); mf1<true>(r0, q0.v(), k.fr32[0]); WM2(1); mf1<true>(i0, q0.v(), k.fi32[0]); WM2(2); mf1<true>(r1, q0.v(), k.fr32[2]); WM2(3); mf1<true>(i1, q0.v(), k.fi32[2]);
  WM2(4); WM2(5); snop<2>();
  mf1<false>(r0, q1.v(), k.fr32[1]); mf1<false>(i0, q1.v(), k.fi32[1]); mf1<false>(r1, q1.v(), k.fr32[3]); mf1<false>(i1, q1.v(), k.fi32[3]);
  snop<6>();
  cmul_all<false>(q0, r0, i0, k.tic, k.tis, 0);
  ldsw128(xp, q0.v());
  cmul_all<false>(q1, r1, i1, k.tic, k.tis, 1);
  ldsw128(xp + 1024, q1.v());
  wgbar();
  STAMP(58);
#pragma unroll
  for (int u = 0; u < 8; ++u) a[u] = ldsr128(xq + (2 * (u & 3) + (u >> 2)) * 1024);
  unsigned sr = 0x34003400u, si = jj == 223 ? 0u : 0xb400b400u;
  pin1(sr); pin1(si);
  SB();
  const auto fin = [&](int h, const f4& re, const f4& im, int n) {
    if (n == 0) cm_cvt(m, re, im, h & 1);
    else { asm volatile("v_pk_fma_f16 %0, %2, %3, %4\n\tv_pk_fma_f16 %1, %5, %6, %7" : "=&v"(q0.d[h]), "=v"(q1.d[h]) : "v"(m.a), "v"(sr), "v"(xr[h]), "v"(m.b), "v"(si), "v"(xi[h])); SB(); } };
  DFT64(r0, i0, 0); DFT64(r0, i0, 1); DFT64(r0, i0, 2); DFT64(r0, i0, 3);
  DFT64(r1, i1, 4); DFT64(r1, i1, 5); snop<4>();
  fin(0, r0, i0, 0); mf1<false>(r1, a[6], k.fr64[2]); fin(0, r0, i0, 1); mf1<false>(i1, a[6], k.fi64[2]);
  fin(1, r0, i0, 0); mf1<false>(r1, a[7], k.fr64[3]); fin(1, r0, i0, 1); mf1<false>(i1, a[7], k.fi64[3]);
  snop<8>();
  fin(2, r1, i1, 0); fin(2, r1, i1, 1); fin(3, r1, i1, 0); fin(3, r1, i1, 1);
  st128<SC1>(q0.v(), rw, so); st128<SC1>(q1.v(), rw, so + 128);
#undef DFT64
#undef TW1
#undef TW2
#undef WM2
}

DI void gen_ti(const Ids& id, int j, v4u (&T)[14]) {
  const float df = (float)(64 * j + 16 * id.w + id.l15) * (1.0f / 448.0f);
  const float base = (float)(2 * id.g + 1) * df;
  const float tc = 2.0f * vcos(16.0f * df);
  f2 v[2][4];
#pragma unroll
  for (int ks = 0; ks < 14; ++ks) {
    unsigned u[4];
#pragma unroll
    for (int e = 0; e < 4; ++e) {
      if (ks < 2) { const float x = __builtin_fmaf((float)(16 * ks + 8 * (e >> 1) + (e & 1)), df, base); v[ks][e] = f2{vcos(x), -vsin(x)}; }
      else v[ks & 1][e] = v[~ks & 1][e] * tc - v[ks & 1][e];
      float c = v[ks & 1][e][0];
      if (ks == 13 && e == 3) c *= id.g == 3 ? 0.70710678118654752f : 1.0f;
      u[e] = pk_h(c, v[ks & 1][e][1]);
    }
    T[ks] = v4u{u[0], u[1], u[2], u[3]};
    pin1(T[ks]);
  }
}
DI void s3_issue(const Ids& id, rsrc_t rw, int layer, int i, lds_t L) {
  const int vo = img_src(id, 128);
  dma_tile<4096, 4096>(rw, L + L_A + id.w * 1024, vo, XH1_OFF + layer * XB + i * 57344 + id.w * 1024);
}
constexpr int s3_k(int u) { return u < 36 ? u >> 2 : (u < 41 ? u - 27 : 9 + (u - 41) / 3); }
constexpr int s3_n(int u) { return u < 36 ? u & 3 : (u < 41 ? 0 : 1 + (u - 41) % 3); }
DI void s3_compute(const Ids& id, rsrc_t rw, int layer, int i, int j, lds_t L, const v4u (&T)[14], f4 (&mu)[4], f4 (&x1)[4] STV) {
  constexpr int PD = 5;
  lds_t tb[4];
#pragma unroll
  for (int n = 0; n < 4; ++n) tb[n] = L + L_A + img_rd(id, n);
  LAS float* ew = id.g == id.l15 >> 2 ? (LAS float*)(L + L_E) + 16 * id.w + id.l15 : (LAS float*)(L + L_D) + id.l15;
  LAS float* er[2] = {(LAS float*)(L + L_E) + 16 * id.w + 4 * id.g, (LAS float*)(L + L_E) + 16 * (id.w ^ 1) + 4 * id.g};
  const int e2 = (id.w & 2) ? -32 : 32;
  int so = id.lane * 16 + HF_OFF + layer * XB + ((j * 64 + 2 * i + (id.w >> 1)) * 4 + id.w) * 1024;
  pin1(ew); pin(er); pin1(so);
  vmwait<10>(); rawbar();
  STAMP(59);
  v4u a[PD];
#pragma unroll
  for (int u = 0; u < PD; ++u) a[u] = img_frag(tb[s3_n(u)] + s3_k(u) * 4096);
  SB();
  f4 acc[4], gram, rr[4];
  static_for<0, 56>([&](auto U) {
    constexpr int u = U.v, k = s3_k(u), n = s3_n(u), v = u + PD;
    const v4u cur = a[u % PD];
    if constexpr (n == 0) gram = mfma_h(cur, cur, k == 0 ? Z4 : gram);
    acc[n] = mfma_h(cur, T[k], k == 0 ? Z4 : acc[n]);
    asm volatile("" : "+a"(acc[n]));
    if constexpr (v == 16) { vmwait<5>(); rawbar(); }
    if constexpr (v == 36) { STAMP(60); vmwait<0>(); rawbar(); pin(mu); STAMP(61); }
    if constexpr (v < 56) a[u % PD] = img_frag(tb[s3_n(v)] + s3_k(v) * 4096);
    if constexpr (u == 42) {
      SB();
      const float vr = diag(gram, id.l15) * (1.0f / 448.0f);
      const float r1 = __builtin_amdgcn_rsqf(vr + EPS);
      *ew = r1 * __builtin_amdgcn_rsqf(vr * r1 * r1 + EPS) * 0.066815310478106f;
    }
    if constexpr (u == 47) {
      lgwait<10>(); rawbar();
      rr[0] = *(LAS f4*)er[0]; rr[1] = *(LAS f4*)er[1]; rr[2] = *(LAS f4*)(er[0] + e2); rr[3] = *(LAS f4*)(er[1] + e2);
    }
    SB();
  });
  STAMP(62);
  v2u hq[4];
#pragma unroll
  for (int n = 0; n < 4; ++n) { const f4 x = acc[n] * rr[n]; hq[n] = v2u{pk_bf(x[0], x[1]), pk_bf(x[2], x[3])}; }
  st128<SC1>(pair_frag(id.w, hq[0], hq[1]), rw, so);
  st128<SC1>(pair_frag(id.w, hq[2], hq[3]), rw, so ^ 4096);
  SB();
#pragma unroll
  for (int n = 0; n < 4; ++n) x1[n] = acc[n] * 0.066815310478106f + mu[n];
  pin(x1);
}

template <int NKS, int W0, int SO, int NS, int NX, int NR>
DI void g_main(const Ids& id, rsrc_t rw, int abase, lds_t L, const v4u (&R)[NR][4], v4u (&W)[10][4], f4 (&res)[4], const f4 (&init)[4]) {
  constexpr int WIN = GWIN, NU = 4 * NKS;
  v4u a[WIN];
  const int vo = id.lane * 16;
  int sb[4];
#pragma unroll
  for (int n = 0; n < 4; ++n) sb[n] = abase + id.w * NKS * 4096 + ((((n + 1) & 3) ^ id.w) << 10);
#pragma unroll
  for (int u = 0; u < WIN; ++u) a[u] = ld128<SC1>(rw, vo, sb[u / NKS] + (u % NKS) * 4096);
  lds_t rd[4];
#pragma unroll
  for (int n = 0; n < 4; ++n) rd[n] = L + L_A + ((n ^ id.w) << 10) + vo;
  SB();
  vmwait<WIN>();
  w_read<NS, W0>(id, L + L_W + id.w * WSTAGE + SO, W);
  w_read<NX, W0 + NS>(id, L + L_A + id.w * 12288, W);
  SB();
  f4 part[4], acc[4];
  v4u t0, t1, t2;
  static_for<0, NU>([&](auto U) {
    constexpr int u = U.v, qn = u / NKS, ks = u % NKS, v = u - 1 + WIN;
    const v4u cur = a[u % WIN];
    const auto mm = [&](int f) { if constexpr (ks < NR) acc[f] = mfma_bf(R[ks][f], cur, ks == 0 ? (qn == 3 ? init[f] : Z4) : acc[f]); else acc[f] = mfma_bf(W[ks - NR][f], cur, acc[f]); };
    mm(0);
    if constexpr (u >= 1 && v < NU) { SB(); a[(u - 1) % WIN] = ld128<SC1>(rw, vo, sb[v / NKS] + (v % NKS) * 4096); SB(); }
    mm(1); mm(2); mm(3);
    asm volatile("" : "+a"(acc[0]), "+a"(acc[1]), "+a"(acc[2]), "+a"(acc[3]));
    if constexpr (qn == 3 && ks == 3) wgbar();
    if constexpr (qn == 3 && ks >= 4 && ks < 8) { part[ks - 4] = (asf4(t0) + asf4(t1)) + asf4(t2); pin1(part[ks - 4]); }
    if constexpr (qn == 3 && ks >= 3 && ks < 7) { lds_t rp = rd[ks - 3] + id.w * 12288; t0 = ldsr128(rp); t1 = ldsr128(rp + 4096); t2 = ldsr128(rp + 8192); }
    if constexpr (ks == NKS - 1 && qn < 3) {
      if constexpr (qn == 0) wgbar();
#pragma unroll
      for (int f = 0; f < 4; ++f) ldsw128(rd[f] + ((id.w ^ (qn + 1)) * 3 + qn) * 4096, asu4(acc[f]));
    }
    SB();
  });
#pragma unroll
  for (int f = 0; f < 4; ++f) res[f] = acc[f] + part[f];
}
DI void load_bias(const Ids& id, const Params& p, int o1, int o2, v2u (&b1)[4], v2u (&b2)[4]) {
  const rsrc_t r1 = mkrs(p.b1, F * 2), r2 = mkrs(p.b2, S * 2);
#pragma unroll
  for (int f = 0; f < 4; ++f) {
    b1[f] = __builtin_amdgcn_raw_buffer_load_b64(r1, 8 * id.g, 2 * o1 + 32 * (f ^ id.w), 0);
    b2[f] = __builtin_amdgcn_raw_buffer_load_b64(r2, 8 * id.g, 2 * o2 + 32 * (f ^ id.w), 0);
  }
  SB();
}
template <int N> DI void pin_acc4(f4 (&a)[N]) {
#pragma unroll
  for (int n = 0; n < N; ++n) asm volatile("" : "+a"(a[n]));
}
DI f4 bias4(v2u b) { return f4{bf_lo(b.x), bf_hi(b.x), bf_lo(b.y), bf_hi(b.y)}; }
struct GeluK { f2 c1, c0, one; };
DI GeluK gelu_k() { GeluK k{{0.044715f * -2.302208198144325f, 0.044715f * -2.302208198144325f}, {-2.302208198144325f, -2.302208198144325f}, {1.0f, 1.0f}}; pin1(k.c1); pin1(k.c0); return k; }
DI unsigned gelu2(f2 u, const GeluK& k) {
  const f2 z = u * (u * u * k.c1 + k.c0);
  const f2 d = f2{__builtin_amdgcn_exp2f(z[0]), __builtin_amdgcn_exp2f(z[1])} + k.one;
  const f2 g = u * f2{__builtin_amdgcn_rcpf(d[0]), __builtin_amdgcn_rcpf(d[1])};
  return pk_bf(g[0], g[1]);
}
DI int g1_addr(const Ids& id, int layer, int j, int m) { int so = id.lane * 16 + GF_OFF + layer * GB + ((j * 56 + 2 * m + (id.w >> 1)) * 4 + id.w) * 1024; pin1(so); return so; }
DI void g1_epi(int w, rsrc_t rw, int so, const GeluK& k, const f4 (&res)[4]) {
#pragma unroll
  for (int q = 0; q < 2; ++q) {
    v2u o[2];
#pragma unroll
    for (int e = 0; e < 2; ++e) {
      const f4 u = res[2 * q + e];
      o[e] = v2u{gelu2(f2{u[0], u[1]}, k), gelu2(f2{u[2], u[3]}, k)};
      pin1(o[e]); SB();
    }
    st128<SC1>(pair_frag(w, o[0], o[1]), rw, so ^ (q << 12));
  }
}
struct G2A { lds_t wp[4], tp[4]; int vo; };
DI G2A g2_addr(const Ids& id, lds_t L) {
  G2A a;
  lds_t T = L + L_A + 49152;
  const int d0 = 16 * id.w + id.l15, h0 = (d0 & 15) ^ ((d0 >> 4) & 1) ^ (((d0 >> 5) & 1) << 2), q = id.l15 >> 2, p = id.l15 & 3;
#pragma unroll
  for (int n = 0; n < 4; ++n) {
    a.wp[n] = T + d0 * 128 + (((4 * (n ^ id.w) + id.g) ^ h0) << 3);
    const int sc = 4 * id.w + (n & 2) + (id.g >> 1), d = 8 * (4 * (id.g & 1) + p) + 4 * (n & 1) + q;
    a.tp[n] = T + d * 128 + ((sc ^ (d & 15) ^ ((d >> 4) & 1) ^ (((d >> 5) & 1) << 2)) << 3);
  }
  a.vo = (16 * id.w + 4 * (id.lane >> 5) + (id.lane & 3)) * 896 + 16 * ((id.lane >> 2) & 7);
  pin(a.wp); pin(a.tp); pin1(a.vo);
  return a;
}
DI void g2_epi(const G2A& a, rsrc_t ro, int obase, const f4 (&res)[4], bool plain) {
#pragma unroll
  for (int q = 0; q < 4; ++q) *(LAS v2u*)a.wp[q] = v2u{pk_bf(res[q][0], res[q][1]), pk_bf(res[q][2], res[q][3])};
  wgbar();
  const v4u o0 = cat(trread(a.tp[0]), trread(a.tp[1])), o1 = cat(trread(a.tp[2]), trread(a.tp[3]));
  SB();
  if (plain) { st128<0>(o0, ro, a.vo, obase); st128<0>(o1, ro, a.vo, obase + 8 * 896); } else { st128<SC1>(o0, ro, a.vo, obase); st128<SC1>(o1, ro, a.vo, obase + 8 * 896); }
}

constexpr int C_EP = 0, C_FL = 1, C_H = 15, C_F0 = 16, C_P = C_F0 + 2 * 128, C_S3 = C_P + 2 * 128, C_G1 = C_S3 + 2 * 7 * 32, C_G2 = C_G1 + 2 * 7 * 32, C_LINES = C_G2 + 32 * 8;
static_assert(C_LINES * 128 <= CTRL_BYTES && C_LINES <= C_F0 + 128 + 7 * 256, "control block");
constexpr unsigned SPIN_CAP = 1u << 20;
DI unsigned ld_word(unsigned char* ws, int line) { return __hip_atomic_load((gu32*)ws + 32 * line, __ATOMIC_RELAXED, __HIP_MEMORY_SCOPE_AGENT); }
DI void st_word(unsigned char* ws, int line, unsigned v) { __hip_atomic_store((gu32*)ws + 32 * line, v, __ATOMIC_RELAXED, __HIP_MEMORY_SCOPE_AGENT); }
template <int NS, int NR, int YOUNG = 0, class Fn> DI void seam_arrive(const Ids& id, unsigned char* ws, int line0, int shard, Fn work, bool on = true) {
  SB(); work(); SB();
  vmwait<YOUNG>();
  wgbar();
  if (on && id.w == 0 && id.lane < NR) __hip_atomic_fetch_add((gu32*)ws + 32 * (line0 + id.lane * NS + shard), 1u, __ATOMIC_RELAXED, __HIP_MEMORY_SCOPE_AGENT);
}
template <int NS> DI void seam_wait(const Ids& id, unsigned char* ws, int line0, int rep, unsigned want) {
  if (id.w == 0) {
    const int line = line0 + rep * NS + (id.lane & (NS - 1));
    for (unsigned sp = 0; sp < SPIN_CAP; ++sp)
      if (__all(ld_word(ws, line) >= want)) break;
    __builtin_amdgcn_fence(__ATOMIC_ACQUIRE, "wavefront");
  }
  wgbar();
}
template <int NS> DI void poll_early(const Ids& id, rsrc_t rw, lds_t L, int line0, int rep) {
  if (EARLY && id.w == 0) dma4_sc1(rw, L + L_D, 128 * (line0 + rep * NS + (id.lane & (NS - 1))));
}
template <int NS, int YOUNG> DI void seam_wait_early(const Ids& id, unsigned char* ws, lds_t L, int line0, int rep, unsigned want) {
  if (id.w == 0) {
    bool ok = false;
    if (EARLY) { vmwait<YOUNG>(); ok = __all(((volatile LAS unsigned*)(L + L_D))[id.lane] >= want); }
    if (!ok) {
      const int line = line0 + rep * NS + (id.lane & (NS - 1));
      for (unsigned sp = 0; sp < SPIN_CAP; ++sp)
        if (__all(ld_word(ws, line) >= want)) break;
    }
    __builtin_amdgcn_fence(__ATOMIC_ACQUIRE, "wavefront");
  }
  wgbar();
}
DI void wait_init(const Ids& id, unsigned char* ws, int rep, unsigned tag) {
  if (id.w == 0) {
    for (unsigned sp = 0; sp < SPIN_CAP; ++sp)
      if (__all(ld_word(ws, C_FL + rep) == tag)) break;
    __builtin_amdgcn_fence(__ATOMIC_ACQUIRE, "wavefront");
  }
}

DI void poll_ge(unsigned char* ws, int line, unsigned want) {
  for (unsigned sp = 0; sp < SPIN_CAP; ++sp) {
    if (__all(ld_word(ws, line) >= want)) break;
    __builtin_amdgcn_s_sleep(12);
  }
}

template <class T> DI T* fresh_p(T* v) { asm volatile("" : "+s"(v)); return v; }
DI Params fresh_p(const Params& a) { return Params{fresh_p(a.x), fresh_p(a.wr), fresh_p(a.wi), fresh_p(a.w1), fresh_p(a.b1), fresh_p(a.w2), fresh_p(a.b2), fresh_p(a.out), fresh_p(a.ws)}; }

DI void helper(const Params& p, lds_t L, const Ids& id0 STV) {
  const int b = blockIdx.x, q = b >> 3, x8 = b & 7;
  const auto nop = []() {};
  const unsigned want0 = (id0.lane & 15) < 8 ? 15u : 14u;
  if (q == 28) {
    const unsigned tag = ld_word(p.ws, C_EP) + 1u;
    if (b == 224) {
      if (id0.w == 0) {
        st_word(p.ws, C_F0 + id0.lane, 0u); st_word(p.ws, C_F0 + 64 + id0.lane, 0u);
        if (id0.lane == 0) st_word(p.ws, C_H, 0u);
        vmwait<0>();
        STAMP(27);
        if (id0.lane < 8) st_word(p.ws, C_FL + id0.lane, tag);
        vmwait<0>();
        if (id0.lane < 8) __hip_atomic_fetch_add((gu32*)p.ws + 32 * (C_F0 + id0.lane * 16), 1u, __ATOMIC_RELAXED, __HIP_MEMORY_SCOPE_AGENT);
        __builtin_amdgcn_s_sleep(96);
        poll_ge(p.ws, C_F0 + (id0.lane & 15), want0);
        poll_ge(p.ws, C_H, 24u);
        if (id0.lane < 9) st_word(p.ws, id0.lane == 8 ? C_EP : C_FL + id0.lane, id0.lane == 8 ? tag : tag - 1u);
      }
    } else {
      const int n = C_F0 + 128 + (b - 225) * 256 + (int)threadIdx.x;
      if (n < C_LINES) st_word(p.ws, n, 0u);
      wait_init(id0, p.ws, x8, tag);
      seam_arrive<16, 8>(id0, p.ws, C_F0, x8, nop);
    }
    STAMP(26);
    STAMP_OUT();
    return;
  }
  {
    const unsigned tag = ld_word(p.ws, C_EP) + 1u;
    const rsrc_t r2 = mkrs(p.w2, S * F * 2), r1 = mkrs(p.w1, x8 < 7 ? S * F * 2 : 0);
    const int h = 4 * (q - 29) + id0.w;
    const auto t1 = [&](int n0, int n1) { for (int n = h + n0; n < n1; n += 12) touch(r1, L + L_D, id0.lane * (F * 2), ((n >> 2 & 3) * 512 + (n >> 4) * 64) * F * 2 + (4 * x8 + (n & 3)) * 128); };
    const auto t2 = [&](int n0, int n1) { for (int n = h + n0; n < n1; n += 12) touch(r2, L + L_D, id0.lane * (S * 2), ((n >> 2 & 3) * 448 + (n >> 4) * 64) * S * 2 + (4 * x8 + (n & 3)) * 128); };
    if (HMODE == 0) { t1(0, 96); t2(0, 48); t1(96, 128); } else if (HMODE == 1) { t1(0, 128); } else { t1(0, 128); t2(0, 112); }
    vmwait<0>();
    STAMP(26);
    if (id0.w == 0) {
      wait_init(id0, p.ws, x8, tag);
      if (id0.lane == 0) __hip_atomic_fetch_add((gu32*)p.ws + 32 * C_H, 1u, __ATOMIC_RELAXED, __HIP_MEMORY_SCOPE_AGENT);
      poll_ge(p.ws, C_F0 + x8 * 16 + (id0.lane & 15), want0);
    }
    if (HMODE) {
      if (id0.w == 0) poll_ge(p.ws, C_P + x8 * 16, 1u);
      wgbar();
      t2(0, 48);
      vmwait<0>();
    }
#pragma unroll 1
    for (int st = 0; st < 3; ++st) {
      if (id0.w == 0) poll_ge(p.ws, st == 1 ? C_G2 + 4 * x8 * 8 + 7 : C_S3 + (st >> 1) * 7 * 32 + x8 * 4, st == 1 ? 7u : TRIG);
      wgbar();
      if (st == 1) t1(48, 128); else t2(32, 112);
      vmwait<0>();
    }
    STAMP_OUT();
  }
}

__global__ void __launch_bounds__(256, 1) k_all(Params p0) {
  extern __shared__ __attribute__((aligned(16))) unsigned char smem[];
  lds_t L = (lds_t)smem;
  const Ids id0 = get_ids();
  const auto nop = []() {};
  STAMPE(0);
  if (blockIdx.x >= 224) { helper(p0, L, id0 STA); return; }
#define ID fresh(id0)
#define W1S w_src(ID, p.w1, fresh_s(g1 ? S * F * 2 : 0), F * 2, 512, fresh_s(m) * 128, L)
#define W2S w_src(ID, p.w2, S * F * 2, S * 2, 448, fresh_s(i) * 128, L)
#define STAGE (L + L_W + fresh_s(id0.w) * WSTAGE)
#define ASTAGE (L + L_A + fresh_s(id0.w) * 12288)
  v4u R1[NR1][4], R2[NR2][4];
#pragma unroll 1
  for (int layer = 0; layer < 2; ++layer) {
    const int sb = layer * 30; (void)sb;
    const int b = fresh_s(blockIdx.x), q = b >> 3, x8 = b & 7, i = 4 * x8 + q / 7, j = q % 7, m = i, jj = x8 * 28 + q;
    const bool g1 = i < 28;
    PC k;
    gen_pc_a(ID, k);
    SB();
    const Params p = fresh_p(p0);
    const rsrc_t rw = mkrs(p.ws, WS_BYTES);
    if (layer == 0) {
      STAMP(28);
      f0_issue(ID, mkrs(p.x, XB), 0, i, L);
      STAMP(24);
      wt_issue(p, ID, x8, q / 7, j, L);
      if (threadIdx.x == 0) dma4_sc1(rw, L + L_D + 256, C_EP * 128);
      STAMP(25);
    }
    if (layer == 1) {
      seam_arrive<1, 8>(ID, p.ws, C_G2 + i * 8, 0, nop);
      STAMP(23);
    }
    v4u T[14];
    v2u bq1[4], bq2[4];
    {
      gen_tf(ID, j, T, [&]() { if (layer == 1) poll_early<1>(ID, rw, L, C_G2 + i * 8, j); });
      if (layer == 1) {
        seam_wait_early<1, 0>(ID, p.ws, L, C_G2 + i * 8, j, 7u);
        f0_issue(ID, rw, X2_OFF, i, L);
      }
      STAMP(sb + 1);
      f0_compute(ID, rw, layer, i, j, L, T);
      STAMP(sb + 2);
      if (layer == 0) {
        if (threadIdx.x == 0) dma4_sc1(rw, L + L_D + 260, (C_FL + x8) * 128);
        wt_store(rw, x8, q / 7, q % 7, L);
      }
      STAMP(sb + 3);
      seam_arrive<16, 8>(ID, p.ws, C_F0 + layer * 128, b & 15, [&]() {
        gen_pc_b(ID, k);
        if (layer == 0 && id0.w == 0) {
          vmwait<0>();
          const unsigned tag = ((const volatile LAS unsigned*)(L + L_D))[64] + 1u, fl = ((const volatile LAS unsigned*)(L + L_D))[65];
          if (__builtin_amdgcn_readfirstlane(fl != tag)) wait_init(id0, p.ws, x8, tag);
        } });
      STAMP(sb + 4);
      if (layer == 0) { const WSrc ws = W1S; w_issue<0, F0P>(ws, 0, ws.stage); poll_early<16>(ID, rw, L, C_F0, x8); w_issue<F0P, 4 * NR1>(ws, 0, ws.stage); }
      else { __builtin_amdgcn_s_sleep(F0SL); poll_early<16>(ID, rw, L, C_F0 + 128, x8); vmwait<0>(); }
      STAMP(sb + 5);
      seam_wait_early<16, 4 * NR1 - F0P>(ID, p.ws, L, C_F0 + layer * 128, x8, layer == 0 && (ID.lane & 15) < 8 ? 15u : 14u);
      STAMP(sb + 6);
      p_phase(ID, rw, layer, jj, L, k, [&]() {
        load_bias(ID, p, 64 * m, 64 * i, bq1, bq2);
        gen_ti(ID, j, T); } STA);
    }
    STAMP(sb + 7);
    v4u W[10][4];
    f4 x1[4];
    {
      f4 mu[4];
      seam_arrive<16, 8, PD_D + 4>(ID, p.ws, C_P + layer * 128, b & 15, [&]() {
        const Ids id = ID;
        pin(bq1); pin(bq2);
#pragma unroll
        for (int n = 0; n < 4; ++n) mu[n] = asf4(ld128<SC1>(rw, 16 * id.g, MU_OFF + (layer * S + 64 * i + 16 * (n ^ id.w)) * 4));
        SB();
        if (layer == 0) { w_read<NR1, 0>(ID, STAGE, R1); lgwait<0>(); pin_acc<0, NR1>(R1); }
        const WSrc ws = W1S; w_issue<0, PD_D>(ws, NR1, ws.stage); });
      STAMP(sb + 8);
      { const WSrc ws = W1S; w_issue<PD_D, PP_D>(ws, NR1, ws.stage); poll_early<16>(ID, rw, L, C_P + layer * 128, x8); w_issue<PP_D, 24>(ws, NR1, ws.stage); }
      if (S3PRE) { vmwait<24 - 4 * S3PRE>(); w_read<S3PRE, 0>(ID, STAGE, W); }
      STAMP(sb + 9);
      seam_wait_early<16, 24 - PP_D>(ID, p.ws, L, C_P + layer * 128, x8, 14u);
      STAMP(sb + 10);
      s3_issue(ID, rw, layer, i, L);
      vmwait<14>();
      w_read<6 - S3PRE, S3PRE>(ID, STAGE + S3PRE * 4096, W);
      STAMP(sb + 11);
      s3_compute(ID, rw, layer, i, j, L, T, mu, x1 STA);
      pin_acc<0, 3>(W);
      STAMP(sb + 12);
    }
    if (layer == 0) { const WSrc ws = W2S; w_issue<0, 8>(ws, 0, ws.stage); }
    seam_arrive<4, 8, 12>(ID, p.ws, C_S3 + (layer * 7 + j) * 32, i & 3, [&]() {
      { const WSrc ws = W1S; w_issue<0, 4>(ws, NR1 + 6, ws.stage + 20480); }
      if (layer == 1) vmwait<4>(); });
    STAMP(sb + 13);
    f4 res[4], init[4];
    {
      const rsrc_t rg = mkrs(p.ws, fresh_s(g1 ? WS_BYTES : 0));
#pragma unroll
      for (int f = 0; f < 4; ++f) { init[f] = bias4(bq1[f]); x1[f] += bias4(bq2[f]); }
      pin_acc4(init); pin(x1);
      if (layer == 0) { const WSrc ws = W2S; w_issue<8, 4 * NR2>(ws, 0, ws.stage); }
      { const WSrc ws = W1S; lds_t as = ASTAGE; w_issue<0, 6>(ws, NR1 + 7, as); poll_early<4>(ID, rw, L, C_S3 + (layer * 7 + j) * 32, x8); w_issue<6, 12>(ws, NR1 + 7, as); }
      const int g1o = g1_addr(ID, layer, j, m);
      const GeluK gk = gelu_k();
      STAMP(sb + 14);
      seam_wait_early<4, 6>(ID, p.ws, L, C_S3 + (layer * 7 + j) * 32, x8, 8u);
      STAMP(sb + 15);
      g_main<16, 6, 20480, 1, 3>(ID, rg, HF_OFF + layer * XB + j * 64 * 4096, L, R1, W, res, init);
      STAMP(sb + 16);
      g1_epi(fresh_s(id0.w), rg, g1o, gk, res);
      STAMP(sb + 17);
      seam_arrive<4, 8, DRN_D>(ID, p.ws, C_G1 + (layer * 7 + j) * 32, m & 3, [&]() {
        if (layer == 0) { w_read<NR2, 0>(ID, STAGE, R2); lgwait<0>(); pin_acc<0, NR2>(R2); }
        const WSrc ws = W2S; w_issue<0, DRN_D>(ws, NR2, ws.stage); }, g1);
      STAMP(sb + 18);
    }
    { const WSrc ws = W2S; lds_t as = ASTAGE; w_issue<DRN_D, 24>(ws, NR2, ws.stage); w_issue<0, G2P>(ws, NR2 + 6, as);
      poll_early<4>(ID, rw, L, C_G1 + (layer * 7 + j) * 32, x8); w_issue<G2P, 12>(ws, NR2 + 6, as); }
    const G2A ga = g2_addr(ID, L);
    vmwait<36 - 16>(); w_read<4, 0>(ID, STAGE, W);
    STAMP(sb + 19);
    seam_wait_early<4, 12 - G2P>(ID, p.ws, L, C_G1 + (layer * 7 + j) * 32, x8, 7u);
    STAMP(sb + 20);
    g_main<14, 4, 16384, 2, 3>(ID, rw, GF_OFF + layer * GB + j * 56 * 4096, L, R2, W, res, x1);
    STAMP(sb + 21);
    g2_epi(ga, layer == 0 ? rw : mkrs(p.out, XB), (layer == 0 ? X2_OFF : 0) + 64 * i * 896 + j * 128, res, layer == 1);
    STAMP(sb + 22);
  }
  STAMPE(53);
  const Params& p = p0; (void)p;
  STAMP_OUT();
}

extern "C" void kernel_launch(void* const* d_in, const int* in_sizes, int n_in, void* d_out, int out_size, void* d_ws, size_t ws_size, hipStream_t stream) {
  Params p;
  p.x = (const unsigned short*)d_in[0]; p.wr = (const unsigned short*)d_in[1]; p.wi = (const unsigned short*)d_in[2];
  p.w1 = (const unsigned short*)d_in[4]; p.b1 = (const unsigned short*)d_in[5]; p.w2 = (const unsigned short*)d_in[6]; p.b2 = (const unsigned short*)d_in[7];
  p.out = (unsigned short*)d_out; p.ws = (unsigned char*)d_ws;
  (void)hipFuncSetAttribute((const void*)k_all, hipFuncAttributeMaxDynamicSharedMemorySize, LDS_BYTES);
  k_all<<<256, 256, LDS_BYTES, stream>>>(p);
}
```

```cpp
#include <hip/hip_runtime.h>
#include <hip/hip_bf16.h>
#include <stdint.h>

#define DI __device__ __forceinline__
#define GAS __attribute__((address_space(1)))
#define LAS __attribute__((address_space(3)))
typedef unsigned v4u __attribute__((ext_vector_type(4)));
typedef unsigned v2u __attribute__((ext_vector_type(2)));
typedef float f4 __attribute__((ext_vector_type(4)));
typedef float f2 __attribute__((ext_vector_type(2)));
typedef _Float16 h2 __attribute__((ext_vector_type(2)));
typedef __bf16 b2 __attribute__((ext_vector_type(2)));
typedef short s4 __attribute__((ext_vector_type(4)));
typedef __amdgpu_buffer_rsrc_t rsrc_t;
typedef LAS unsigned char* lds_t;
typedef GAS unsigned gu32;

constexpr int GWIN = 10, DRN_D = 9, PD_D = 6, PP_D = 18, F0P = 18, G2P = 3, S3PRE = 4, F0SL = 8, S3P = 9, CODEWARM = 30080;

constexpr int S = 2048, D = 448, F = 1792, NKF = 225;
constexpr float EPS = 1e-5f;
constexpr int XB = S * D * 2;
constexpr int GB = D * F * 2;
constexpr int SC1 = 16;
constexpr int CTRL_BYTES = 262144;
constexpr int MU_OFF = CTRL_BYTES;
constexpr int RS_OFF = MU_OFF + 2 * S * 4;
constexpr int WT_OFF = RS_OFF + 2 * S * 2;
constexpr int XH0_OFF = WT_OFF + XB;
constexpr int XH1_OFF = XH0_OFF + 2 * XB;
constexpr int HF_OFF = XH1_OFF + 2 * XB;
constexpr int GF_OFF = HF_OFF + 2 * XB;
constexpr int X2_OFF = GF_OFF + 2 * GB;
constexpr int WS_BYTES = X2_OFF + XB;
static_assert(HF_OFF % 8192 == 0 && XB % 8192 == 0 && GF_OFF % 8192 == 0 && GB % 8192 == 0, "fragment pairs are addressed by an exclusive or");
constexpr int L_A = 0, L_E = 57344, L_D = L_E + 1024, L_W = 59392, WSTAGE = 24576, LDS_BYTES = L_W + 4 * WSTAGE;
constexpr int NR1 = 6, NR2 = 5;

struct Params {
  const unsigned short* x; const unsigned short* wr; const unsigned short* wi;
  const unsigned short* w1; const unsigned short* b1; const unsigned short* w2; const unsigned short* b2;
  unsigned short* out; unsigned char* ws;
};

DI rsrc_t mkrs(const void* p, int bytes) { return __builtin_amdgcn_make_buffer_rsrc((void*)p, 0, bytes, 0x00020000); }
DI unsigned pk_bf(float a, float b) { f2 v = {a, b}; return __builtin_bit_cast(unsigned, __builtin_convertvector(v, b2)); }
DI unsigned pk_h(float a, float b) { f2 v = {a, b}; return __builtin_bit_cast(unsigned, __builtin_convertvector(v, h2)); }
DI float bf_lo(unsigned u) { return __builtin_bit_cast(float, u << 16); }
DI float bf_hi(unsigned u) { return __builtin_bit_cast(float, u & 0xffff0000u); }
DI unsigned mul_h2(unsigned a, unsigned b) { return __builtin_bit_cast(unsigned, __builtin_bit_cast(h2, a) * __builtin_bit_cast(h2, b)); }
DI float vcos(float x) { return __builtin_amdgcn_cosf(x); }
DI float vsin(float x) { return __builtin_amdgcn_sinf(x); }
DI v2u trread(lds_t p) { return __builtin_bit_cast(v2u, __builtin_amdgcn_ds_read_tr16_b64_v4i16((LAS s4*)p)); }
DI v4u cat(v2u a, v2u b) { return v4u{a.x, a.y, b.x, b.y}; }
DI v4u ldsr128(lds_t p) { return *(LAS v4u*)p; }
DI void ldsw128(lds_t p, v4u v) { *(LAS v4u*)p = v; }
DI void dma16(rsrc_t rs, lds_t dst, int voff, int soff) {
  const unsigned la = (unsigned)(size_t)dst;
  asm volatile("s_mov_b32 m0, %0\n\ts_nop 0\n\tbuffer_load_dwordx4 %1, %2, %3 offen lds" ::"s"(la), "v"(voff), "s"(rs), "s"(soff) : "memory");
}
template <int N> DI void dma_run(rsrc_t rs, lds_t dst, int voff, int soff, int sstep) {
  constexpr int R = N % 3, Q = N / 3;
#pragma unroll
  for (int n = 0; n < R; ++n) dma16(rs, dst + n * 1024, voff, soff + n * sstep);
  if constexpr (Q > 0) {
    const unsigned la = (unsigned)(size_t)dst + R * 1024;
    int so = soff + R * sstep;
    unsigned cnt;
    asm volatile("s_mov_b32 m0, %2\n\ts_mov_b32 %1, %7\n1:\n\tbuffer_load_dwordx4 %3, %4, %0 offen lds\n\ts_add_u32 %0, %0, %5\n\t"
                 "buffer_load_dwordx4 %3, %4, %0 offen offset:1024 lds\n\ts_add_u32 %0, %0, %5\n\tbuffer_load_dwordx4 %3, %4, %0 offen offset:2048 lds\n\t"
                 "s_add_u32 m0, m0, 3072\n\ts_add_u32 %0, %0, %6\n\ts_sub_u32 %1, %1, 1\n\ts_cbranch_scc0 1b"
                 : "+s"(so), "=&s"(cnt) : "s"(la), "v"(voff), "s"(rs), "s"(sstep - 1024), "s"(sstep + 2048), "n"(Q - 1) : "memory", "scc");
  }
}
struct Tile { unsigned la; int vo, so; };
DI Tile tile(lds_t dst, int vo, int so) { unsigned la = (unsigned)(size_t)dst; asm volatile("" : "+s"(la), "+s"(so)); asm volatile("" : "+v"(vo)); return Tile{la, vo, so}; }
template <int S0, int S1> DI void dma_tile(rsrc_t rs, const Tile& t) {
  const unsigned la = t.la;
  const int voff = t.vo;
  int so = t.so;
  unsigned cnt;
  asm volatile("s_mov_b32 m0, %2\n\ts_mov_b32 %1, 6\n1:\n\tbuffer_load_dwordx4 %3, %4, %0 offen sc1 lds\n\ts_add_u32 m0, m0, 4096\n\ts_add_u32 %0, %0, %5\n\t"
               "buffer_load_dwordx4 %3, %4, %0 offen sc1 lds\n\ts_add_u32 m0, m0, 4096\n\ts_add_u32 %0, %0, %6\n\ts_sub_u32 %1, %1, 1\n\ts_cbranch_scc0 1b"
               : "+s"(so), "=&s"(cnt) : "s"(la), "v"(voff), "s"(rs), "n"(S0), "n"(S1) : "memory", "scc");
}
template <int IMM> DI void ald128(v4u& o, rsrc_t rs, int voff, int soff) { asm volatile("buffer_load_dwordx4 %0, %1, %2, %3 offen offset:%4 sc1" : "=v"(o) : "v"(voff), "s"(rs), "s"(soff), "n"(IMM) : "memory"); }
DI void touch(rsrc_t rs, lds_t dummy, int voff, int soff) {
  const unsigned la = (unsigned)(size_t)dummy;
  asm volatile("s_mov_b32 m0, %0\n\ts_nop 0\n\tbuffer_load_dword %1, %2, %3 offen lds" ::"s"(la), "v"(voff), "s"(rs), "s"(soff) : "memory");
}
DI void dma4_sc1(rsrc_t rs, lds_t dst, int voff) {
  const unsigned la = (unsigned)(size_t)dst;
  asm volatile("s_mov_b32 m0, %0\n\ts_nop 0\n\tbuffer_load_dword %1, %2, 0 offen sc1 lds" ::"s"(la), "v"(voff), "s"(rs) : "memory");
}
template <int AUX> DI v4u ld128(rsrc_t rs, int voff, int soff = 0) { return __builtin_amdgcn_raw_buffer_load_b128(rs, voff, soff, AUX); }
template <int AUX> DI void st128(v4u v, rsrc_t rs, int voff, int soff = 0) { __builtin_amdgcn_raw_buffer_store_b128(v, rs, voff + soff, 0, AUX); }
DI f4 asf4(v4u v) { return __builtin_bit_cast(f4, v); }
DI v4u asu4(f4 v) { return __builtin_bit_cast(v4u, v); }
template <int N> DI void vmwait() { asm volatile("s_waitcnt vmcnt(%0)" ::"n"(N) : "memory"); }
template <int N> DI void lgwait() { asm volatile("s_waitcnt lgkmcnt(%0)" ::"n"(N) : "memory"); }
DI void rawbar() { asm volatile("" ::: "memory"); __builtin_amdgcn_s_barrier(); asm volatile("" ::: "memory"); }
DI void wgbar() { lgwait<0>(); rawbar(); }
#define SB() __builtin_amdgcn_sched_barrier(0)
template <class T> DI void pin1(T& v) { asm volatile("" : "+v"(v)); }
template <class T, int N> DI void pin(T (&a)[N]) {
#pragma unroll
  for (int n = 0; n < N; ++n) pin1(a[n]);
}
template <int A, int B, int N> DI void pin_acc(v4u (&W)[N][4]) {
#pragma unroll
  for (int ks = A; ks < B; ++ks)
#pragma unroll
    for (int n = 0; n < 4; ++n) asm volatile("" : "+a"(W[ks][n]));
}
template <int V> struct IC { static constexpr int v = V; };
template <int A, int B, class Fn> DI void static_for(Fn&& fn) { if constexpr (A < B) { fn(IC<A>{}); static_for<A + 1, B>(fn); } }

DI float diag(const f4& c, int l15) { const float a = l15 & 1 ? c[1] : c[0], b = l15 & 1 ? c[3] : c[2]; return l15 & 2 ? b : a; }

#define MF1(NAME, OP, AC) template <int MODE> DI void NAME(f4& d, v4u a, v4u b) { \
  if constexpr (MODE == 1) asm volatile(OP " %0, %1, %2, 0" : "=&v"(d) : AC(a), "v"(b)); \
  else asm volatile(OP " %0, %1, %2, %0" : "+v"(d) : AC(a), "v"(b)); \
  SB(); }
MF1(mf1, "v_mfma_f32_16x16x32_f16", "v")
MF1(mb1, "v_mfma_f32_16x16x32_bf16", "v")
MF1(mb1a, "v_mfma_f32_16x16x32_bf16", "a")
#undef MF1
template <int N> DI void snop() { asm volatile("s_nop %0" ::"n"(N - 1)); SB(); }
template <int N> DI void mwait(f4& a) { asm volatile("s_nop %1" : "+v"(a) : "n"(N - 1)); SB(); }
template <int N> DI void mwait(f4 (&a)[4]) { asm volatile("s_nop %4" : "+v"(a[0]), "+v"(a[1]), "+v"(a[2]), "+v"(a[3]) : "n"(N - 1)); SB(); }

struct Ids { int lane, w, l15, g; };
DI Ids get_ids() { Ids d; d.lane = threadIdx.x & 63; d.w = __builtin_amdgcn_readfirstlane(threadIdx.x >> 6); d.l15 = d.lane & 15; d.g = d.lane >> 4; return d; }
DI Ids fresh(const Ids& a) { Ids d; d.lane = a.lane; d.w = a.w; asm volatile("" : "+v"(d.lane), "+s"(d.w)); d.l15 = d.lane & 15; d.g = d.lane >> 4; return d; }
DI int fresh_s(int v) { asm volatile("" : "+s"(v)); return v; }

DI int img_src(const Ids& id, int pitch) { const int r7 = id.lane >> 3; return r7 * pitch + (((id.lane & 7) ^ (r7 & 6)) << 4); }
DI int img_rd(const Ids& id, int n) { const int row = 4 * id.g + (id.l15 >> 2); return row * 128 + 32 * (n ^ id.w ^ ((row >> 1) & 3)) + 8 * (id.l15 & 3); }
DI v4u img_frag(lds_t p) { return cat(trread(p), trread(p + 2048)); }
struct WSrc { rsrc_t rs; int pitch, base, vo; lds_t stage; };
DI WSrc w_src(const Ids& id, const void* w, int bytes, int pitch, int rows_per_wave, int col0, lds_t L) {
  int base = id.w * rows_per_wave * pitch + col0, vo = img_src(id, pitch), so = L_W + id.w * WSTAGE;
  asm volatile("" : "+s"(base), "+s"(so)); pin1(vo);
  return WSrc{mkrs(w, bytes), pitch, base, vo, L + so};
}
template <int N0, int N1> DI void w_issue(const WSrc& ws, int k0, lds_t dst) {
  dma_run<N1 - N0>(ws.rs, dst + N0 * 1024, ws.vo, ws.base + (32 * k0 + 8 * N0) * ws.pitch, 8 * ws.pitch);
}
struct Img { int o[4]; };
DI Img img_set(const Ids& id) { Img m; for (int n = 0; n < 4; ++n) m.o[n] = img_rd(id, n); pin(m.o); return m; }
template <int CNT, int O, int N> DI void w_read(const Img& im, lds_t stage, v4u (&W)[N][4]) {
  lds_t b[4];
#pragma unroll
  for (int n = 0; n < 4; ++n) b[n] = stage + im.o[n];
#pragma unroll
  for (int ks = 0; ks < CNT; ++ks)
#pragma unroll
    for (int n = 0; n < 4; ++n) W[O + ks][n] = img_frag(b[n] + ks * 4096);
}
DI v4u pair_frag(int w, v2u a, v2u b) { const bool sw = w & 1; return v4u{sw ? b.x : a.x, sw ? b.y : a.y, sw ? a.x : b.x, sw ? a.y : b.y}; }

template <class Fn> DI void gen_tf(const Ids& id, int j, v4u (&T)[14], Fn mid) {
  const int c = 64 * j + 16 * id.w + id.l15;
  const bool dc = c == 447;
  const float kf = dc ? 0.f : (float)((c >> 1) + 1) * (1.0f / 448.0f);
  const float base = __builtin_fmaf((float)(8 * id.g), kf, (c & 1) && !dc ? 0.25f : 0.f);
  const float tc = 2.0f * vcos(32.0f * kf);
  f2 v[2][4];
#pragma unroll
  for (int ks = 0; ks < 14; ++ks) {
    unsigned u[4];
#pragma unroll
    for (int e = 0; e < 4; ++e) {
      if (ks < 2) v[ks][e] = f2{vcos(__builtin_fmaf((float)(32 * ks + 2 * e), kf, base)), vcos(__builtin_fmaf((float)(32 * ks + 2 * e + 1), kf, base))};
      else v[ks & 1][e] = v[~ks & 1][e] * tc - v[ks & 1][e];
      u[e] = pk_bf(v[ks & 1][e][0], v[ks & 1][e][1]);
    }
    T[ks] = v4u{u[0], u[1], u[2], u[3]};
    pin1(T[ks]);
    if (ks == 7) mid();
  }
}
DI Tile f0_tile(const Ids& id, int xbase, int i, lds_t L) {
  const int r7 = id.lane >> 3;
  return tile(L + L_A + id.w * 1024, r7 * 896 + (((id.lane & 7) ^ (2 * id.w + ((r7 >> 1) & 1))) << 4), xbase + (64 * i + 8 * id.w) * 896);
}
DI void f0_issue(rsrc_t rx, const Tile& t) { dma_tile<28672, -28544>(rx, t); }
constexpr int f0_k(int u) { return u < 40 ? u >> 2 : (u < 44 ? u - 30 : 10 + (u - 44) / 3); }
constexpr int f0_n(int u) { return u < 40 ? u & 3 : (u < 44 ? 0 : 1 + (u - 44) % 3); }
struct F0A { lds_t pa[2][4]; LAS float* ew; LAS float* sw; f2 sc; int so; };
DI F0A f0_addr(const Ids& id, int layer, int i, int j, lds_t L) {
  F0A a;
  const int rho = id.l15, xr = (rho >> 1) & 7;
  const int rowb = (8 * (rho >> 2) + (rho & 3)) * 128, tw = 4096 * (id.w >> 1) + 512 * (id.w & 1);
#pragma unroll
  for (int n = 0; n < 4; ++n) {
    const int o = rowb + ((4096 * (n >> 1) + 512 * (n & 1)) ^ tw);
    a.pa[0][n] = L + L_A + o + ((id.g ^ xr) << 4); a.pa[1][n] = L + L_A + o + (((id.g ^ xr) << 4) ^ 64);
  }
  const int c = 64 * j + 16 * id.w + id.l15;
  LAS float* eb = (LAS float*)(L + L_E);
  a.ew = id.g == rho >> 2 ? eb + 32 * (id.w >> 1) + 4 * (id.w & 1) + 8 * (rho >> 2) + (rho & 3) : (LAS float*)(L + L_D) + rho;
  a.sw = eb + 64 + 8 * id.g;
  a.sc[0] = a.sc[1] = c == 447 ? 0.f : (c == 446 ? 0.047245559126153f : 0.066815310478106f);
  a.so = c * 128 + 16 * id.g + XH0_OFF + layer * XB + i * 57344 + 64 * (id.w >> 1);
  pin(a.pa[0]); pin(a.pa[1]); pin1(a.ew); pin1(a.sw); pin1(a.sc); pin1(a.so);
  return a;
}
DI void f0_compute(const Ids& id, const F0A& fa, rsrc_t rw, int layer, int i, int j, lds_t L, const v4u (&T)[14]) {
  constexpr int YOUNG0 = 3, PD = 8;
  const int rho = id.l15, so = fa.so;
  const lds_t (&pa)[2][4] = fa.pa;
  LAS float* eb = (LAS float*)(L + L_E);
  LAS float* ew = fa.ew; LAS float* sw_ = fa.sw;
  const f2 sc = fa.sc;
  const bool sums = id.w == 3 && rho == 15;
  if (layer == 0) vmwait<10 + YOUNG0>(); else vmwait<10>();
  rawbar();
  v4u a[PD];
#pragma unroll
  for (int u = 0; u < PD; ++u) a[u] = ldsr128(pa[f0_k(u) & 1][f0_n(u)] + (f0_k(u) >> 1) * 8192);
  SB();
  f4 acc[4], gram;
  static_for<0, 56>([&](auto U) {
    constexpr int u = U.v, ks = f0_k(u), n = f0_n(u), v = u + PD;
    const v4u cur = a[u % PD];
    if constexpr (n == 0) mb1<ks == 0>(gram, cur, cur);
    mb1<ks == 0>(acc[n], cur, T[ks]);
    if constexpr (v == 16) { if (layer == 0) vmwait<4 + YOUNG0>(); else vmwait<4>(); rawbar(); }
    if constexpr (v == 40) { if (layer == 0) vmwait<YOUNG0>(); else vmwait<0>(); rawbar(); }
    if constexpr (v < 56) a[u % PD] = ldsr128(pa[f0_k(v) & 1][f0_n(v)] + (f0_k(v) >> 1) * 8192);
    if constexpr (u == 45) { mwait<4>(gram); *ew = diag(gram, rho); }
    if constexpr (u == 50) { vmwait<0>(); lgwait<2>(); rawbar(); }
    SB();
  });
  mwait<8>(acc);
  if (sums) {
#pragma unroll
    for (int n = 0; n < 4; ++n) *(LAS f4*)(sw_ + 32 * ((n ^ 3) >> 1) + 4 * ((n ^ 3) & 1)) = acc[n];
  }
  SB();
#pragma unroll
  for (int q = 0; q < 2; ++q) {
    const f4 x = acc[2 * q], y = acc[2 * q + 1];
    const f2 x0 = f2{x[0], x[1]} * sc, x1 = f2{x[2], x[3]} * sc, y0 = f2{y[0], y[1]} * sc, y1 = f2{y[2], y[3]} * sc;
    st128<SC1>(pair_frag(id.w, v2u{pk_h(x0[0], x0[1]), pk_h(x1[0], x1[1])}, v2u{pk_h(y0[0], y0[1]), pk_h(y1[0], y1[1])}), rw, so ^ (64 * q));
    SB();
  }
  if (j == 6 && id.w == 3) {
    lgwait<0>();
    const float m = eb[64 + id.lane] * (1.0f / 448.0f);
    const float r = __builtin_amdgcn_rsqf(__builtin_fmaxf(__builtin_fmaf(-m, m, eb[id.lane] * (1.0f / 448.0f)), 0.f) + EPS);
    __builtin_amdgcn_raw_buffer_store_b32(__builtin_bit_cast(unsigned, m), rw, id.lane * 4, MU_OFF + (layer * S + 64 * i) * 4, SC1);
    __builtin_amdgcn_raw_buffer_store_b16((unsigned short)(pk_h(r, r) & 0xffffu), rw, id.lane * 2, RS_OFF + (layer * S + 64 * i) * 2, SC1);
  }
}
constexpr int WT_LD = 3;
DI void wt_issue(const Params& p, const Ids& id, int kg, int g, int cg, lds_t L) {
  const rsrc_t rs = mkrs(id.w < 2 ? p.wr : p.wi, S * NKF * 2);
#pragma unroll
  for (int n = 0; n < WT_LD; ++n) {
    const int pc = (id.w & 1) * 192 + n * 64 + id.lane, row = pc / 5, ch = pc - 5 * row;
    const int vo = ((((row & 7) + 64 * ((row >> 3) & 7)) * NKF + 1) * 2 & ~3) + 16 * ch;
    dma16(rs, L + L_W + fresh_s(id.w * 3072) + (id.w & 2) * 1024 + n * 1024, vo + ((8 * kg + 512 * g) * NKF + 32 * cg) * 2, 0);
  }
}
DI void wt_store(rsrc_t rw, int kg, int g, int cg, lds_t L) {
  int t = threadIdx.x;
  pin1(t);
  const LAS unsigned short* q = (const LAS unsigned short*)(L + L_W + (t & 7) * 80 + (t >> 3) * 2 + (~t & 1) * 2);
  unsigned u[8];
#pragma unroll
  for (int m = 0; m < 8; ++m) u[m] = (unsigned)q[320 * m] | ((unsigned)q[320 * m + 4096] << 16);
  const int so = WT_OFF + 32 * cg * 8192 + (kg >> 1) * 1024 + (g * 16 + (kg & 1) * 8) * 16, vo = (t >> 3) * 8192 + (t & 7) * 16;
  st128<SC1>(v4u{u[0], u[1], u[2], u[3]}, rw, vo, so);
  st128<SC1>(v4u{u[4], u[5], u[6], u[7]}, rw, vo + 4096, so);
}

struct PC { v4u fr64[4], fi64[4], fr32[4], fi32[4]; v4u twc, tws, tic, tis; };
DI void tuple(unsigned c0, unsigned c1, unsigned s0, unsigned s1, v4u& re, v4u& im) {
  re = v4u{c0, c1, s0, s1}; im = v4u{s0 ^ 0x80008000u, s1 ^ 0x80008000u, c0, c1};
  pin1(re); pin1(im);
}
DI void cs4(float x0, float dx, unsigned& c0, unsigned& c1, unsigned& s0, unsigned& s1) {
  const float x1 = x0 + dx, x2 = x1 + dx, x3 = x2 + dx;
  c0 = pk_h(vcos(x0), vcos(x1)); c1 = pk_h(vcos(x2), vcos(x3)); s0 = pk_h(vsin(x0), vsin(x1)); s1 = pk_h(vsin(x2), vsin(x3));
}
DI void gen_pc_a(const Ids& id, PC& k) {
  unsigned c0, c1, s0, s1;
  const float a = (float)(16 * id.w + id.l15) * (1.0f / 64.0f);
  cs4((float)(4 * id.g) * a, a, c0, c1, s0, s1);
  const bool odd = id.l15 & 1;
  const unsigned mc = ((id.l15 + 1) & 2) ? 0x80008000u : 0u, ms = (id.l15 & 2) ? 0x80008000u : 0u;
#pragma unroll
  for (int ks = 0; ks < 4; ++ks) {
    tuple(c0, c1, s0, s1, k.fr64[ks], k.fi64[ks]);
    const unsigned a0 = (odd ? s0 : c0) ^ mc, a1 = (odd ? s1 : c1) ^ mc, b0 = (odd ? c0 : s0) ^ ms, b1 = (odd ? c1 : s1) ^ ms;
    c0 = a0; c1 = a1; s0 = b0; s1 = b1;
  }
}
DI void gen_pc_b(const Ids& id, PC& k) {
  const float pl = (float)(8 * (id.l15 >> 2) + (id.l15 & 3));
#pragma unroll
  for (int t = 0; t < 4; ++t) {
    unsigned c0, c1, s0, s1;
    const float a = (pl + (float)(4 * (t >> 1))) * (1.0f / 32.0f);
    cs4((float)(8 * id.g + 4 * (t & 1)) * a, a, c0, c1, s0, s1);
    tuple(c0, c1, s0, s1, k.fr32[t], k.fi32[t]);
  }
}
DI void gen_pc_c(const Ids& id, PC& k) {
  const float pl = (float)(8 * (id.l15 >> 2) + (id.l15 & 3)), a = (float)(16 * id.w + id.l15) * (1.0f / 2048.0f), kb = (float)(16 * id.w + 4 * id.g);
  unsigned c[4], s[4], d[4], e[4];
#pragma unroll
  for (int t = 0; t < 2; ++t) {
    cs4((float)(8 * id.g + 4 * t) * a, a, c[2 * t], c[2 * t + 1], s[2 * t], s[2 * t + 1]);
    const float b = (pl + (float)(4 * t)) * (1.0f / 2048.0f);
    cs4(kb * b, b, d[2 * t], d[2 * t + 1], e[2 * t], e[2 * t + 1]);
  }
#pragma unroll
  for (int n = 0; n < 4; ++n) {
    k.twc[n] = mul_h2(c[n], 0x30003000u); k.tws[n] = mul_h2(s[n], 0x30003000u); k.tic[n] = mul_h2(d[n], 0x30003000u); k.tis[n] = mul_h2(e[n], 0x30003000u);
  }
  pin1(k.twc); pin1(k.tws); pin1(k.tic); pin1(k.tis);
}
struct Q4 { unsigned d[4]; DI v4u v() const { return v4u{d[0], d[1], d[2], d[3]}; } };
struct CM { unsigned a, b, u, v; };
DI void cm_cvt(CM& m, const f4& re, const f4& im, int h) {
  asm volatile("v_cvt_pk_f16_f32 %0, %2, %3\n\tv_cvt_pk_f16_f32 %1, %4, %5" : "=&v"(m.a), "=v"(m.b) : "v"(re[2 * h]), "v"(re[2 * h + 1]), "v"(im[2 * h]), "v"(im[2 * h + 1])); SB(); }
DI void cm_mul(CM& m, unsigned x, unsigned xs, unsigned y, unsigned ys) {
  asm volatile("v_pk_mul_f16 %0, %2, %3\n\tv_pk_mul_f16 %1, %4, %5" : "=&v"(m.u), "=v"(m.v) : "v"(x), "v"(xs), "v"(y), "v"(ys)); SB(); }
DI void cm_fma(unsigned& o0, unsigned& o1, const CM& m, unsigned x, unsigned xs, unsigned y, unsigned ys) {
  asm volatile("v_pk_fma_f16 %0, %2, %3, %6\n\tv_pk_fma_f16 %1, %4, %5, %7 neg_lo:[0,0,1] neg_hi:[0,0,1]" : "=&v"(o0), "=v"(o1) : "v"(x), "v"(xs), "v"(y), "v"(ys), "v"(m.u), "v"(m.v)); SB(); }
template <bool CONJW> DI void cmul(int n, CM& m, Q4& o, const f4& re, const f4& im, const v4u& c, const v4u& s, int t) {
  const int h = n / 3;
  const unsigned cc = c[2 * t + h], ss = s[2 * t + h];
  if (n % 3 == 0) cm_cvt(m, re, im, h);
  else if (n % 3 == 1) { if (CONJW) cm_mul(m, m.b, ss, m.b, cc); else cm_mul(m, m.b, ss, m.a, ss); }
  else { if (CONJW) cm_fma(o.d[h], o.d[2 + h], m, m.a, cc, m.a, ss); else cm_fma(o.d[h], o.d[2 + h], m, m.a, cc, m.b, cc); }
}
template <bool CONJW> DI void cmul_all(Q4& o, const f4& re, const f4& im, const v4u& c, const v4u& s, int t) {
  unsigned a0, b0, a1, b1;
#define CM_HEAD "v_cvt_pk_f16_f32 %4, %8, %9\n\tv_cvt_pk_f16_f32 %5, %12, %13\n\tv_cvt_pk_f16_f32 %6, %10, %11\n\tv_cvt_pk_f16_f32 %7, %14, %15\n\tv_pk_mul_f16 %0, %5, %18\n\tv_pk_mul_f16 %1, %7, %19\n\t"
#define CM_OPS : "=&v"(o.d[0]), "=&v"(o.d[1]), "=&v"(o.d[2]), "=&v"(o.d[3]), "=&v"(a0), "=&v"(b0), "=&v"(a1), "=&v"(b1) \
               : "v"(re[0]), "v"(re[1]), "v"(re[2]), "v"(re[3]), "v"(im[0]), "v"(im[1]), "v"(im[2]), "v"(im[3]), "v"(c[2 * t]), "v"(c[2 * t + 1]), "v"(s[2 * t]), "v"(s[2 * t + 1])
  if constexpr (CONJW)
    asm volatile(CM_HEAD "v_pk_mul_f16 %2, %5, %16\n\tv_pk_mul_f16 %3, %7, %17\n\tv_pk_fma_f16 %0, %4, %16, %0\n\tv_pk_fma_f16 %1, %6, %17, %1\n\t"
                 "v_pk_fma_f16 %2, %4, %18, %2 neg_lo:[0,0,1] neg_hi:[0,0,1]\n\tv_pk_fma_f16 %3, %6, %19, %3 neg_lo:[0,0,1] neg_hi:[0,0,1]" CM_OPS);
  else
    asm volatile(CM_HEAD "v_pk_mul_f16 %2, %4, %18\n\tv_pk_mul_f16 %3, %6, %19\n\tv_pk_fma_f16 %0, %4, %16, %0\n\tv_pk_fma_f16 %1, %6, %17, %1\n\t"
                 "v_pk_fma_f16 %2, %5, %16, %2 neg_lo:[0,0,1] neg_hi:[0,0,1]\n\tv_pk_fma_f16 %3, %7, %17, %3 neg_lo:[0,0,1] neg_hi:[0,0,1]" CM_OPS);
#undef CM_HEAD
#undef CM_OPS
  SB();
}
struct PA { lds_t tp[2], xp, xq; LAS v2u* z0; LAS v2u* z1; int ro, xo, wo; };
DI PA p_addr(const Ids& id, int layer, int jj, lds_t L) {
  PA a;
  const int s0 = 512 * id.w + 32 * id.l15 + 8 * id.g;
  a.ro = s0 * 2 + RS_OFF + layer * S * 2;
  a.xo = (s0 >> 6) * 57344 + (s0 & 63) * 2 + jj * 256 + XH0_OFF + layer * XB;
  a.wo = id.lane * 16 + WT_OFF + jj * 8192 + id.w * 1024;
  const int tr = 4 * id.g + (id.l15 >> 2), to = tr * 64 + (((id.l15 & 3) ^ (((tr >> 1) & 1) | ((tr >> 2) & 2))) << 4), th = (tr << 1) & 8;
  a.tp[0] = L + L_A + to + th; a.tp[1] = L + L_A + to + (th ^ 8);
  const int zc = (16 * id.w + id.l15) * 64 + ((id.g ^ (((id.l15 >> 1) & 1) | ((id.l15 >> 2) & 2))) << 4), zh = (id.l15 << 1) & 8;
  a.z0 = (LAS v2u*)(L + L_A + zc + zh); a.z1 = (LAS v2u*)(L + L_A + zc + (zh ^ 8));
  a.xp = L + L_A + 8192 + 2 * id.w * 1024 + id.lane * 16; a.xq = L + L_A + 8192 + id.lane * 16;
  pin(a.tp); pin1(a.z0); pin1(a.z1); pin1(a.xp); pin1(a.xq); pin1(a.ro); pin1(a.xo); pin1(a.wo);
  return a;
}
template <class Fn> DI void p_phase(const PA& pa, rsrc_t rw, int jj, const PC& k, Fn slot) {
  v4u rs, xr, xi, wt[2];
  ald128<0>(rs, rw, pa.ro, 0); ald128<0>(xr, rw, pa.xo, 0); ald128<128>(xi, rw, pa.xo, 0);
  ald128<0>(wt[0], rw, pa.wo, 0); ald128<0>(wt[1], rw, pa.wo, 4096);
  lds_t tp[2] = {pa.tp[0], pa.tp[1]}, xp = pa.xp, xq = pa.xq;
  LAS v2u* z0 = pa.z0; LAS v2u* z1 = pa.z1;
  int so = pa.xo + (XH1_OFF - XH0_OFF);
  pin1(so);
  slot();
  vmwait<10>(); SB();
  pin1(rs); pin1(xr); pin1(xi);
  {
    z0[0] = v2u{mul_h2(xr[0], rs[0]), mul_h2(xr[1], rs[1])}; z1[0] = v2u{mul_h2(xr[2], rs[2]), mul_h2(xr[3], rs[3])};
    z0[512] = v2u{mul_h2(xi[0], rs[0]), mul_h2(xi[1], rs[1])}; z1[512] = v2u{mul_h2(xi[2], rs[2]), mul_h2(xi[3], rs[3])};
  }
  wgbar();
  v4u a[8];
#pragma unroll
  for (int u = 0; u < 8; ++u) a[u] = cat(trread(tp[u >> 2] + (u & 3) * 1024), trread(tp[u >> 2] + 4096 + (u & 3) * 1024));
  vmwait<8>(); SB(); pin(wt);
  v4u wr, wi;
#pragma unroll
  for (int m = 0; m < 4; ++m) {
    const unsigned t0 = wt[m >> 1][2 * (m & 1)], t1 = wt[m >> 1][2 * (m & 1) + 1];
    wr[m] = mul_h2(pk_h(bf_lo(t0), bf_lo(t1)), 0x30003000u); wi[m] = mul_h2(pk_h(bf_hi(t0), bf_hi(t1)), 0xb000b000u);
  }
  pin1(wr); pin1(wi);
  SB();
  f4 r0, i0, r1, i1, r2, i2, r3, i3;
  Q4 q0, q1;
  CM m;
#define DFT64(R, I, U) mf1<(U & 3) == 0>(R, a[U], k.fr64[U & 3]); mf1<(U & 3) == 0>(I, a[U], k.fi64[U & 3])
#define TW1(n) cmul<false>(n, m, q0, r0, i0, k.twc, k.tws, 0)
  DFT64(r0, i0, 0); DFT64(r0, i0, 1); DFT64(r0, i0, 2); DFT64(r0, i0, 3);
  DFT64(r1, i1, 4); snop<6>();
  TW1(0); mf1<false>(r1, a[5], k.fr64[1]); TW1(1); mf1<false>(i1, a[5], k.fi64[1]); TW1(2); mf1<false>(r1, a[6], k.fr64[2]);
  TW1(3); mf1<false>(i1, a[6], k.fi64[2]); TW1(4); mf1<false>(r1, a[7], k.fr64[3]); TW1(5); mf1<false>(i1, a[7], k.fi64[3]);
#define TW2(n) cmul<false>(n, m, q1, r1, i1, k.twc, k.tws, 1)
  snop<1>();
  mf1<true>(r2, k.fr32[0], q0.v()); mf1<true>(i2, k.fi32[0], q0.v()); snop<5>();
  TW2(0); TW2(1); mf1<true>(r3, k.fr32[2], q0.v()); TW2(2); TW2(3); mf1<true>(i3, k.fi32[2], q0.v()); TW2(4); TW2(5);
  snop<2>();
  mf1<false>(r2, k.fr32[1], q1.v()); mf1<false>(i2, k.fi32[1], q1.v()); mf1<false>(r3, k.fr32[3], q1.v()); mf1<false>(i3, k.fi32[3], q1.v());
  snop<6>();
#define WM2(n) cmul<true>(n, m, q1, r3, i3, wr, wi, 1)
  cmul_all<true>(q0, r2, i2, wr, wi, 0);
  WM2(0); mf1<true>(r0, q0.v(), k.fr32[0]); WM2(1); mf1<true>(i0, q0.v(), k.fi32[0]); WM2(2); mf1<true>(r1, q0.v(), k.fr32[2]); WM2(3); mf1<true>(i1, q0.v(), k.fi32[2]);
  WM2(4); WM2(5); snop<2>();
  mf1<false>(r0, q1.v(), k.fr32[1]); mf1<false>(i0, q1.v(), k.fi32[1]); mf1<false>(r1, q1.v(), k.fr32[3]); mf1<false>(i1, q1.v(), k.fi32[3]);
  snop<6>();
  cmul_all<false>(q0, r0, i0, k.tic, k.tis, 0);
  ldsw128(xp, q0.v());
  cmul_all<false>(q1, r1, i1, k.tic, k.tis, 1);
  ldsw128(xp + 1024, q1.v());
  wgbar();
#pragma unroll
  for (int u = 0; u < 8; ++u) a[u] = ldsr128(xq + (2 * (u & 3) + (u >> 2)) * 1024);
  unsigned sr = 0x34003400u, si = jj == 223 ? 0u : 0xb400b400u;
  pin1(sr); pin1(si);
  SB();
  const auto fin = [&](int h, const f4& re, const f4& im, int n) {
    if (n == 0) cm_cvt(m, re, im, h & 1);
    else { asm volatile("v_pk_fma_f16 %0, %2, %3, %4\n\tv_pk_fma_f16 %1, %5, %6, %7" : "=&v"(q0.d[h]), "=v"(q1.d[h]) : "v"(m.a), "v"(sr), "v"(xr[h]), "v"(m.b), "v"(si), "v"(xi[h])); SB(); } };
  DFT64(r0, i0, 0); DFT64(r0, i0, 1); DFT64(r0, i0, 2); DFT64(r0, i0, 3);
  DFT64(r1, i1, 4); DFT64(r1, i1, 5); snop<4>();
  fin(0, r0, i0, 0); mf1<false>(r1, a[6], k.fr64[2]); fin(0, r0, i0, 1); mf1<false>(i1, a[6], k.fi64[2]);
  fin(1, r0, i0, 0); mf1<false>(r1, a[7], k.fr64[3]); fin(1, r0, i0, 1); mf1<false>(i1, a[7], k.fi64[3]);
  snop<8>();
  fin(2, r1, i1, 0); fin(2, r1, i1, 1); fin(3, r1, i1, 0); fin(3, r1, i1, 1);
  st128<SC1>(q0.v(), rw, so); st128<SC1>(q1.v(), rw, so + 128);
#undef DFT64
#undef TW1
#undef TW2
#undef WM2
}

DI void gen_ti(const Ids& id, int j, v4u (&T)[14]) {
  const float df = (float)(64 * j + 16 * id.w + id.l15) * (1.0f / 448.0f);
  const float base = (float)(2 * id.g + 1) * df;
  const float tc = 2.0f * vcos(16.0f * df);
  f2 v[2][4];
#pragma unroll
  for (int ks = 0; ks < 14; ++ks) {
    unsigned u[4];
#pragma unroll
    for (int e = 0; e < 4; ++e) {
      if (ks < 2) { const float x = __builtin_fmaf((float)(16 * ks + 8 * (e >> 1) + (e & 1)), df, base); v[ks][e] = f2{vcos(x), -vsin(x)}; }
      else v[ks & 1][e] = v[~ks & 1][e] * tc - v[ks & 1][e];
      float c = v[ks & 1][e][0];
      if (ks == 13 && e == 3) c *= id.g == 3 ? 0.70710678118654752f : 1.0f;
      u[e] = pk_h(c, v[ks & 1][e][1]);
    }
    T[ks] = v4u{u[0], u[1], u[2], u[3]};
    pin1(T[ks]);
  }
}
DI Tile s3_tile(const Ids& id, int layer, int i, lds_t L) { return tile(L + L_A + id.w * 1024, img_src(id, 128), XH1_OFF + layer * XB + i * 57344 + id.w * 1024); }
constexpr int s3_k(int u) { return u < 36 ? u >> 2 : (u < 41 ? u - 27 : 9 + (u - 41) / 3); }
constexpr int s3_n(int u) { return u < 36 ? u & 3 : (u < 41 ? 0 : 1 + (u - 41) % 3); }
struct S3A { LAS float* ew; LAS float* er[2]; int so; };
DI S3A s3_addr(const Ids& id, int layer, int i, int j, lds_t L) {
  S3A a;
  a.ew = id.g == id.l15 >> 2 ? (LAS float*)(L + L_E) + 16 * id.w + id.l15 : (LAS float*)(L + L_D) + id.l15;
  a.er[0] = (LAS float*)(L + L_E) + 16 * id.w + 4 * id.g; a.er[1] = (LAS float*)(L + L_E) + 16 * (id.w ^ 1) + 4 * id.g;
  a.so = id.lane * 16 + HF_OFF + layer * XB + ((j * 64 + 2 * i + (id.w >> 1)) * 4 + id.w) * 1024;
  pin1(a.ew); pin(a.er); pin1(a.so);
  return a;
}
DI void s3_compute(const Ids& id, const Img& im, const S3A& sa, rsrc_t rw, lds_t L, const v4u (&T)[14], f4 (&mu)[4], f4 (&x1)[4]) {
  constexpr int PD = 5;
  lds_t tb[4];
#pragma unroll
  for (int n = 0; n < 4; ++n) tb[n] = L + L_A + im.o[n];
  LAS float* ew = sa.ew; LAS float* er[2] = {sa.er[0], sa.er[1]};
  const int e2 = (id.w & 2) ? -32 : 32, so = sa.so;
  vmwait<10>(); rawbar();
  v4u a[PD];
#pragma unroll
  for (int u = 0; u < PD; ++u) a[u] = img_frag(tb[s3_n(u)] + s3_k(u) * 4096);
  SB();
  f4 acc[4], gram, rr[4];
  static_for<0, 56>([&](auto U) {
    constexpr int u = U.v, k = s3_k(u), n = s3_n(u), v = u + PD;
    const v4u cur = a[u % PD];
    if constexpr (n == 0) mf1<k == 0>(gram, cur, cur);
    mf1<k == 0>(acc[n], cur, T[k]);
    if constexpr (v == 16) { vmwait<5>(); rawbar(); }
    if constexpr (v == 36) { vmwait<0>(); rawbar(); pin(mu); }
    if constexpr (v < 56) a[u % PD] = img_frag(tb[s3_n(v)] + s3_k(v) * 4096);
    if constexpr (u == 42) {
      mwait<2>(gram);
      const float vr = diag(gram, id.l15) * (1.0f / 448.0f);
      const float r1 = __builtin_amdgcn_rsqf(vr + EPS);
      *ew = r1 * __builtin_amdgcn_rsqf(vr * r1 * r1 + EPS) * 0.066815310478106f;
    }
    if constexpr (u == 47) {
      lgwait<10>(); rawbar();
      rr[0] = *(LAS f4*)er[0]; rr[1] = *(LAS f4*)er[1]; rr[2] = *(LAS f4*)(er[0] + e2); rr[3] = *(LAS f4*)(er[1] + e2);
    }
    SB();
  });
  mwait<8>(acc);
  v2u hq[4];
#pragma unroll
  for (int n = 0; n < 4; ++n) { const f4 x = acc[n] * rr[n]; hq[n] = v2u{pk_bf(x[0], x[1]), pk_bf(x[2], x[3])}; }
  st128<SC1>(pair_frag(id.w, hq[0], hq[1]), rw, so);
  st128<SC1>(pair_frag(id.w, hq[2], hq[3]), rw, so ^ 4096);
  SB();
#pragma unroll
  for (int n = 0; n < 4; ++n) x1[n] = acc[n] * 0.066815310478106f + mu[n];
  pin(x1);
}

struct GA { int vo, sb[4]; Img im; };
template <int NKS> DI GA g_addr(const Ids& id, const Img& im, int abase) {
  GA g; g.vo = id.lane * 16; pin1(g.vo); g.im = im;
#pragma unroll
  for (int n = 0; n < 4; ++n) g.sb[n] = fresh_s(abase + id.w * NKS * 4096 + ((((n + 1) & 3) ^ id.w) << 10));
  return g;
}
template <int NKS, int W0, int SO, int NS, int NX, int NR>
DI void g_main(const Ids& id, rsrc_t rw, const GA& ga, lds_t L, const v4u (&R)[NR][4], v4u (&W)[10][4], f4 (&res)[4], const f4 (&init)[4]) {
  constexpr int WIN = GWIN, NU = 4 * NKS;
  v4u a[WIN];
  const int vo = ga.vo;
  const int (&sb)[4] = ga.sb;
#pragma unroll
  for (int u = 0; u < WIN; ++u) a[u] = ld128<SC1>(rw, vo, sb[u / NKS] + (u % NKS) * 4096);
  lds_t rd[4];
#pragma unroll
  for (int n = 0; n < 4; ++n) rd[n] = L + L_A + ((n ^ id.w) << 10) + vo;
  SB();
  vmwait<WIN>();
  w_read<NS, W0>(ga.im, L + L_W + id.w * WSTAGE + SO, W);
  w_read<NX, W0 + NS>(ga.im, L + L_A + id.w * 12288, W);
  SB();
  f4 part[4], acc[2][4], t[3];
  const auto add2 = [&](f4& d, const f4& x, const f4& y, int h) {
    asm volatile("v_add_f32 %0, %2, %4\n\tv_add_f32 %1, %3, %5" : "=&v"(d[2 * h]), "=v"(d[2 * h + 1]) : "v"(x[2 * h]), "v"(x[2 * h + 1]), "v"(y[2 * h]), "v"(y[2 * h + 1])); SB(); };
  static_for<0, NU>([&](auto U) {
    constexpr int u = U.v, qn = u / NKS, ks = u % NKS, v = u - 1 + WIN, PA = 6;
    constexpr bool adds = qn == 3 && ks > PA && ks <= PA + 4;
    f4 (&ac)[4] = acc[qn & 1];
    const v4u cur = a[u % WIN];
    const auto mm = [&](int f) {
      if constexpr (u == 3 * NKS) ac[f] = init[f];
      if constexpr (ks < NR) mb1a<ks == 0 && qn < 3>(ac[f], R[ks][f], cur);
      else if constexpr (ks < NR + 5) mb1a<0>(ac[f], W[ks - NR][f], cur);
      else mb1<0>(ac[f], W[ks - NR][f], cur); };
    f4 s01;
    mm(0);
    if constexpr (u >= 1 && v < NU) { a[(u - 1) % WIN] = ld128<SC1>(rw, vo, sb[v / NKS] + (v % NKS) * 4096); SB(); }
    if constexpr (adds) add2(s01, t[0], t[1], 0);
    mm(1);
    if constexpr (adds) add2(s01, t[0], t[1], 1);
    if constexpr (u == NKS + 1) wgbar();
    if constexpr (qn > 0 && ks >= 1 && ks < 5) { ldsw128(rd[ks - 1] + ((id.w ^ qn) * 3 + qn - 1) * 4096, asu4(acc[~qn & 1][ks - 1])); SB(); }
    mm(2);
    if constexpr (adds) add2(part[ks - PA - 1], s01, t[2], 0);
    mm(3);
    if constexpr (adds) add2(part[ks - PA - 1], s01, t[2], 1);
    if constexpr (qn == 3 && ks == PA) wgbar();
    if constexpr (qn == 3 && ks >= PA && ks < PA + 4) {
      lds_t rp = rd[ks - PA] + id.w * 12288;
      t[0] = asf4(ldsr128(rp)); t[1] = asf4(ldsr128(rp + 4096)); t[2] = asf4(ldsr128(rp + 8192));
    }
    SB();
  });
  mwait<8>(acc[1]);
#pragma unroll
  for (int f = 0; f < 4; ++f) res[f] = acc[1][f] + part[f];
}
DI void load_bias(const Ids& id, const Params& p, int o1, int o2, v2u (&b1)[4], v2u (&b2)[4]) {
  const rsrc_t r1 = mkrs(p.b1, F * 2), r2 = mkrs(p.b2, S * 2);
#pragma unroll
  for (int f = 0; f < 4; ++f) {
    b1[f] = __builtin_amdgcn_raw_buffer_load_b64(r1, 8 * id.g, 2 * o1 + 32 * (f ^ id.w), 0);
    b2[f] = __builtin_amdgcn_raw_buffer_load_b64(r2, 8 * id.g, 2 * o2 + 32 * (f ^ id.w), 0);
  }
  SB();
}
DI f4 bias4(v2u b) { return f4{bf_lo(b.x), bf_hi(b.x), bf_lo(b.y), bf_hi(b.y)}; }
struct GeluK { f2 c1, c0, one; };
DI GeluK gelu_k() { GeluK k{{0.044715f * -2.302208198144325f, 0.044715f * -2.302208198144325f}, {-2.302208198144325f, -2.302208198144325f}, {1.0f, 1.0f}}; pin1(k.c1); pin1(k.c0); return k; }
DI unsigned gelu2(f2 u, const GeluK& k) {
  const f2 z = u * (u * u * k.c1 + k.c0);
  const f2 d = f2{__builtin_amdgcn_exp2f(z[0]), __builtin_amdgcn_exp2f(z[1])} + k.one;
  const f2 g = u * f2{__builtin_amdgcn_rcpf(d[0]), __builtin_amdgcn_rcpf(d[1])};
  return pk_bf(g[0], g[1]);
}
DI int g1_addr(const Ids& id, int layer, int j, int m) { int so = id.lane * 16 + GF_OFF + layer * GB + ((j * 56 + 2 * m + (id.w >> 1)) * 4 + id.w) * 1024; pin1(so); return so; }
DI void g1_epi(int w, rsrc_t rw, int so, const GeluK& k, const f4 (&res)[4]) {
#pragma unroll
  for (int q = 0; q < 2; ++q) {
    v2u o[2];
#pragma unroll
    for (int e = 0; e < 2; ++e) {
      const f4 u = res[2 * q + e];
      o[e] = v2u{gelu2(f2{u[0], u[1]}, k), gelu2(f2{u[2], u[3]}, k)};
      pin1(o[e]); SB();
    }
    st128<SC1>(pair_frag(w, o[0], o[1]), rw, so ^ (q << 12));
  }
}
struct G2A { lds_t wp[4], tp[4]; int vo; };
DI G2A g2_addr(const Ids& id, lds_t L) {
  G2A a;
  lds_t T = L + L_A + 49152;
  const int d0 = 16 * id.w + id.l15, h0 = (d0 & 15) ^ ((d0 >> 4) & 1) ^ (((d0 >> 5) & 1) << 2), q = id.l15 >> 2, p = id.l15 & 3;
#pragma unroll
  for (int n = 0; n < 4; ++n) {
    a.wp[n] = T + d0 * 128 + (((4 * (n ^ id.w) + id.g) ^ h0) << 3);
    const int sc = 4 * id.w + (n & 2) + (id.g >> 1), d = 8 * (4 * (id.g & 1) + p) + 4 * (n & 1) + q;
    a.tp[n] = T + d * 128 + ((sc ^ (d & 15) ^ ((d >> 4) & 1) ^ (((d >> 5) & 1) << 2)) << 3);
  }
  a.vo = (16 * id.w + 4 * (id.lane >> 5) + (id.lane & 3)) * 896 + 16 * ((id.lane >> 2) & 7);
  pin(a.wp); pin(a.tp); pin1(a.vo);
  return a;
}
DI void g2_epi(const G2A& a, rsrc_t ro, int obase, const f4 (&res)[4], bool plain) {
#pragma unroll
  for (int q = 0; q < 4; ++q) *(LAS v2u*)a.wp[q] = v2u{pk_bf(res[q][0], res[q][1]), pk_bf(res[q][2], res[q][3])};
  wgbar();
  const v4u o0 = cat(trread(a.tp[0]), trread(a.tp[1])), o1 = cat(trread(a.tp[2]), trread(a.tp[3]));
  SB();
  if (plain) { st128<0>(o0, ro, a.vo, obase); st128<0>(o1, ro, a.vo, obase + 8 * 896); } else { st128<SC1>(o0, ro, a.vo, obase); st128<SC1>(o1, ro, a.vo, obase + 8 * 896); }
}

constexpr int C_EP = 0, C_FL = 1, C_H = 15, C_F0 = 16, C_P = C_F0 + 2 * 128, C_S3 = C_P + 2 * 128, C_G1 = C_S3 + 2 * 7 * 32, C_G2 = C_G1 + 2 * 7 * 32, C_LINES = C_G2 + 32 * 8;
static_assert(C_LINES * 128 <= CTRL_BYTES && C_LINES == C_F0 + 128 + 3 * 512, "control block");
constexpr unsigned SPIN_CAP = 1u << 20;
DI unsigned ld_word(unsigned char* ws, int line) { return __hip_atomic_load((gu32*)ws + 32 * line, __ATOMIC_RELAXED, __HIP_MEMORY_SCOPE_AGENT); }
DI void st_word(unsigned char* ws, int line, unsigned v) { __hip_atomic_store((gu32*)ws + 32 * line, v, __ATOMIC_RELAXED, __HIP_MEMORY_SCOPE_AGENT); }
template <int NS, int NR, int YOUNG = 0, class Fn> DI void seam_arrive(const Ids& id, unsigned char* ws, int line0, int shard, Fn work, bool on = true) {
  gu32* ctr = (gu32*)ws + 32 * (line0 + id.lane * NS + shard);
  const bool go = on && id.w == 0 && id.lane < NR;
  pin1(ctr);
  SB(); work(); SB();
  vmwait<YOUNG>();
  wgbar();
  if (go) __hip_atomic_fetch_add(ctr, 1u, __ATOMIC_RELAXED, __HIP_MEMORY_SCOPE_AGENT);
}
template <int NS> DI void poll_early(unsigned& v, const Ids& id, rsrc_t rw, int line0, int rep) {
  const int vo = id.w == 0 ? 128 * (line0 + rep * NS + (id.lane & (NS - 1))) : 0x7ffffff0;
  asm volatile("buffer_load_dword %0, %1, %2, 0 offen sc1" : "+v"(v) : "v"(vo), "s"(rw) : "memory");
}
template <int NS, int YOUNG> DI void seam_wait(const Ids& id, unsigned char* ws, unsigned first, int line0, int rep, unsigned want) {
  vmwait<YOUNG>();
  SB(); pin1(first);
  if (id.w == 0 && !__all(first >= want)) {
    const int line = line0 + rep * NS + (id.lane & (NS - 1));
    for (unsigned sp = 0; sp < SPIN_CAP; ++sp)
      if (__all(ld_word(ws, line) >= want)) break;
  }
  __builtin_amdgcn_fence(__ATOMIC_ACQUIRE, "wavefront");
  wgbar();
}
DI void wait_init(const Ids& id, unsigned char* ws, int rep, unsigned tag) {
  if (id.w == 0) {
    for (unsigned sp = 0; sp < SPIN_CAP; ++sp)
      if (__all(ld_word(ws, C_FL + rep) == tag)) break;
    __builtin_amdgcn_fence(__ATOMIC_ACQUIRE, "wavefront");
  }
}

DI void poll_ge(unsigned char* ws, int line, unsigned want) {
  for (unsigned sp = 0; sp < SPIN_CAP; ++sp) {
    if (__all(ld_word(ws, line) >= want)) break;
    __builtin_amdgcn_s_sleep(12);
  }
}

template <class T> DI T* fresh_p(T* v) { asm volatile("" : "+s"(v)); return v; }
DI Params fresh_p(const Params& a) { return Params{fresh_p(a.x), fresh_p(a.wr), fresh_p(a.wi), fresh_p(a.w1), fresh_p(a.b1), fresh_p(a.w2), fresh_p(a.b2), fresh_p(a.out), fresh_p(a.ws)}; }

DI void helper(const Params& p, lds_t L, const Ids& id0) {
  const int b = blockIdx.x, q = b >> 3, x8 = b & 7;
  const unsigned want0 = (id0.lane & 15) < 4 ? 15u : 14u;
  const unsigned tag = ld_word(p.ws, C_EP) + 1u;
  const rsrc_t r2 = mkrs(p.w2, S * F * 2), r1 = mkrs(p.w1, x8 < 7 ? S * F * 2 : 0);
  const int h = 4 * (q - 28) + id0.w;
  const int v1 = (id0.lane >> 2) * (F * 2) + (4 * x8 + (id0.lane & 3)) * 128, v2 = (id0.lane >> 2) * (S * 2) + (4 * x8 + (id0.lane & 3)) * 128;
  const auto t1 = [&](int n0, int n1) { for (int n = h + n0; n < n1; n += 16) touch(r1, L + L_D, v1, ((n & 3) * 512 + (n >> 2) * 16) * F * 2); };
  const auto t2 = [&](int n0, int n1) { for (int n = h + n0; n < n1; n += 16) touch(r2, L + L_D, v2, ((n & 3) * 448 + (n >> 2) * 16) * S * 2); };
  if (x8 == 7) {
    if (q == 28) {
      if (id0.w == 0) {
        st_word(p.ws, C_F0 + id0.lane, 0u); st_word(p.ws, C_F0 + 64 + id0.lane, 0u);
        if (id0.lane == 0) st_word(p.ws, C_H, 0u);
        vmwait<0>();
        if (id0.lane < 8) st_word(p.ws, C_FL + id0.lane, tag);
        vmwait<0>();
        if (id0.lane < 8) __hip_atomic_fetch_add((gu32*)p.ws + 32 * (C_F0 + id0.lane * 16), 1u, __ATOMIC_RELAXED, __HIP_MEMORY_SCOPE_AGENT);
        vmwait<0>();
      }
      wgbar();
    } else {
      const int n = C_F0 + 128 + (q - 29) * 512 + (int)threadIdx.x;
      st_word(p.ws, n, 0u); st_word(p.ws, n + 256, 0u);
      wait_init(id0, p.ws, x8, tag);
      seam_arrive<16, 8>(id0, p.ws, C_F0, q - 28, []() {});
      vmwait<0>(); wgbar();
    }
  }
  t1(0, 48);
  if (h == 0) { touch(mkrs(p.b1, F * 2), L + L_D, id0.lane * 128, 0); touch(mkrs(p.b2, S * 2), L + L_D, id0.lane * 128, 0); }
  t1(48, 96); t2(0, 40); t1(96, 128);
  vmwait<0>();
  if (id0.w == 0) {
    if (x8 < 7) {
      wait_init(id0, p.ws, x8, tag);
      if (id0.lane == 0) __hip_atomic_fetch_add((gu32*)p.ws + 32 * C_H, 1u, __ATOMIC_RELAXED, __HIP_MEMORY_SCOPE_AGENT);
    }
    poll_ge(p.ws, C_F0 + x8 * 16 + (id0.lane & 15), want0);
  }
#pragma unroll 1
  for (int st = 0; st < 3; ++st) {
    if (id0.w == 0) poll_ge(p.ws, st == 1 ? C_G2 + 4 * x8 * 8 + 7 : C_S3 + (st >> 1) * 7 * 32 + x8 * 4, st == 1 ? 7u : 1u);
    wgbar();
    if (st == 1) t1(48, 128); else t2(40, 112);
    vmwait<0>();
    if (st == 0 && b == 231 && id0.w == 0) {
      poll_ge(p.ws, C_H, 28u);
      if (id0.lane < 9) st_word(p.ws, id0.lane == 8 ? C_EP : C_FL + id0.lane, id0.lane == 8 ? tag : tag - 1u);
    }
  }
}

__global__ void __launch_bounds__(256, 1) k_all(Params p0) {
  extern __shared__ __attribute__((aligned(16))) unsigned char smem[];
  unsigned long long pc0;
  asm volatile("s_getpc_b64 %0" : "=s"(pc0));
  lds_t L = (lds_t)smem;
  const Ids id0 = get_ids();
  const auto nop = []() {};
  if ((blockIdx.x >> 3) == 29) touch(mkrs((const void*)(pc0 & ~127ull), CODEWARM), L + L_D, (int)threadIdx.x * 128, 0);
  if (blockIdx.x >= 224) { helper(p0, L, id0); return; }
#define ID fresh(id0)
#define W1S w_src(ID, p.w1, fresh_s(g1 ? S * F * 2 : 0), F * 2, 512, fresh_s(i) * 128, L)
#define W2S w_src(ID, p.w2, S * F * 2, S * 2, 448, fresh_s(i) * 128, L)
#define ASTAGE (L + L_A + fresh_s(id0.w) * 12288)
  v4u R1[NR1][4], R2[NR2][4];
#pragma unroll 1
  for (int layer = 0; layer < 2; ++layer) {
    const int b = fresh_s(blockIdx.x), q = b >> 3, x8 = b & 7, i = 4 * x8 + q / 7, j = q % 7, jj = x8 * 28 + q;
    const bool g1 = i < 28;
    PC k;
    const Params p = fresh_p(p0);
    const rsrc_t rw = mkrs(p.ws, WS_BYTES);
    if (layer == 0) {
      f0_issue(mkrs(p.x, XB), f0_tile(ID, 0, i, L));
      wt_issue(p, ID, x8, q / 7, j, L);
      if (threadIdx.x == 0) dma4_sc1(rw, L + L_D + 256, C_EP * 128);
    }
    gen_pc_a(ID, k);
    const F0A fa = f0_addr(ID, layer, i, j, L);
    const Tile ft = f0_tile(ID, X2_OFF, i, L);
    if (layer == 1) {
      seam_arrive<1, 8>(ID, p.ws, C_G2 + i * 8, 0, nop);
    }
    v4u T[14];
    v2u bq1[4], bq2[4];
    WSrc w1;
    {
      unsigned pv = 0;
      gen_tf(ID, j, T, [&]() { if (layer == 1) poll_early<1>(pv, ID, rw, C_G2 + i * 8, j); });
      if (layer == 1) {
        seam_wait<1, 0>(ID, p.ws, pv, C_G2 + i * 8, j, 7u);
        f0_issue(rw, ft);
      }
      f0_compute(ID, fa, rw, layer, i, j, L, T);
      if (layer == 0) {
        if (threadIdx.x == 0) dma4_sc1(rw, L + L_D + 260, (C_FL + x8) * 128);
        wt_store(rw, x8, q / 7, q % 7, L);
      }
      seam_arrive<16, 8>(ID, p.ws, C_F0 + layer * 128, b & 15, [&]() {
        gen_pc_b(ID, k);
        w1 = W1S;
        if (layer == 0) gen_pc_c(ID, k);
        if (layer == 0 && id0.w == 0) {
          vmwait<0>();
          const unsigned tag = ((const volatile LAS unsigned*)(L + L_D))[64] + 1u, fl = ((const volatile LAS unsigned*)(L + L_D))[65];
          if (__builtin_amdgcn_readfirstlane(fl != tag)) wait_init(id0, p.ws, x8, tag);
        } });
      if (layer == 0) w_issue<0, F0P>(w1, 0, w1.stage);
      if (layer == 1) gen_pc_c(ID, k);
      const PA pa = p_addr(ID, layer, jj, L);
      if (layer == 1) __builtin_amdgcn_s_sleep(F0SL);
      poll_early<16>(pv, ID, rw, C_F0 + layer * 128, x8);
      if (layer == 0) w_issue<F0P, 4 * NR1>(w1, 0, w1.stage); else vmwait<0>();
      seam_wait<16, 4 * NR1 - F0P>(ID, p.ws, pv, C_F0 + layer * 128, x8, layer == 0 && (ID.lane & 15) < 4 ? 15u : 14u);
      p_phase(pa, rw, jj, k, [&]() { load_bias(ID, p, g1 ? 64 * i : 0, 64 * i, bq1, bq2); gen_ti(ID, j, T); });
    }
    v4u W[10][4];
    f4 x1[4];
    Img im;
    {
      f4 mu[4];
      Tile t; S3A sa;
      seam_arrive<16, 8, PD_D + 4>(ID, p.ws, C_P + layer * 128, b & 15, [&]() {
        pin(bq1); pin(bq2);
        if (layer == 0) { w_read<NR1, 0>(img_set(ID), w1.stage, R1); lgwait<0>(); pin_acc<0, NR1>(R1); }
        w_issue<0, PD_D>(w1, NR1, w1.stage);
        const Ids id = ID;
#pragma unroll
        for (int n = 0; n < 4; ++n) mu[n] = asf4(ld128<SC1>(rw, 16 * id.g, MU_OFF + (layer * S + 64 * i + 16 * (n ^ id.w)) * 4));
        SB();
        t = s3_tile(ID, layer, i, L); im = img_set(ID); sa = s3_addr(ID, layer, i, j, L); });
      unsigned pv = 0;
      w_issue<PD_D, PP_D>(w1, NR1, w1.stage); poll_early<16>(pv, ID, rw, C_P + layer * 128, x8); w_issue<PP_D, 24>(w1, NR1, w1.stage);
      vmwait<24 - 4 * S3PRE>(); w_read<S3PRE, 0>(im, w1.stage, W);
      seam_wait<16, 24 - PP_D>(ID, p.ws, pv, C_P + layer * 128, x8, 14u);
      dma_tile<4096, 4096>(rw, t);
      vmwait<14>();
      w_read<6 - S3PRE, S3PRE>(im, w1.stage + S3PRE * 4096, W);
      s3_compute(ID, im, sa, rw, L, T, mu, x1);
    }
    f4 res[4], init[4];
    WSrc w2;
    int g1o; GeluK gk; GA ga, gb; G2A gc;
    seam_arrive<4, 8, 12>(ID, p.ws, C_S3 + (layer * 7 + j) * 32, i & 3, [&]() {
      pin_acc<0, 5>(W);
      w_issue<0, 4>(w1, NR1 + 6, w1.stage + 20480);
      w2 = W2S;
      if (layer == 0) w_issue<0, 8>(w2, 0, w2.stage);
#pragma unroll
      for (int f = 0; f < 4; ++f) { init[f] = bias4(bq1[f]); x1[f] += bias4(bq2[f]); }
      pin(init); pin(x1);
      g1o = g1_addr(ID, layer, j, i); gk = gelu_k(); ga = g_addr<16>(ID, im, HF_OFF + layer * XB + j * 64 * 4096);
      if (layer == 1) vmwait<4>(); });
    {
      const rsrc_t rg = mkrs(p.ws, fresh_s(g1 ? WS_BYTES : 0));
      if (layer == 0) w_issue<8, 4 * NR2>(w2, 0, w2.stage);
      unsigned pv = 0;
      { lds_t as = ASTAGE; w_issue<0, S3P>(w1, NR1 + 7, as); poll_early<4>(pv, ID, rw, C_S3 + (layer * 7 + j) * 32, x8); w_issue<S3P, 12>(w1, NR1 + 7, as); }
      gb = g_addr<14>(ID, im, GF_OFF + layer * GB + j * 56 * 4096); gc = g2_addr(ID, L);
      seam_wait<4, 12 - S3P>(ID, p.ws, pv, C_S3 + (layer * 7 + j) * 32, x8, 8u);
      g_main<16, 6, 20480, 1, 3>(ID, rg, ga, L, R1, W, res, init);
      g1_epi(fresh_s(id0.w), rg, g1o, gk, res);
      seam_arrive<4, 8, DRN_D>(ID, p.ws, C_G1 + (layer * 7 + j) * 32, i & 3, [&]() {
        if (layer == 0) { w_read<NR2, 0>(im, w2.stage, R2); lgwait<0>(); pin_acc<0, NR2>(R2); }
        w_issue<0, DRN_D>(w2, NR2, w2.stage); }, g1);
    }
    unsigned pv = 0;
    { lds_t as = ASTAGE; w_issue<DRN_D, 24>(w2, NR2, w2.stage); w_issue<0, G2P>(w2, NR2 + 6, as);
      poll_early<4>(pv, ID, rw, C_G1 + (layer * 7 + j) * 32, x8); w_issue<G2P, 12>(w2, NR2 + 6, as); }
    vmwait<36 - 16>(); w_read<4, 0>(im, w2.stage, W);
    seam_wait<4, 12 - G2P>(ID, p.ws, pv, C_G1 + (layer * 7 + j) * 32, x8, 7u);
    g_main<14, 4, 16384, 2, 3>(ID, rw, gb, L, R2, W, res, x1);
    g2_epi(gc, layer == 0 ? rw : mkrs(p.out, XB), (layer == 0 ? X2_OFF : 0) + 64 * i * 896 + j * 128, res, layer == 1);
  }
}

extern "C" void kernel_launch(void* const* d_in, const int* in_sizes, int n_in, void* d_out, int out_size, void* d_ws, size_t ws_size, hipStream_t stream) {
  Params p;
  p.x = (const unsigned short*)d_in[0]; p.wr = (const unsigned short*)d_in[1]; p.wi = (const unsigned short*)d_in[2];
  p.w1 = (const unsigned short*)d_in[4]; p.b1 = (const unsigned short*)d_in[5]; p.w2 = (const unsigned short*)d_in[6]; p.b2 = (const unsigned short*)d_in[7];
  p.out = (unsigned short*)d_out; p.ws = (unsigned char*)d_ws;
  (void)hipFuncSetAttribute((const void*)k_all, hipFuncAttributeMaxDynamicSharedMemorySize, LDS_BYTES);
  k_all<<<256, 256, LDS_BYTES, stream>>>(p);
}
```
